```python
import torch

def _softmax(x, axis=-1):
    return torch.softmax(x, dim=axis)
def _gelu(x):
    return torch.nn.functional.gelu(x, approximate="tanh")
def _silu(x):
    return torch.nn.functional.silu(x)
def _sigmoid(x):
    return torch.sigmoid(x)
def _layernorm(x, axis=-1, eps=1e-5):
    m = x.mean(dim=axis, keepdim=True)
    v = x.var(dim=axis, keepdim=True, unbiased=False)
    return (x - m) / torch.sqrt(v + eps)
def _scatter_add(tgt, idx, src, axis=0):
    return tgt.clone().index_add_(axis, idx, src)
def _segment_sum(src, seg, n):
    out = torch.zeros((n,) + src.shape[1:], dtype=src.dtype)
    return out.index_add_(0, seg, src)
def _pad(x, pw):
    flat = tuple(v for pair in reversed(pw) for v in pair)
    return torch.nn.functional.pad(x, flat)

kernel_name = 'parallel_sum__softmax_full_mha__dense_activation_gelu__prompt'

def setup_inputs():

    _w = (lambda a: (lambda t: t.to(torch.bfloat16) if t.is_floating_point() else t)(torch.as_tensor(a)))
    def _A_setup():
        B, S, D = 1, 2048, 512
        return {
            "x": _w((lambda *s: torch.randn(*s, dtype=torch.bfloat16) * 0.05)(B, S, D)),
            "wqkv": _w((lambda *s: torch.randn(*s, dtype=torch.bfloat16) * 0.05)(D, 3 * D)),
            "wo": _w((lambda *s: torch.randn(*s, dtype=torch.bfloat16) * 0.05)(D, D)),
        }
    _A_in = _A_setup()

    def _B_setup():
        B, S, D, F = 1, 2048, 512, 1792
        return {
            "x": _w((lambda *s: torch.randn(*s, dtype=torch.bfloat16) * 0.05)(B, S, D)),
            "w1": _w((lambda *s: torch.randn(*s, dtype=torch.bfloat16) * 0.8)(D, F) * (D ** -0.5)),
            "b1": _w((lambda *s: torch.randn(*s, dtype=torch.bfloat16) * 12.8)(F) * 0.01),
            "w2": _w((lambda *s: torch.randn(*s, dtype=torch.bfloat16) * 0.8)(F, D) * (F ** -0.5)),
            "b2": _w((lambda *s: torch.randn(*s, dtype=torch.bfloat16) * 12.8)(D) * 0.01),
        }
    _B_in = _B_setup()

    return {
        "A_x": _A_in["x"],
        "A_wqkv": _A_in["wqkv"],
        "A_wo": _A_in["wo"],
        "B_x": _B_in["x"],
        "B_w1": _B_in["w1"],
        "B_b1": _B_in["b1"],
        "B_w2": _B_in["w2"],
        "B_b2": _B_in["b2"],
    }

def reference(A_x, A_wqkv, A_wo, B_x, B_w1, B_b1, B_w2, B_b2):
    def _A_ref(x, wqkv, wo):
        B, S, D = x.shape
        H = 32; Dh = D // H
        qkv = (lambda x, *d: x.permute(*d))((x @ wqkv).reshape(B, S, 3, H, Dh), 2, 0, 3, 1, 4)
        q, k, v = qkv[0], qkv[1], qkv[2]
        scale = Dh ** -0.5
        scores = torch.einsum("bhqd,bhkd->bhqk", q, k) * scale
        p = _softmax(scores, axis=-1)
        o = torch.einsum("bhqk,bhkd->bhqd", p, v)
        return (lambda x, *d: x.permute(*d))(o, 0, 2, 1, 3).reshape(B, S, D) @ wo

    def _B_ref(x, w1, b1, w2, b2):
        h = torch.einsum("bsd,df->bsf", x, w1) + b1
        h = _gelu(h)
        return torch.einsum("bsf,fd->bsd", h, w2) + b2

    x = A_x
    xn = _layernorm(x, axis=-1)
    return x + _A_ref(xn, A_wqkv, A_wo) + _B_ref(xn, B_w1, B_b1, B_w2, B_b2)
```

```cpp
#include <hip/hip_runtime.h>
#include <hip/hip_bf16.h>
#include <stdint.h>

typedef __bf16 bf16x2 __attribute__((ext_vector_type(2)));
typedef __bf16 bf16x8 __attribute__((ext_vector_type(8)));
typedef __bf16 bf16x16 __attribute__((ext_vector_type(16)));
typedef float f32x2 __attribute__((ext_vector_type(2)));
typedef float f32x4 __attribute__((ext_vector_type(4)));
typedef float f32x8 __attribute__((ext_vector_type(8)));
typedef float f32x16 __attribute__((ext_vector_type(16)));
typedef unsigned u32x2 __attribute__((ext_vector_type(2)));
typedef unsigned u32x4 __attribute__((ext_vector_type(4)));
typedef short s16x4 __attribute__((ext_vector_type(4)));
typedef short s16x8 __attribute__((ext_vector_type(8)));
typedef unsigned long long u64;
#define LAS __attribute__((address_space(3)))
#define GAS __attribute__((address_space(1)))
typedef LAS char* ldsp;

constexpr int SEQ = 2048, DM = 512, NH = 32, FF = 1792;
constexpr unsigned WS_EPOCH = 0, WS_F1 = 4096, WS_F2 = WS_F1 + 32768, WS_F3 = WS_F2 + 32768, WS_KMAX = WS_F3 + 32768;
constexpr unsigned WS_Q = 1u << 20, WS_K = 3u << 20, WS_V = 5u << 20, WS_H = 8u << 20, WS_O = 16u << 20, WS_BYTES = 18u << 20;
constexpr unsigned VT = 544, VHEAD = 128 * VT;
constexpr int SLOT = 43008, OFF_B1 = 16384, OFF_B2 = 28672;
constexpr int L_K = 0, L_V = 65536, L_Q = 135168, L_KMAX = 143360, L_G2 = 143616;
constexpr int L_ROWST = 143616, L_COLS = L_ROWST + 2048;
constexpr int L_MISC = 163840 - 64, LDS_BYTES = 163840;
constexpr int RED_PITCH = 272;
constexpr float QSCALE = 0.25f * 1.4426950408889634f;
constexpr float SAFE_BOUND = 50.0f;
constexpr unsigned SPIN_CAP = 1u << 18;

#define MFMA16(a, b, c) __builtin_amdgcn_mfma_f32_16x16x32_bf16(a, b, c, 0, 0, 0)
#define MFMA32(a, b, c) __builtin_amdgcn_mfma_f32_32x32x16_bf16(a, b, c, 0, 0, 0)
#define SMF(a, b, c, i) __builtin_amdgcn_smfmac_f32_32x32x32_bf16(a, b, c, i, 0, 0)
#define BAR() asm volatile("s_waitcnt lgkmcnt(0)\n\ts_barrier" ::: "memory")
#define WAITV(n) asm volatile("s_waitcnt vmcnt(" #n ")" ::: "memory")
#define RLX_AGENT __ATOMIC_RELAXED, __HIP_MEMORY_SCOPE_AGENT

#define STAMP(i) do {} while (0)

__device__ __forceinline__ bf16x8 ld_b128(ldsp p) { return *(const LAS bf16x8*)p; }
__device__ __forceinline__ bf16x8 ld_tr2(ldsp p0, ldsp p1) {
    s16x4 lo = __builtin_amdgcn_ds_read_tr16_b64_v4i16((LAS s16x4*)p0);
    s16x4 hi = __builtin_amdgcn_ds_read_tr16_b64_v4i16((LAS s16x4*)p1);
    s16x8 t = __builtin_shufflevector(lo, hi, 0, 1, 2, 3, 4, 5, 6, 7);
    return __builtin_bit_cast(bf16x8, t);
}
__device__ __forceinline__ unsigned pk2(float a, float b) { f32x2 v = {a, b}; return __builtin_bit_cast(unsigned, __builtin_convertvector(v, bf16x2)); }
__device__ __forceinline__ float bf_lo(unsigned u) { return __uint_as_float(u << 16); }
__device__ __forceinline__ float bf_hi(unsigned u) { return __uint_as_float(u & 0xffff0000u); }
__device__ __forceinline__ float rbf(float a) { return (float)(__bf16)a; }
__device__ __forceinline__ float dot2(unsigned a, unsigned b, float c) { return __builtin_amdgcn_fdot2_f32_bf16(__builtin_bit_cast(bf16x2, a), __builtin_bit_cast(bf16x2, b), c, false); }
__device__ __forceinline__ float sum_x16(float v) { auto r = __builtin_amdgcn_permlane16_swap(__float_as_uint(v), __float_as_uint(v), false, false); return __uint_as_float(r[0]) + __uint_as_float(r[1]); }
__device__ __forceinline__ float sum_x32(float v) { auto r = __builtin_amdgcn_permlane32_swap(__float_as_uint(v), __float_as_uint(v), false, false); return __uint_as_float(r[0]) + __uint_as_float(r[1]); }
__device__ __forceinline__ float max_x16(float v) { auto r = __builtin_amdgcn_permlane16_swap(__float_as_uint(v), __float_as_uint(v), false, false); return fmaxf(__uint_as_float(r[0]), __uint_as_float(r[1])); }
__device__ __forceinline__ float max_x32(float v) { auto r = __builtin_amdgcn_permlane32_swap(__float_as_uint(v), __float_as_uint(v), false, false); return fmaxf(__uint_as_float(r[0]), __uint_as_float(r[1])); }
template <int N> __device__ __forceinline__ float ror16(float v) { return __uint_as_float(__builtin_amdgcn_update_dpp(0u, __float_as_uint(v), 0x120 + N, 0xf, 0xf, false)); }
__device__ __forceinline__ float nmax(float a, float b) { return (a > b || a != a) ? a : b; }
__device__ __forceinline__ float nmax_row16(float v) { v = nmax(v, ror16<8>(v)); v = nmax(v, ror16<4>(v)); v = nmax(v, ror16<2>(v)); return nmax(v, ror16<1>(v)); }

__device__ __forceinline__ void glds16(const char* g, ldsp l) { __builtin_amdgcn_global_load_lds((const GAS unsigned*)g, (LAS unsigned*)l, 16, 0, 0); }

__device__ __forceinline__ void wait_flags(const char* ws, unsigned off, u64 tag) {
    const GAS u64* p = (const GAS u64*)(ws + off);
    for (unsigned spins = 0; spins < SPIN_CAP; ++spins) {
        const u64 v = __hip_atomic_load(p, RLX_AGENT);
        if (__all(v == tag)) break;
        __builtin_amdgcn_s_sleep(1);
    }
    asm volatile("" ::: "memory");
}
__device__ __forceinline__ void arrive_publish(ldsp lds, int ctr, int n, char* ws, unsigned flag_off, int lane) {
    WAITV(0);
    if (lane == 0) {
        const unsigned old = __hip_atomic_fetch_add((LAS unsigned*)(lds + L_MISC + 16 + 4 * ctr), 1u, __ATOMIC_RELAXED, __HIP_MEMORY_SCOPE_WORKGROUP);
        if (old == (unsigned)(n - 1)) {
            const u64 tag = *(const volatile LAS u64*)(lds + L_MISC);
            __hip_atomic_store((GAS u64*)(ws + flag_off), tag, RLX_AGENT);
        }
    }
}

__device__ __forceinline__ float gelu_tanh(float x) {
    constexpr float C1 = 2.0f * 0.7978845608028654f * 1.4426950408889634f, C2 = C1 * 0.044715f;
    const float t = __builtin_fmaf(x * x, -C2, -C1);
    const float e = __builtin_amdgcn_exp2f(x * t);
    return x * __builtin_amdgcn_rcpf(1.0f + e);
}

template <int NF>
__device__ __forceinline__ void g1_compute(ldsp lds, char* ws, const __amdgpu_buffer_rsrc_t wsr, const __hip_bfloat16* b1, int wm, int lane, int mt, int nt) {
    constexpr int WN = (NF == 4) ? 0 : 1, NT = 3 + NF;
    const int m = lane & 15, g = lane >> 4, q = (lane & 15) >> 2, p = lane & 3;
    f32x4 acc[NT][2];
#pragma unroll
    for (int t = 0; t < NT; ++t) { acc[t][0] = f32x4{}; acc[t][1] = f32x4{}; }
    float rsum[2] = {0.f, 0.f}, rsq[2] = {0.f, 0.f}, cs[NT];
#pragma unroll
    for (int t = 0; t < NT; ++t) cs[t] = 0.f;
    const int arow = 32 * wm + m, swz = (arow >> 1) & 7;
    const int aoff0 = arow * 128 + ((g ^ swz) << 4), aoff1 = arow * 128 + (((4 + g) ^ swz) << 4);
    const int b1e = OFF_B1 + (8 * g + q) * 192 + 32 * (g & 1) + 8 * p, b1o = OFF_B1 + (8 * g + q) * 192 + 32 * ((g & 1) ^ 1) + 8 * p;
    const int b2b = OFF_B2 + (16 * (g >> 1) + 4 * (g & 1) + q) * 224 + 8 * p;
    u32x2 bias[NF];
#pragma unroll
    for (int t = 0; t < NF; ++t) bias[t] = *(const GAS u32x2*)((const char*)b1 + (112 * nt + 64 * WN + 16 * t + 4 * g) * 2);

    u64 epoch = 0;
    if (WN == 0 && wm == 0) epoch = __hip_atomic_load((const GAS u64*)(ws + WS_EPOCH), RLX_AGENT);

    int slot = 0;
    for (int s = 0; s < 8; ++s) {
        BAR();
        ldsp base = lds + slot;
        slot = (slot == 2 * SLOT) ? 0 : slot + SLOT;
        const bool docs = ((s & 3) == wm);
#pragma unroll
        for (int ks = 0; ks < 2; ++ks) {
            const bf16x8 x0 = ld_b128(base + (ks ? aoff1 : aoff0)), x1 = ld_b128(base + (ks ? aoff1 : aoff0) + 2048);
            bf16x8 wf[NT];
#pragma unroll
            for (int t = 0; t < 3; ++t) { const int tt = 3 * WN + t; ldsp pp = base + ((tt & 1) ? b1o : b1e) + (tt & ~1) * 32 + ks * 32 * 192; wf[t] = ld_tr2(pp, pp + 4 * 192); }
#pragma unroll
            for (int t = 0; t < NF; ++t) { ldsp pp = base + b2b + (4 * WN + t) * 32 + ks * 32 * 224; wf[3 + t] = ld_tr2(pp, pp + 8 * 224); }
#pragma unroll
            for (int t = 0; t < NT; ++t) { acc[t][0] = MFMA16(wf[t], x0, acc[t][0]); acc[t][1] = MFMA16(wf[t], x1, acc[t][1]); }
            if (ks == WN) {
                const u32x4 u0 = __builtin_bit_cast(u32x4, x0), u1 = __builtin_bit_cast(u32x4, x1);
#pragma unroll
                for (int i = 0; i < 4; ++i) { rsum[0] = dot2(u0[i], 0x3f803f80u, rsum[0]); rsq[0] = dot2(u0[i], u0[i], rsq[0]); rsum[1] = dot2(u1[i], 0x3f803f80u, rsum[1]); rsq[1] = dot2(u1[i], u1[i], rsq[1]); }
            }
            if (docs) {
#pragma unroll
                for (int t = 0; t < NT; ++t) { const u32x4 u = __builtin_bit_cast(u32x4, wf[t]);
#pragma unroll
                    for (int i = 0; i < 4; ++i) cs[t] = dot2(u[i], 0x3f803f80u, cs[t]); }
            }
        }
    }
#pragma unroll
    for (int i = 0; i < 2; ++i) { const float a = sum_x32(sum_x16(rsum[i])), b = sum_x32(sum_x16(rsq[i]));
        if (g == 0) *(LAS f32x2*)(lds + L_ROWST + (WN * 128 + 32 * wm + 16 * i + m) * 8) = f32x2{a, b}; }
#pragma unroll
    for (int t = 0; t < NT; ++t) { const float a = sum_x32(sum_x16(cs[t])); const int col = (t < 3) ? (3 * WN + t) * 16 : 96 + (4 * WN + t - 3) * 16;
        if (g == 0) *(LAS float*)(lds + L_COLS + (wm * 208 + col + m) * 4) = a; }
    if (WN == 0 && wm == 0 && lane == 0) {
        *(LAS u64*)(lds + L_MISC) = epoch * 6364136223846793005ull + 1442695040888963407ull;
        *(LAS u32x4*)(lds + L_MISC + 16) = u32x4{0u, 0u, 0u, 0u};
    }
    BAR();
    STAMP(2);
    float mean[2], rstd[2];
#pragma unroll
    for (int i = 0; i < 2; ++i) {
        const f32x2 a = *(const LAS f32x2*)(lds + L_ROWST + (32 * wm + 16 * i + m) * 8), b = *(const LAS f32x2*)(lds + L_ROWST + (128 + 32 * wm + 16 * i + m) * 8);
        const float mu = (a[0] + b[0]) * (1.0f / DM), var = fmaxf((a[1] + b[1]) * (1.0f / DM) - mu * mu, 0.f);
        mean[i] = rbf(mu);
        rstd[i] = 1.0f / rbf(sqrtf(rbf(rbf(var) + 1e-5f)));
    }
    auto colsum = [&](int col) { f32x4 c = *(const LAS f32x4*)(lds + L_COLS + (col + 4 * g) * 4);
#pragma unroll
        for (int w2 = 1; w2 < 4; ++w2) c += *(const LAS f32x4*)(lds + L_COLS + (w2 * 208 + col + 4 * g) * 4);
        return c; };
    const int blk32 = 4 * mt + wm;
#pragma unroll
    for (int t = 0; t < 3; ++t) {
        const int tt = 3 * WN + t, c0 = 96 * nt + 16 * tt, kind = c0 >> 9, head = (c0 & 511) >> 4;
        const f32x4 c = colsum(16 * tt);
        const float sc = (kind == 0) ? QSCALE : 1.0f;
        float kn = 0.f;
#pragma unroll
        for (int i = 0; i < 2; ++i) {
            const float rs = rstd[i] * sc; float v[4];
#pragma unroll
            for (int r = 0; r < 4; ++r) v[r] = __builtin_fmaf(-mean[i], c[r], acc[t][i][r]) * rs;
            const u32x2 d = {pk2(v[0], v[1]), pk2(v[2], v[3])};
            unsigned off;
            if (kind < 2) off = WS_Q + kind * (2u << 20) + head * 65536 + blk32 * 1024 + (g >> 1) * 512 + (16 * i + m) * 16 + (g & 1) * 8;
            else off = WS_V + head * VHEAD + (2 * blk32 + i) * VT + m * 32 + g * 8;
            __builtin_amdgcn_raw_buffer_store_b64(d, wsr, off, 0, 16);
            if (kind == 2 && m == 0) __builtin_amdgcn_raw_buffer_store_b64(u32x2{0x3f803f80u, 0x3f803f80u}, wsr, off + 512, 0, 16);
            if (kind == 1) { const float ss = sum_x32(sum_x16(dot2(d[1], d[1], dot2(d[0], d[0], 0.f)))); kn = (i == 0) ? ss : nmax(kn, ss); }
        }
        if (kind == 1) { kn = nmax_row16(kn); if (lane == 0) __builtin_amdgcn_raw_buffer_store_b32(__float_as_uint(kn), wsr, WS_KMAX + (head * 64 + blk32) * 4, 0, 16); }
    }
    u32x2 hv[NF][2];
#pragma unroll
    for (int t = 0; t < NF; ++t) {
        const f32x4 c = colsum(96 + (4 * WN + t) * 16);
        const float bb[4] = {bf_lo(bias[t][0]), bf_hi(bias[t][0]), bf_lo(bias[t][1]), bf_hi(bias[t][1])};
#pragma unroll
        for (int i = 0; i < 2; ++i) { float v[4];
#pragma unroll
            for (int r = 0; r < 4; ++r) v[r] = gelu_tanh(__builtin_fmaf(__builtin_fmaf(-mean[i], c[r], acc[3 + t][i][r]), rstd[i], bb[r]));
            hv[t][i] = u32x2{pk2(v[0], v[1]), pk2(v[2], v[3])}; }
        if (t == 1) { asm volatile("" : "+v"(hv[0][0]), "+v"(hv[0][1]), "+v"(hv[1][0]), "+v"(hv[1][1])); arrive_publish(lds, 0, 8, ws, WS_F1 + (mt * 16 + nt) * 128, lane); STAMP(3); }
    }
#pragma unroll
    for (int t = 0; t < NF; ++t)
#pragma unroll
        for (int i = 0; i < 2; ++i)
            __builtin_amdgcn_raw_buffer_store_b64(hv[t][i], wsr, WS_H + (unsigned)(blk32 * 112 + 7 * nt + 4 * WN + t) * 1024 + (g >> 1) * 512 + (16 * i + m) * 16 + (g & 1) * 8, 0, 16);
    arrive_publish(lds, 1, 8, ws, WS_F2 + (mt * 16 + nt) * 128, lane);
}

template <int N>
__device__ __forceinline__ void g1_load(ldsp dst0, const char* src, const unsigned (&off)[N], unsigned step) {
    auto issue = [&](int s, int slot) {
        const char* sp = src + (size_t)s * step;
#pragma unroll
        for (int i = 0; i < N; ++i) glds16(sp + off[i], dst0 + slot + i * 1024);
    };
    issue(0, 0); issue(1, SLOT); issue(2, 2 * SLOT);
    asm volatile("s_waitcnt vmcnt(%0)\n\ts_barrier" ::"n"(2 * N) : "memory");
    asm volatile("s_waitcnt vmcnt(%0)\n\ts_barrier" ::"n"(N) : "memory");
    int slot = 0;
    for (int s = 3; s < 8; ++s) {
        issue(s, slot); slot = (slot == 2 * SLOT) ? 0 : slot + SLOT;
        asm volatile("s_waitcnt vmcnt(%0)\n\ts_barrier" ::"n"(N) : "memory");
    }
    asm volatile("s_waitcnt vmcnt(0)\n\ts_barrier" ::: "memory");
    asm volatile("s_barrier" ::: "memory");
}

__device__ __forceinline__ bf16x16 lo8(const f32x16& s) { return __builtin_bit_cast(bf16x16, __builtin_shufflevector(s, s, 0, 1, 2, 3, 4, 5, 6, 7)); }
__device__ __forceinline__ bf16x16 hi8(const f32x16& s) { return __builtin_bit_cast(bf16x16, __builtin_shufflevector(s, s, 8, 9, 10, 11, 12, 13, 14, 15)); }

__device__ __forceinline__ void attention(ldsp lds, char* ws, const __amdgpu_buffer_rsrc_t wsr, int w, int lane, int head, int qt) {
    const bf16x8 qf = ld_b128(lds + L_Q + w * 1024 + lane * 16);
    float km = *(const LAS float*)(lds + L_KMAX + lane * 4);
    km = nmax_row16(km);
    { auto r = __builtin_amdgcn_permlane16_swap(__float_as_uint(km), __float_as_uint(km), false, false); km = nmax(__uint_as_float(r[0]), __uint_as_float(r[1])); }
    { auto r = __builtin_amdgcn_permlane32_swap(__float_as_uint(km), __float_as_uint(km), false, false); km = nmax(__uint_as_float(r[0]), __uint_as_float(r[1])); }
    const u32x4 qu = __builtin_bit_cast(u32x4, qf);
    const float qn = sum_x32(dot2(qu[3], qu[3], dot2(qu[2], qu[2], dot2(qu[1], qu[1], dot2(qu[0], qu[0], 0.f)))));
    const float bound = sqrtf(qn * km) * 1.001f;
    const int G = lane >> 4, q = (lane & 15) >> 2, p = lane & 3;
    ldsp kp = lds + L_K + lane * 16;
    ldsp v0 = lds + L_V + ((G & 1) ? 512 + 8 * p : (8 * (G >> 1) + q) * 32 + 8 * p);
    ldsp v1 = lds + L_V + ((G & 1) ? 512 + 8 * p : (8 * (G >> 1) + 4 + q) * 32 + 8 * p);
    int idx = (int)0xDDDDDDDDu;
    asm volatile("" : "+v"(idx));
    f32x16 o = {};
    if (__all(bound <= SAFE_BOUND)) {
        f32x16 cm;
#pragma unroll
        for (int r = 0; r < 16; ++r) cm[r] = -bound;
        asm volatile("" : "+v"(cm));
        f32x16 sa = MFMA32(ld_b128(kp), qf, cm), sb;
        for (int it = 0; it < 16; ++it) {
#pragma unroll
            for (int u = 0; u < 2; ++u) {
                const bf16x8 k1 = ld_b128(kp + (2 * u + 1) * 1024);
                const bf16x8 va = ld_tr2(v0 + (4 * u) * VT, v1 + (4 * u) * VT), vb = ld_tr2(v0 + (4 * u + 1) * VT, v1 + (4 * u + 1) * VT);
                sb = MFMA32(k1, qf, cm);
#pragma unroll
                for (int r = 0; r < 16; ++r) sa[r] = __builtin_amdgcn_exp2f(sa[r]);
                o = SMF(va, lo8(sa), o, idx); o = SMF(vb, hi8(sa), o, idx);
                const bf16x8 k2 = ld_b128(kp + (2 * u + 2) * 1024);
                const bf16x8 vc = ld_tr2(v0 + (4 * u + 2) * VT, v1 + (4 * u + 2) * VT), vd = ld_tr2(v0 + (4 * u + 3) * VT, v1 + (4 * u + 3) * VT);
                sa = MFMA32(k2, qf, cm);
#pragma unroll
                for (int r = 0; r < 16; ++r) sb[r] = __builtin_amdgcn_exp2f(sb[r]);
                o = SMF(vc, lo8(sb), o, idx); o = SMF(vd, hi8(sb), o, idx);
            }
            kp += 4096; v0 += 8 * VT; v1 += 8 * VT;
        }
    } else {
        float mx = -INFINITY;
        for (int it = 0; it < 64; ++it) {
            f32x16 s = MFMA32(ld_b128(kp), qf, f32x16{});
            const bf16x8 va = ld_tr2(v0, v1), vb = ld_tr2(v0 + VT, v1 + VT);
            float bm = s[0];
#pragma unroll
            for (int r = 1; r < 16; ++r) bm = fmaxf(bm, s[r]);
            const float mn = fmaxf(mx, max_x32(bm));
            const float al = __builtin_amdgcn_exp2f(mx - mn);
            mx = mn;
#pragma unroll
            for (int r = 0; r < 16; ++r) { o[r] *= al; s[r] = __builtin_amdgcn_exp2f(s[r] - mn); }
            o = SMF(va, lo8(s), o, idx); o = SMF(vb, hi8(s), o, idx);
            kp += 1024; v0 += 2 * VT; v1 += 2 * VT;
        }
    }
    const float inv = 1.0f / o[8];
    const u32x4 d = {pk2(o[0] * inv, o[1] * inv), pk2(o[2] * inv, o[3] * inv), pk2(o[4] * inv, o[5] * inv), pk2(o[6] * inv, o[7] * inv)};
    __builtin_amdgcn_raw_buffer_store_b128(d, wsr, WS_O + (unsigned)((8 * qt + w) * 32 + head) * 1024 + lane * 16, 0, 16);
    arrive_publish(lds, 2, 8, ws, WS_F3 + (head * 8 + qt) * 128, lane);
}

template <int NS, int DEPTH>
__device__ __forceinline__ void g2_run(f32x16 (&acc)[2][2], ldsp stage, const char* wsrc, const __amdgpu_buffer_rsrc_t wsr, unsigned a0, unsigned a1, int lane,
                                       u32x4 (&wr)[DEPTH][2], bool w_preloaded) {
    const int G = lane >> 4, q = (lane & 15) >> 2, p = lane & 3;
    ldsp rd = stage + (8 * (G >> 1) + q) * 128 + 32 * (G & 1) + 8 * p;
    const int sw = ((q >> 1) & 1) * 64;
    u32x4 ar[DEPTH][2];
#pragma unroll
    for (int d = 0; d < DEPTH; ++d) {
        if (!w_preloaded) { wr[d][0] = *(const GAS u32x4*)(wsrc + (size_t)d * 16384); wr[d][1] = *(const GAS u32x4*)(wsrc + (size_t)d * 16384 + 8192); }
        ar[d][0] = __builtin_amdgcn_raw_buffer_load_b128(wsr, a0 + d * 1024, 0, 16); ar[d][1] = __builtin_amdgcn_raw_buffer_load_b128(wsr, a1 + d * 1024, 0, 16);
    }
    for (int s0 = 0; s0 < NS; s0 += DEPTH) {
#pragma unroll
        for (int d = 0; d < DEPTH; ++d) {
            ldsp st = stage + (d & 1) * 2048;
            *(LAS u32x4*)(st + lane * 16) = wr[d][0]; *(LAS u32x4*)(st + 1024 + lane * 16) = wr[d][1];
            const bf16x8 h0 = __builtin_bit_cast(bf16x8, ar[d][0]), h1 = __builtin_bit_cast(bf16x8, ar[d][1]);
            ldsp r0 = rd + (d & 1) * 2048;
            const bf16x8 w0 = ld_tr2(r0 + sw, r0 + sw + 512), w1 = ld_tr2(r0 + (sw ^ 64), r0 + (sw ^ 64) + 512);
            const int sn = s0 + DEPTH + d;
            if (sn < NS) {
                wr[d][0] = *(const GAS u32x4*)(wsrc + (size_t)sn * 16384); wr[d][1] = *(const GAS u32x4*)(wsrc + (size_t)sn * 16384 + 8192);
                ar[d][0] = __builtin_amdgcn_raw_buffer_load_b128(wsr, a0 + sn * 1024, 0, 16); ar[d][1] = __builtin_amdgcn_raw_buffer_load_b128(wsr, a1 + sn * 1024, 0, 16);
            }
            acc[0][0] = MFMA32(w0, h0, acc[0][0]); acc[0][1] = MFMA32(w0, h1, acc[0][1]);
            acc[1][0] = MFMA32(w1, h0, acc[1][0]); acc[1][1] = MFMA32(w1, h1, acc[1][1]);
        }
    }
}

__global__ __launch_bounds__(768, 3) void fused_block_kernel(const __hip_bfloat16* __restrict__ x, const __hip_bfloat16* __restrict__ wqkv, const __hip_bfloat16* __restrict__ wo,
                                                             const __hip_bfloat16* __restrict__ w1, const __hip_bfloat16* __restrict__ b1, const __hip_bfloat16* __restrict__ w2,
                                                             const __hip_bfloat16* __restrict__ b2, __hip_bfloat16* __restrict__ out, char* __restrict__ ws) {
    extern __shared__ __attribute__((aligned(16))) char smem[];
    ldsp lds = (ldsp)smem;
    const int lane = threadIdx.x & 63, w = __builtin_amdgcn_readfirstlane(threadIdx.x >> 6);
    const int bg = blockIdx.x & 7, br = blockIdx.x >> 3;
    const int mt = (bg & 1) * 8 + (br & 7), nt = (bg >> 1) * 4 + (br >> 3);
    const int head = bg * 4 + (br >> 3), qt = br & 7;
    const int rt = bg * 4 + (br >> 3), ct = br & 7;
    const __amdgpu_buffer_rsrc_t wsr = __builtin_amdgcn_make_buffer_rsrc(ws, 0, WS_BYTES, 0x00020000);
    STAMP(0);
    u32x4 xres; u32x4 bres;
    const int orow = 64 * rt + (threadIdx.x >> 3), ocol = 64 * ct + (threadIdx.x & 7) * 8;
    if (w < 8) {
        if (w < 4) g1_compute<4>(lds, ws, wsr, b1, w, lane, mt, nt); else g1_compute<3>(lds, ws, wsr, b1, w - 4, lane, mt, nt);
        STAMP(4);
        xres = *(const GAS u32x4*)((const char*)x + (orow * DM + ocol) * 2);
        bres = *(const GAS u32x4*)((const char*)b2 + ocol * 2);
        BAR();
        STAMP(5);
        attention(lds, ws, wsr, w, lane, head, qt);
        STAMP(6);
        BAR();
    } else {
        const int j = w - 8;
        if (j < 2) {
            unsigned off[8];
#pragma unroll
            for (int i = 0; i < 8; ++i) { const int row = 64 * j + 8 * i + (lane >> 3); off[i] = row * 1024 + (((lane & 7) ^ ((row >> 1) & 7)) << 4); }
            g1_load<8>(lds + j * 8192, (const char*)x + (size_t)mt * 128 * 1024, off, 128);
        } else if (j == 2) {
            unsigned off[12];
#pragma unroll
            for (int i = 0; i < 12; ++i) { const int e = 64 * i + lane, k = e / 12, pos = e - 12 * k; off[i] = k * 3072 + ((pos ^ (((k >> 3) & 1) << 1)) << 4); }
            g1_load<12>(lds + OFF_B1, (const char*)wqkv + nt * 192, off, 64 * 3072);
        } else {
            unsigned off[14];
#pragma unroll
            for (int i = 0; i < 14; ++i) { const int e = 64 * i + lane, rho = e / 14, pos = e - 14 * rho; const int k = (rho & ~12) | ((rho & 4) << 1) | ((rho & 8) >> 1); off[i] = k * 3584 + (pos << 4); }
            g1_load<14>(lds + OFF_B2, (const char*)w1 + nt * 224, off, 64 * 3584);
        }
        STAMP(2);
        const u64 tag = *(const volatile LAS u64*)(lds + L_MISC);
        {
            const int ntk = (512 + 16 * head) / 96, ntv = (1024 + 16 * head) / 96, ntq = (16 * head) / 96;
            const int l16 = lane & 15;
            const int fm = (l16 < 8) ? 4 * j + (l16 & 3) : 2 * qt + (j >> 1), fn = (l16 < 4) ? ntk : (l16 < 8) ? ntv : ntq;
            wait_flags(ws, WS_F1 + (fm * 16 + fn) * 128, tag);
            STAMP(3);
            u32x4 t[17];
#pragma unroll
            for (int i = 0; i < 16; ++i) t[i] = __builtin_amdgcn_raw_buffer_load_b128(wsr, WS_K + head * 65536 + j * 16384 + i * 1024 + lane * 16, 0, 16);
            const u32x4 tq0 = __builtin_amdgcn_raw_buffer_load_b128(wsr, WS_Q + head * 65536 + qt * 8192 + j * 2048 + lane * 16, 0, 16);
            const u32x4 tq1 = __builtin_amdgcn_raw_buffer_load_b128(wsr, WS_Q + head * 65536 + qt * 8192 + j * 2048 + 1024 + lane * 16, 0, 16);
            const unsigned tk = __builtin_amdgcn_raw_buffer_load_b32(wsr, WS_KMAX + (head * 64 + 16 * j + l16) * 4, 0, 16);
#pragma unroll
            for (int i = 0; i < 16; ++i) *(LAS u32x4*)(lds + L_K + j * 16384 + i * 1024 + lane * 16) = t[i];
#pragma unroll
            for (int i = 0; i < 17; ++i) t[i] = __builtin_amdgcn_raw_buffer_load_b128(wsr, WS_V + head * VHEAD + j * 17408 + i * 1024 + lane * 16, 0, 16);
            *(LAS u32x4*)(lds + L_Q + j * 2048 + lane * 16) = tq0; *(LAS u32x4*)(lds + L_Q + j * 2048 + 1024 + lane * 16) = tq1;
            if (lane < 16) *(LAS unsigned*)(lds + L_KMAX + (16 * j + lane) * 4) = tk;
#pragma unroll
            for (int i = 0; i < 17; ++i) *(LAS u32x4*)(lds + L_V + j * 17408 + i * 1024 + lane * 16) = t[i];
        }
        STAMP(4);
        BAR();
        f32x16 acc[2][2] = {};
        ldsp stage = lds + L_G2 + j * 4096;
        const int wrow = lane >> 3, wch = (lane & 7) ^ (((wrow >> 1) & 1) << 2);
        u32x4 wr[4][2];
        wait_flags(ws, WS_F2 + ((rt >> 1) * 16 + 4 * j + (lane & 3)) * 128, tag);
        STAMP(5);
        g2_run<28, 4>(acc, stage, (const char*)w2 + (size_t)(448 * j + wrow) * 1024 + ct * 128 + wch * 16, wsr,
                      WS_H + (unsigned)(2 * rt * 112 + 28 * j) * 1024 + lane * 16, WS_H + (unsigned)((2 * rt + 1) * 112 + 28 * j) * 1024 + lane * 16, lane, wr, false);
        STAMP(6);
        if (blockIdx.x == 0 && j == 3) {
#pragma unroll
            for (int i = 0; i < 4; ++i) wait_flags(ws, WS_F1 + (i * 64 + lane) * 128, tag);
            if (lane == 0) __hip_atomic_store((GAS u64*)(ws + WS_EPOCH), tag, RLX_AGENT);
        }
        u32x4 wq[8][2];
        {
            const int d0 = (wrow & 3) | ((wrow & 4) << 1);
            const char* wsrc = (const char*)wo + (size_t)(128 * j + d0) * 1024 + ct * 128 + wch * 16;
#pragma unroll
            for (int d = 0; d < 8; ++d) { wq[d][0] = *(const GAS u32x4*)(wsrc + (size_t)d * 16384); wq[d][1] = *(const GAS u32x4*)(wsrc + (size_t)d * 16384 + 4096); }
        }
        wait_flags(ws, WS_F3 + ((8 * j + (lane & 7)) * 8 + (rt >> 2)) * 128, tag);
        STAMP(7);
        g2_run<8, 8>(acc, stage, nullptr, wsr, WS_O + (unsigned)(2 * rt * 32 + 8 * j) * 1024 + lane * 16, WS_O + (unsigned)((2 * rt + 1) * 32 + 8 * j) * 1024 + lane * 16, lane, wq, true);
        STAMP(8);
        BAR();
#pragma unroll
        for (int a = 0; a < 2; ++a)
#pragma unroll
            for (int b = 0; b < 2; ++b)
#pragma unroll
                for (int r4 = 0; r4 < 4; ++r4)
                    *(LAS f32x4*)(lds + j * (64 * RED_PITCH) + (32 * b + (lane & 31)) * RED_PITCH + (32 * a + 8 * r4 + 4 * (lane >> 5)) * 4) =
                        f32x4{acc[a][b][4 * r4], acc[a][b][4 * r4 + 1], acc[a][b][4 * r4 + 2], acc[a][b][4 * r4 + 3]};
    }
    BAR();
    if (w < 8) {
        ldsp rp = lds + (threadIdx.x >> 3) * RED_PITCH + (threadIdx.x & 7) * 32;
        f32x4 s0 = *(const LAS f32x4*)rp, s1 = *(const LAS f32x4*)(rp + 16);
#pragma unroll
        for (int k = 1; k < 4; ++k) { s0 += *(const LAS f32x4*)(rp + k * 64 * RED_PITCH); s1 += *(const LAS f32x4*)(rp + k * 64 * RED_PITCH + 16); }
        u32x4 o;
        o[0] = pk2(s0[0] + bf_lo(xres[0]) + bf_lo(bres[0]), s0[1] + bf_hi(xres[0]) + bf_hi(bres[0]));
        o[1] = pk2(s0[2] + bf_lo(xres[1]) + bf_lo(bres[1]), s0[3] + bf_hi(xres[1]) + bf_hi(bres[1]));
        o[2] = pk2(s1[0] + bf_lo(xres[2]) + bf_lo(bres[2]), s1[1] + bf_hi(xres[2]) + bf_hi(bres[2]));
        o[3] = pk2(s1[2] + bf_lo(xres[3]) + bf_lo(bres[3]), s1[3] + bf_hi(xres[3]) + bf_hi(bres[3]));
        *(GAS u32x4*)((char*)out + (orow * DM + ocol) * 2) = o;
    }
    STAMP(9);
}

extern "C" void kernel_launch(void* const* d_in, const int* in_sizes, int n_in, void* d_out, int out_size, void* d_ws, size_t ws_size, hipStream_t stream) {
    (void)in_sizes; (void)n_in; (void)out_size; (void)ws_size;
    (void)hipFuncSetAttribute((const void*)fused_block_kernel, hipFuncAttributeMaxDynamicSharedMemorySize, LDS_BYTES);
    fused_block_kernel<<<256, 768, LDS_BYTES, stream>>>((const __hip_bfloat16*)d_in[0], (const __hip_bfloat16*)d_in[1], (const __hip_bfloat16*)d_in[2],
                                                       (const __hip_bfloat16*)d_in[4], (const __hip_bfloat16*)d_in[5], (const __hip_bfloat16*)d_in[6],
                                                       (const __hip_bfloat16*)d_in[7], (__hip_bfloat16*)d_out, (char*)d_ws);
}
```

```cpp
#include <hip/hip_runtime.h>
#include <hip/hip_bf16.h>
#include <stdint.h>

typedef __bf16 bf16x2 __attribute__((ext_vector_type(2)));
typedef __bf16 bf16x8 __attribute__((ext_vector_type(8)));
typedef __bf16 bf16x16 __attribute__((ext_vector_type(16)));
typedef float f32x2 __attribute__((ext_vector_type(2)));
typedef float f32x4 __attribute__((ext_vector_type(4)));
typedef float f32x8 __attribute__((ext_vector_type(8)));
typedef float f32x16 __attribute__((ext_vector_type(16)));
typedef unsigned u32x2 __attribute__((ext_vector_type(2)));
typedef unsigned u32x4 __attribute__((ext_vector_type(4)));
typedef short s16x4 __attribute__((ext_vector_type(4)));
typedef short s16x8 __attribute__((ext_vector_type(8)));
typedef unsigned long long u64;
#define LAS __attribute__((address_space(3)))
#define GAS __attribute__((address_space(1)))
typedef LAS char* ldsp;

constexpr int SEQ = 2048, DM = 512, NH = 32, FF = 1792;
constexpr unsigned WS_EPOCH = 0, WS_F1 = 4096, WS_F2 = WS_F1 + 32768, WS_F3 = WS_F2 + 32768, WS_KMAX = WS_F3 + 32768;
constexpr unsigned WS_F0 = WS_KMAX + 8192, WS_ST = WS_F0 + 32768;
constexpr unsigned WS_Q = 1u << 20, WS_K = 3u << 20, WS_V = 5u << 20, WS_H = 8u << 20, WS_O = 16u << 20, WS_BYTES = 18u << 20;
constexpr unsigned VT = 544, VHEAD = 128 * VT;
constexpr int SLOT = 43008, OFF_B1 = 16384, OFF_B2 = 28672;
constexpr int L_K = 0, L_V = 65536, L_Q = 135168, L_KMAX = 143360, L_G2 = 143616;
constexpr int L_ROWST = 143616, L_COLS = L_ROWST + 512, L_PUBC = L_COLS + 1024, L_PUBR = L_PUBC + 832;
constexpr int L_MISC = 163840 - 64, LDS_BYTES = 163840;
constexpr int RED_PITCH = 272;
constexpr float QSCALE = 0.25f * 1.4426950408889634f;
constexpr float SAFE_BOUND = 50.0f;
constexpr unsigned SPIN_CAP = 1u << 18;

#define MFMA16(a, b, c) __builtin_amdgcn_mfma_f32_16x16x32_bf16(a, b, c, 0, 0, 0)
#define MFMA32(a, b, c) __builtin_amdgcn_mfma_f32_32x32x16_bf16(a, b, c, 0, 0, 0)
#define SMF(a, b, c, i) __builtin_amdgcn_smfmac_f32_32x32x32_bf16(a, b, c, i, 0, 0)
#define BAR() asm volatile("s_waitcnt lgkmcnt(0)\n\ts_barrier" ::: "memory")
#define WAITV(n) asm volatile("s_waitcnt vmcnt(" #n ")" ::: "memory")
#define RLX_AGENT __ATOMIC_RELAXED, __HIP_MEMORY_SCOPE_AGENT

#define CSTAMP(i) do {} while (0)
#define STAMP(i) do {} while (0)

__device__ __forceinline__ bf16x8 ld_b128(ldsp p) { return *(const LAS bf16x8*)p; }
__device__ __forceinline__ bf16x8 ld_tr2(ldsp p0, ldsp p1) {
    s16x4 lo = __builtin_amdgcn_ds_read_tr16_b64_v4i16((LAS s16x4*)p0);
    s16x4 hi = __builtin_amdgcn_ds_read_tr16_b64_v4i16((LAS s16x4*)p1);
    s16x8 t = __builtin_shufflevector(lo, hi, 0, 1, 2, 3, 4, 5, 6, 7);
    return __builtin_bit_cast(bf16x8, t);
}
__device__ __forceinline__ unsigned pk2(float a, float b) { f32x2 v = {a, b}; return __builtin_bit_cast(unsigned, __builtin_convertvector(v, bf16x2)); }
__device__ __forceinline__ float bf_lo(unsigned u) { return __uint_as_float(u << 16); }
__device__ __forceinline__ float bf_hi(unsigned u) { return __uint_as_float(u & 0xffff0000u); }
__device__ __forceinline__ float rbf(float a) { return (float)(__bf16)a; }
__device__ __forceinline__ float dot2(unsigned a, unsigned b, float c) { return __builtin_amdgcn_fdot2_f32_bf16(__builtin_bit_cast(bf16x2, a), __builtin_bit_cast(bf16x2, b), c, false); }
__device__ __forceinline__ float sum_x16(float v) { auto r = __builtin_amdgcn_permlane16_swap(__float_as_uint(v), __float_as_uint(v), false, false); return __uint_as_float(r[0]) + __uint_as_float(r[1]); }
__device__ __forceinline__ float sum_x32(float v) { auto r = __builtin_amdgcn_permlane32_swap(__float_as_uint(v), __float_as_uint(v), false, false); return __uint_as_float(r[0]) + __uint_as_float(r[1]); }
__device__ __forceinline__ float max_x16(float v) { auto r = __builtin_amdgcn_permlane16_swap(__float_as_uint(v), __float_as_uint(v), false, false); return fmaxf(__uint_as_float(r[0]), __uint_as_float(r[1])); }
__device__ __forceinline__ float max_x32(float v) { auto r = __builtin_amdgcn_permlane32_swap(__float_as_uint(v), __float_as_uint(v), false, false); return fmaxf(__uint_as_float(r[0]), __uint_as_float(r[1])); }
template <int N> __device__ __forceinline__ float ror16(float v) { return __uint_as_float(__builtin_amdgcn_update_dpp(0u, __float_as_uint(v), 0x120 + N, 0xf, 0xf, false)); }
__device__ __forceinline__ float nmax(float a, float b) { return (a > b || a != a) ? a : b; }
__device__ __forceinline__ float nmax_row16(float v) { v = nmax(v, ror16<8>(v)); v = nmax(v, ror16<4>(v)); v = nmax(v, ror16<2>(v)); return nmax(v, ror16<1>(v)); }

__device__ __forceinline__ void glds16(const char* g, ldsp l) { __builtin_amdgcn_global_load_lds((const GAS unsigned*)g, (LAS unsigned*)l, 16, 0, 0); }

__device__ __forceinline__ void wait_flags(const char* ws, unsigned off, u64 tag) {
    const GAS u64* p = (const GAS u64*)(ws + off);
#pragma unroll 1
    for (unsigned spins = 0; spins < SPIN_CAP; ++spins) {
        const u64 v = __hip_atomic_load(p, RLX_AGENT);
        if (__all(v == tag)) break;
        __builtin_amdgcn_s_sleep(1);
    }
    asm volatile("" ::: "memory");
}
__device__ __forceinline__ void arrive_publish(ldsp lds, int ctr, int n, char* ws, unsigned flag_off, int lane) {
    WAITV(0);
    if (lane == 0) {
        const unsigned old = __hip_atomic_fetch_add((LAS unsigned*)(lds + L_MISC + 16 + 4 * ctr), 1u, __ATOMIC_RELAXED, __HIP_MEMORY_SCOPE_WORKGROUP);
        if (old == (unsigned)(n - 1)) {
            const u64 tag = *(const volatile LAS u64*)(lds + L_MISC);
            __hip_atomic_store((GAS u64*)(ws + flag_off), tag, RLX_AGENT);
        }
    }
}

__device__ __forceinline__ float gelu_tanh(float x) {
    constexpr float C1 = 2.0f * 0.7978845608028654f * 1.4426950408889634f, C2 = C1 * 0.044715f;
    const float t = __builtin_fmaf(x * x, -C2, -C1);
    const float e = __builtin_amdgcn_exp2f(x * t);
    return x * __builtin_amdgcn_rcpf(1.0f + e);
}

template <int NT> struct G1Frag { bf16x8 x0, x1, w[NT]; };
__device__ __forceinline__ u32x4 widen(u32x2 d0, u32x2 d1) {
    const auto a = __builtin_amdgcn_permlane16_swap(d0[0], d1[0], false, false), b = __builtin_amdgcn_permlane16_swap(d0[1], d1[1], false, false);
    return u32x4{a[0], b[0], a[1], b[1]};
}
__device__ __forceinline__ float sum_row16(float v) { v += ror16<8>(v); v += ror16<4>(v); v += ror16<2>(v); return v + ror16<1>(v); }
__device__ __forceinline__ void g1_rowstat(ldsp lds, const char* x, int w, int lane, int mt, int nt) {
    const u32x4 xr = *(const GAS u32x4*)(x + (size_t)(128 * mt + 8 * nt + w) * 1024 + lane * 16);
    float e[8], s = 0.f;
#pragma unroll
    for (int i = 0; i < 4; ++i) { e[2 * i] = bf_lo(xr[i]); e[2 * i + 1] = bf_hi(xr[i]); s += e[2 * i] + e[2 * i + 1]; }
    const float mu = sum_x32(sum_x16(sum_row16(s))) * (1.0f / DM);
    float v = 0.f;
#pragma unroll
    for (int i = 0; i < 8; ++i) v = __builtin_fmaf(e[i] - mu, e[i] - mu, v);
    v = sum_x32(sum_x16(sum_row16(v))) * (1.0f / DM);
    if (lane == 0) *(LAS unsigned*)(lds + L_PUBR + w * 4) = pk2(mu, __builtin_amdgcn_sqrtf(rbf(rbf(v) + 1e-5f)));
}
template <int NF>
__device__ __forceinline__ void g1_compute(ldsp lds, char* ws, const __amdgpu_buffer_rsrc_t wsr, const __hip_bfloat16* b1, int wm, int lane, int mt, int nt) {
    constexpr int WN = (NF == 4) ? 0 : 1, NT = 3 + NF;
    const int m = lane & 15, g = lane >> 4, q = (lane & 15) >> 2, p = lane & 3;
    f32x4 acc[NT][2];
#pragma unroll
    for (int t = 0; t < NT; ++t) { acc[t][0] = f32x4{}; acc[t][1] = f32x4{}; }
    const int arow = 32 * wm + m, swz = (arow >> 1) & 7;
    const int aoff0 = arow * 128 + ((g ^ swz) << 4), aoff1 = arow * 128 + (((4 + g) ^ swz) << 4);
    const int b1e = OFF_B1 + (8 * g + q) * 192 + 32 * (g & 1) + 8 * p, b1o = OFF_B1 + (8 * g + q) * 192 + 32 * ((g & 1) ^ 1) + 8 * p;
    const int b2b = OFF_B2 + (16 * (g >> 1) + 4 * (g & 1) + q) * 224 + 8 * p;
    auto load = [&](G1Frag<NT>& f, ldsp base, int ks) {
        f.x0 = ld_b128(base + (ks ? aoff1 : aoff0));
#pragma unroll
        for (int t = 0; t < 3; ++t) { const int tt = 3 * WN + t; ldsp pp = base + ((tt & 1) ? b1o : b1e) + (tt & ~1) * 32 + ks * 32 * 192; f.w[t] = ld_tr2(pp, pp + 4 * 192);
            if (t == 0) f.x1 = ld_b128(base + (ks ? aoff1 : aoff0) + 2048); }
#pragma unroll
        for (int t = 0; t < NF; ++t) { ldsp pp = base + b2b + (4 * WN + t) * 32 + ks * 32 * 224; f.w[3 + t] = ld_tr2(pp, pp + 8 * 224); }
    };
    auto mma = [&](const G1Frag<NT>& f) {
#pragma unroll
        for (int t = 0; t < NT; ++t) { acc[t][0] = MFMA16(f.w[t], f.x0, acc[t][0]); acc[t][1] = MFMA16(f.w[t], f.x1, acc[t][1]); }
    };
    auto weave = [&]() {
        __builtin_amdgcn_sched_group_barrier(0x8, 1, 0); __builtin_amdgcn_sched_group_barrier(0x100, 2, 0);
        __builtin_amdgcn_sched_group_barrier(0x8, 1, 0); __builtin_amdgcn_sched_group_barrier(0x100, 2, 0);
#pragma unroll
        for (int i = 2; i < 2 * NT; ++i) { __builtin_amdgcn_sched_group_barrier(0x8, 1, 0); __builtin_amdgcn_sched_group_barrier(0x100, 1, 0); }
        __builtin_amdgcn_sched_barrier(0);
    };
    auto colpart = [&](const G1Frag<NT>& f) {
#pragma unroll
        for (int t = 0; t < NT; ++t)
            if ((t & 3) == wm) { const u32x4 u = __builtin_bit_cast(u32x4, f.w[t]);
                const float c = sum_x32(sum_x16(dot2(u[3], 0x3f803f80u, dot2(u[2], 0x3f803f80u, dot2(u[1], 0x3f803f80u, dot2(u[0], 0x3f803f80u, 0.f))))));
                if (g == 0) *(LAS float*)(lds + L_PUBC + (((t < 3) ? (3 * WN + t) * 16 : 96 + (4 * WN + t - 3) * 16) + m) * 4) = c; }
    };
    G1Frag<NT> X, Y;
    STAMP(16);
    BAR();
    STAMP(24); CSTAMP(7);
    load(X, lds, 0); __builtin_amdgcn_sched_barrier(0);
    load(Y, lds, 1); mma(X); weave();
    if (mt & 1) colpart(Y); else colpart(X);
    int slot = SLOT;
#pragma unroll 1
    for (int s = 1; s < 8; ++s) {
        BAR();
        ldsp base = lds + slot;
        slot = (slot == 2 * SLOT) ? 0 : slot + SLOT;
        load(X, base, 0); mma(Y); weave();
        load(Y, base, 1); mma(X); weave();
    }
    u32x2 bias[NF];
#pragma unroll
    for (int t = 0; t < NF; ++t) bias[t] = *(const GAS u32x2*)((const char*)b1 + (112 * nt + 64 * WN + 16 * t + 4 * g) * 2);
    BAR();
    mma(Y);
    STAMP(2); CSTAMP(8);
    float mean[2], rstd[2];
#pragma unroll
    for (int i = 0; i < 2; ++i) { const unsigned a = *(const LAS unsigned*)(lds + L_ROWST + (32 * wm + 16 * i + m) * 4); mean[i] = bf_lo(a); rstd[i] = __builtin_amdgcn_rcpf(bf_hi(a)); }
    auto colsum = [&](int col) { return *(const LAS f32x4*)(lds + L_COLS + (col + 4 * g) * 4); };
    const int blk32 = 4 * mt + wm;
    const unsigned fragoff = (g >> 1) * 512 + (16 * (g & 1) + m) * 16;
#pragma unroll
    for (int t = 0; t < 3; ++t) {
        const int tt = 3 * WN + t, c0 = 96 * nt + 16 * tt, kind = c0 >> 9, head = (c0 & 511) >> 4;
        const f32x4 c = colsum(16 * tt);
        const float sc = (kind == 0) ? QSCALE : 1.0f;
        u32x2 d[2];
#pragma unroll
        for (int i = 0; i < 2; ++i) {
            const float rs = rstd[i] * sc; float v[4];
#pragma unroll
            for (int r = 0; r < 4; ++r) v[r] = __builtin_fmaf(-mean[i], c[r], acc[t][i][r]) * rs;
            d[i] = u32x2{pk2(v[0], v[1]), pk2(v[2], v[3])};
        }
        const u32x4 e = widen(d[0], d[1]);
        const unsigned offv = WS_V + head * VHEAD + (2 * blk32 + (g & 1)) * VT + (g >> 1) * 16;
        __builtin_amdgcn_raw_buffer_store_b128(e, wsr, (kind < 2) ? WS_Q + kind * (2u << 20) + head * 65536 + blk32 * 1024 + fragoff : offv + m * 32, 0, 16);
        if (kind == 2) { if (m == 0) __builtin_amdgcn_raw_buffer_store_b128(u32x4{(g < 2) ? 0x3f80u : 0u, 0u, 0u, 0u}, wsr, offv + 512, 0, 16); }
        if (kind == 1) {
            float kn = sum_x32(dot2(e[3], e[3], dot2(e[2], e[2], dot2(e[1], e[1], dot2(e[0], e[0], 0.f)))));
            kn = nmax_row16(kn);
            const auto r = __builtin_amdgcn_permlane16_swap(__float_as_uint(kn), __float_as_uint(kn), false, false);
            kn = nmax(__uint_as_float(r[0]), __uint_as_float(r[1]));
            if (lane == 0) __builtin_amdgcn_raw_buffer_store_b32(__float_as_uint(kn), wsr, WS_KMAX + (head * 64 + blk32) * 4, 0, 16);
        }
    }
    STAMP(14);
    u32x4 hv[NF];
#pragma unroll
    for (int t = 0; t < NF; ++t) {
        const f32x4 c = colsum(96 + (4 * WN + t) * 16);
        const float bb[4] = {bf_lo(bias[t][0]), bf_hi(bias[t][0]), bf_lo(bias[t][1]), bf_hi(bias[t][1])};
        u32x2 d[2];
#pragma unroll
        for (int i = 0; i < 2; ++i) { float v[4];
#pragma unroll
            for (int r = 0; r < 4; ++r) v[r] = gelu_tanh(__builtin_fmaf(__builtin_fmaf(-mean[i], c[r], acc[3 + t][i][r]), rstd[i], bb[r]));
            d[i] = u32x2{pk2(v[0], v[1]), pk2(v[2], v[3])}; }
        hv[t] = widen(d[0], d[1]);
        asm volatile("" : "+v"(hv[t]));
    }
    STAMP(15);
    arrive_publish(lds, 0, 8, ws, WS_F1 + (mt * 16 + nt) * 128, lane);
    STAMP(3);
#pragma unroll
    for (int t = 0; t < NF; ++t) __builtin_amdgcn_raw_buffer_store_b128(hv[t], wsr, WS_H + (unsigned)(blk32 * 112 + 7 * nt + 4 * WN + t) * 1024 + fragoff, 0, 16);
}

template <int N>
__device__ __forceinline__ void g1_load(ldsp dst0, const char* src, const unsigned (&off)[N], unsigned step, int rot) {
    auto issue = [&](int s, int slot) {
        const char* sp = src + (size_t)((s + rot) & 7) * step;
#pragma unroll
        for (int i = 0; i < N; ++i) glds16(sp + off[i], dst0 + slot + i * 1024);
    };
#pragma unroll 1
    for (int s = 0; s < 3; ++s) issue(s, s * SLOT);
    asm volatile("s_waitcnt vmcnt(%0)\n\ts_barrier" ::"n"(2 * N) : "memory");
    asm volatile("s_waitcnt vmcnt(%0)\n\ts_barrier" ::"n"(N) : "memory");
    int slot = 0;
#pragma unroll 1
    for (int s = 3; s < 8; ++s) {
        issue(s, slot);
        slot = (slot == 2 * SLOT) ? 0 : slot + SLOT;
        asm volatile("s_waitcnt vmcnt(%0)\n\ts_barrier" ::"n"(N) : "memory");
    }
    asm volatile("s_waitcnt vmcnt(0)\n\ts_barrier" ::: "memory");
    asm volatile("s_barrier" ::: "memory");
}

__device__ __forceinline__ bf16x16 lo8(const f32x16& s) { return __builtin_bit_cast(bf16x16, __builtin_shufflevector(s, s, 0, 1, 2, 3, 4, 5, 6, 7)); }
__device__ __forceinline__ bf16x16 hi8(const f32x16& s) { return __builtin_bit_cast(bf16x16, __builtin_shufflevector(s, s, 8, 9, 10, 11, 12, 13, 14, 15)); }

#define ATT_ASM \
    "v_mov_b32 v96, %10\n" \
    "v_mov_b32 v97, %11\n" \
    "v_mov_b32 v98, %12\n" \
    "v_mov_b32 v99, %13\n" \
    "v_mov_b32 v125, %15\n" \
    "v_mov_b32 v126, %16\n" \
    "v_mov_b32 v127, %17\n" \
    "ds_read_b128 v[100:103], v125\n" \
    "ds_read_b128 v[104:107], v125 offset:1024\n" \
    "ds_read_b64_tr_b16 v[108:109], v126\n" \
    "ds_read_b64_tr_b16 v[110:111], v127\n" \
    "ds_read_b64_tr_b16 v[116:117], v126 offset:544\n" \
    "ds_read_b64_tr_b16 v[118:119], v127 offset:544\n" \
    "v_mov_b32 v124, 0xdddddddd\n" \
    "v_mov_b32 v80, %14\n" \
    "v_mov_b32 v81, %14\n" \
    "v_mov_b32 v82, %14\n" \
    "v_mov_b32 v83, %14\n" \
    "v_mov_b32 v84, %14\n" \
    "v_mov_b32 v85, %14\n" \
    "v_mov_b32 v86, %14\n" \
    "v_mov_b32 v87, %14\n" \
    "v_mov_b32 v88, %14\n" \
    "v_mov_b32 v89, %14\n" \
    "v_mov_b32 v90, %14\n" \
    "v_mov_b32 v91, %14\n" \
    "v_mov_b32 v92, %14\n" \
    "v_mov_b32 v93, %14\n" \
    "v_mov_b32 v94, %14\n" \
    "v_mov_b32 v95, %14\n" \
    "v_mov_b32 v32, 0\n" \
    "v_mov_b32 v33, 0\n" \
    "v_mov_b32 v34, 0\n" \
    "v_mov_b32 v35, 0\n" \
    "v_mov_b32 v36, 0\n" \
    "v_mov_b32 v37, 0\n" \
    "v_mov_b32 v38, 0\n" \
    "v_mov_b32 v39, 0\n" \
    "v_mov_b32 v40, 0\n" \
    "v_mov_b32 v41, 0\n" \
    "v_mov_b32 v42, 0\n" \
    "v_mov_b32 v43, 0\n" \
    "v_mov_b32 v44, 0\n" \
    "v_mov_b32 v45, 0\n" \
    "v_mov_b32 v46, 0\n" \
    "v_mov_b32 v47, 0\n" \
    "v_mov_b32 v72, 0\n" \
    "v_mov_b32 v73, 0\n" \
    "v_mov_b32 v74, 0\n" \
    "v_mov_b32 v75, 0\n" \
    "v_mov_b32 v76, 0\n" \
    "v_mov_b32 v77, 0\n" \
    "v_mov_b32 v78, 0\n" \
    "v_mov_b32 v79, 0\n" \
    "v_mov_b32 v120, 0\n" \
    "v_mov_b32 v121, 0\n" \
    "v_mov_b32 v122, 0\n" \
    "v_mov_b32 v123, 0\n" \
    "s_waitcnt lgkmcnt(0)\n" \
    "v_mfma_f32_32x32x16_bf16 v[48:63], v[100:103], v[96:99], v[80:95]\n" \
    "s_nop 7\n" \
    "s_nop 3\n" \
    "ATT_LOOP_%=:\n" \
    "s_waitcnt lgkmcnt(0)\n" \
    "v_exp_f32 v48, v48\n" \
    "v_exp_f32 v49, v49\n" \
    "v_smfmac_f32_32x32x32_bf16 v[32:47], v[120:123], v[72:79], v124\n" \
    "ds_read_b64_tr_b16 v[112:113], v126 offset:1088\n" \
    "ds_read_b64_tr_b16 v[114:115], v127 offset:1088\n" \
    "v_exp_f32 v50, v50\n" \
    "v_exp_f32 v51, v51\n" \
    "v_exp_f32 v52, v52\n" \
    "v_exp_f32 v53, v53\n" \
    "v_mfma_f32_32x32x16_bf16 v[64:79], v[104:107], v[96:99], v[80:95]\n" \
    "ds_read_b128 v[100:103], v125 offset:2048\n" \
    "ds_read_b64_tr_b16 v[120:121], v126 offset:1632\n" \
    "ds_read_b64_tr_b16 v[122:123], v127 offset:1632\n" \
    "v_exp_f32 v54, v54\n" \
    "v_exp_f32 v55, v55\n" \
    "v_exp_f32 v56, v56\n" \
    "v_exp_f32 v57, v57\n" \
    "v_smfmac_f32_32x32x32_bf16 v[32:47], v[108:111], v[48:55], v124\n" \
    "v_exp_f32 v58, v58\n" \
    "v_exp_f32 v59, v59\n" \
    "v_exp_f32 v60, v60\n" \
    "v_exp_f32 v61, v61\n" \
    "v_exp_f32 v62, v62\n" \
    "v_exp_f32 v63, v63\n" \
    "s_waitcnt lgkmcnt(0)\n" \
    "v_exp_f32 v64, v64\n" \
    "v_exp_f32 v65, v65\n" \
    "v_smfmac_f32_32x32x32_bf16 v[32:47], v[116:119], v[56:63], v124\n" \
    "ds_read_b64_tr_b16 v[108:109], v126 offset:2176\n" \
    "ds_read_b64_tr_b16 v[110:111], v127 offset:2176\n" \
    "v_exp_f32 v66, v66\n" \
    "v_exp_f32 v67, v67\n" \
    "v_exp_f32 v68, v68\n" \
    "v_exp_f32 v69, v69\n" \
    "v_mfma_f32_32x32x16_bf16 v[48:63], v[100:103], v[96:99], v[80:95]\n" \
    "ds_read_b128 v[104:107], v125 offset:3072\n" \
    "ds_read_b64_tr_b16 v[116:117], v126 offset:2720\n" \
    "ds_read_b64_tr_b16 v[118:119], v127 offset:2720\n" \
    "v_exp_f32 v70, v70\n" \
    "v_exp_f32 v71, v71\n" \
    "v_exp_f32 v72, v72\n" \
    "v_exp_f32 v73, v73\n" \
    "v_smfmac_f32_32x32x32_bf16 v[32:47], v[112:115], v[64:71], v124\n" \
    "v_exp_f32 v74, v74\n" \
    "v_exp_f32 v75, v75\n" \
    "v_exp_f32 v76, v76\n" \
    "v_exp_f32 v77, v77\n" \
    "v_exp_f32 v78, v78\n" \
    "v_exp_f32 v79, v79\n" \
    "s_waitcnt lgkmcnt(0)\n" \
    "v_exp_f32 v48, v48\n" \
    "v_exp_f32 v49, v49\n" \
    "v_smfmac_f32_32x32x32_bf16 v[32:47], v[120:123], v[72:79], v124\n" \
    "ds_read_b64_tr_b16 v[112:113], v126 offset:3264\n" \
    "ds_read_b64_tr_b16 v[114:115], v127 offset:3264\n" \
    "v_exp_f32 v50, v50\n" \
    "v_exp_f32 v51, v51\n" \
    "v_exp_f32 v52, v52\n" \
    "v_exp_f32 v53, v53\n" \
    "v_mfma_f32_32x32x16_bf16 v[64:79], v[104:107], v[96:99], v[80:95]\n" \
    "ds_read_b128 v[100:103], v125 offset:4096\n" \
    "ds_read_b64_tr_b16 v[120:121], v126 offset:3808\n" \
    "ds_read_b64_tr_b16 v[122:123], v127 offset:3808\n" \
    "v_exp_f32 v54, v54\n" \
    "v_exp_f32 v55, v55\n" \
    "v_exp_f32 v56, v56\n" \
    "v_exp_f32 v57, v57\n" \
    "v_smfmac_f32_32x32x32_bf16 v[32:47], v[108:111], v[48:55], v124\n" \
    "v_exp_f32 v58, v58\n" \
    "v_exp_f32 v59, v59\n" \
    "v_exp_f32 v60, v60\n" \
    "v_exp_f32 v61, v61\n" \
    "v_exp_f32 v62, v62\n" \
    "v_exp_f32 v63, v63\n" \
    "s_waitcnt lgkmcnt(0)\n" \
    "v_exp_f32 v64, v64\n" \
    "v_exp_f32 v65, v65\n" \
    "v_smfmac_f32_32x32x32_bf16 v[32:47], v[116:119], v[56:63], v124\n" \
    "ds_read_b64_tr_b16 v[108:109], v126 offset:4352\n" \
    "ds_read_b64_tr_b16 v[110:111], v127 offset:4352\n" \
    "v_exp_f32 v66, v66\n" \
    "v_exp_f32 v67, v67\n" \
    "v_exp_f32 v68, v68\n" \
    "v_exp_f32 v69, v69\n" \
    "v_mfma_f32_32x32x16_bf16 v[48:63], v[100:103], v[96:99], v[80:95]\n" \
    "ds_read_b128 v[104:107], v125 offset:5120\n" \
    "ds_read_b64_tr_b16 v[116:117], v126 offset:4896\n" \
    "ds_read_b64_tr_b16 v[118:119], v127 offset:4896\n" \
    "v_exp_f32 v70, v70\n" \
    "v_exp_f32 v71, v71\n" \
    "v_exp_f32 v72, v72\n" \
    "v_exp_f32 v73, v73\n" \
    "v_smfmac_f32_32x32x32_bf16 v[32:47], v[112:115], v[64:71], v124\n" \
    "v_exp_f32 v74, v74\n" \
    "v_exp_f32 v75, v75\n" \
    "v_exp_f32 v76, v76\n" \
    "v_exp_f32 v77, v77\n" \
    "v_exp_f32 v78, v78\n" \
    "v_exp_f32 v79, v79\n" \
    "s_waitcnt lgkmcnt(0)\n" \
    "v_exp_f32 v48, v48\n" \
    "v_exp_f32 v49, v49\n" \
    "v_smfmac_f32_32x32x32_bf16 v[32:47], v[120:123], v[72:79], v124\n" \
    "ds_read_b64_tr_b16 v[112:113], v126 offset:5440\n" \
    "ds_read_b64_tr_b16 v[114:115], v127 offset:5440\n" \
    "v_exp_f32 v50, v50\n" \
    "v_exp_f32 v51, v51\n" \
    "v_exp_f32 v52, v52\n" \
    "v_exp_f32 v53, v53\n" \
    "v_mfma_f32_32x32x16_bf16 v[64:79], v[104:107], v[96:99], v[80:95]\n" \
    "ds_read_b128 v[100:103], v125 offset:6144\n" \
    "ds_read_b64_tr_b16 v[120:121], v126 offset:5984\n" \
    "ds_read_b64_tr_b16 v[122:123], v127 offset:5984\n" \
    "v_exp_f32 v54, v54\n" \
    "v_exp_f32 v55, v55\n" \
    "v_exp_f32 v56, v56\n" \
    "v_exp_f32 v57, v57\n" \
    "v_smfmac_f32_32x32x32_bf16 v[32:47], v[108:111], v[48:55], v124\n" \
    "v_exp_f32 v58, v58\n" \
    "v_exp_f32 v59, v59\n" \
    "v_exp_f32 v60, v60\n" \
    "v_exp_f32 v61, v61\n" \
    "v_exp_f32 v62, v62\n" \
    "v_exp_f32 v63, v63\n" \
    "s_waitcnt lgkmcnt(0)\n" \
    "v_exp_f32 v64, v64\n" \
    "v_exp_f32 v65, v65\n" \
    "v_smfmac_f32_32x32x32_bf16 v[32:47], v[116:119], v[56:63], v124\n" \
    "ds_read_b64_tr_b16 v[108:109], v126 offset:6528\n" \
    "ds_read_b64_tr_b16 v[110:111], v127 offset:6528\n" \
    "v_exp_f32 v66, v66\n" \
    "v_exp_f32 v67, v67\n" \
    "v_exp_f32 v68, v68\n" \
    "v_exp_f32 v69, v69\n" \
    "v_mfma_f32_32x32x16_bf16 v[48:63], v[100:103], v[96:99], v[80:95]\n" \
    "ds_read_b128 v[104:107], v125 offset:7168\n" \
    "ds_read_b64_tr_b16 v[116:117], v126 offset:7072\n" \
    "ds_read_b64_tr_b16 v[118:119], v127 offset:7072\n" \
    "v_exp_f32 v70, v70\n" \
    "v_exp_f32 v71, v71\n" \
    "v_exp_f32 v72, v72\n" \
    "v_exp_f32 v73, v73\n" \
    "v_smfmac_f32_32x32x32_bf16 v[32:47], v[112:115], v[64:71], v124\n" \
    "v_exp_f32 v74, v74\n" \
    "v_exp_f32 v75, v75\n" \
    "v_exp_f32 v76, v76\n" \
    "v_exp_f32 v77, v77\n" \
    "v_exp_f32 v78, v78\n" \
    "v_exp_f32 v79, v79\n" \
    "s_waitcnt lgkmcnt(0)\n" \
    "v_exp_f32 v48, v48\n" \
    "v_exp_f32 v49, v49\n" \
    "v_smfmac_f32_32x32x32_bf16 v[32:47], v[120:123], v[72:79], v124\n" \
    "ds_read_b64_tr_b16 v[112:113], v126 offset:7616\n" \
    "ds_read_b64_tr_b16 v[114:115], v127 offset:7616\n" \
    "v_exp_f32 v50, v50\n" \
    "v_exp_f32 v51, v51\n" \
    "v_exp_f32 v52, v52\n" \
    "v_exp_f32 v53, v53\n" \
    "v_mfma_f32_32x32x16_bf16 v[64:79], v[104:107], v[96:99], v[80:95]\n" \
    "ds_read_b128 v[100:103], v125 offset:8192\n" \
    "ds_read_b64_tr_b16 v[120:121], v126 offset:8160\n" \
    "ds_read_b64_tr_b16 v[122:123], v127 offset:8160\n" \
    "v_exp_f32 v54, v54\n" \
    "v_exp_f32 v55, v55\n" \
    "v_exp_f32 v56, v56\n" \
    "v_exp_f32 v57, v57\n" \
    "v_smfmac_f32_32x32x32_bf16 v[32:47], v[108:111], v[48:55], v124\n" \
    "v_exp_f32 v58, v58\n" \
    "v_exp_f32 v59, v59\n" \
    "v_exp_f32 v60, v60\n" \
    "v_exp_f32 v61, v61\n" \
    "v_exp_f32 v62, v62\n" \
    "v_exp_f32 v63, v63\n" \
    "s_waitcnt lgkmcnt(0)\n" \
    "v_exp_f32 v64, v64\n" \
    "v_exp_f32 v65, v65\n" \
    "v_smfmac_f32_32x32x32_bf16 v[32:47], v[116:119], v[56:63], v124\n" \
    "ds_read_b64_tr_b16 v[108:109], v126 offset:8704\n" \
    "ds_read_b64_tr_b16 v[110:111], v127 offset:8704\n" \
    "v_exp_f32 v66, v66\n" \
    "v_exp_f32 v67, v67\n" \
    "v_exp_f32 v68, v68\n" \
    "v_exp_f32 v69, v69\n" \
    "v_mfma_f32_32x32x16_bf16 v[48:63], v[100:103], v[96:99], v[80:95]\n" \
    "ds_read_b128 v[104:107], v125 offset:9216\n" \
    "ds_read_b64_tr_b16 v[116:117], v126 offset:9248\n" \
    "ds_read_b64_tr_b16 v[118:119], v127 offset:9248\n" \
    "v_exp_f32 v70, v70\n" \
    "v_exp_f32 v71, v71\n" \
    "v_exp_f32 v72, v72\n" \
    "v_exp_f32 v73, v73\n" \
    "v_smfmac_f32_32x32x32_bf16 v[32:47], v[112:115], v[64:71], v124\n" \
    "v_exp_f32 v74, v74\n" \
    "v_exp_f32 v75, v75\n" \
    "v_exp_f32 v76, v76\n" \
    "v_exp_f32 v77, v77\n" \
    "v_exp_f32 v78, v78\n" \
    "v_exp_f32 v79, v79\n" \
    "v_add_u32 v125, 8192, v125\n" \
    "v_add_u32 v126, 8704, v126\n" \
    "v_add_u32 v127, 8704, v127\n" \
    "s_add_i32 %9, %9, -1\n" \
    "s_cmp_lg_u32 %9, 0\n" \
    "s_cbranch_scc1 ATT_LOOP_%=\n" \
    "v_smfmac_f32_32x32x32_bf16 v[32:47], v[120:123], v[72:79], v124\n" \
    "s_waitcnt lgkmcnt(0)\n" \
    "s_nop 7\n" \
    "s_nop 3\n" \
    "v_mov_b32 %0, v32\n" \
    "v_mov_b32 %1, v33\n" \
    "v_mov_b32 %2, v34\n" \
    "v_mov_b32 %3, v35\n" \
    "v_mov_b32 %4, v36\n" \
    "v_mov_b32 %5, v37\n" \
    "v_mov_b32 %6, v38\n" \
    "v_mov_b32 %7, v39\n" \
    "v_mov_b32 %8, v40\n" \
    ""
#define ATT_CLOBBER "v32", "v33", "v34", "v35", "v36", "v37", "v38", "v39", "v40", "v41", "v42", "v43", "v44", "v45", "v46", "v47", "v48", "v49", "v50", "v51", "v52", "v53", "v54", "v55", "v56", "v57", "v58", "v59", "v60", "v61", "v62", "v63", "v64", "v65", "v66", "v67", "v68", "v69", "v70", "v71", "v72", "v73", "v74", "v75", "v76", "v77", "v78", "v79", "v80", "v81", "v82", "v83", "v84", "v85", "v86", "v87", "v88", "v89", "v90", "v91", "v92", "v93", "v94", "v95", "v96", "v97", "v98", "v99", "v100", "v101", "v102", "v103", "v104", "v105", "v106", "v107", "v108", "v109", "v110", "v111", "v112", "v113", "v114", "v115", "v116", "v117", "v118", "v119", "v120", "v121", "v122", "v123", "v124", "v125", "v126", "v127"
__device__ __forceinline__ void attention(ldsp lds, char* ws, const __amdgpu_buffer_rsrc_t wsr, int w, int lane, int head, int qt) {
    const bf16x8 qf = ld_b128(lds + L_Q + w * 1024 + lane * 16);
    float km = *(const LAS float*)(lds + L_KMAX + lane * 4);
    km = nmax_row16(km);
    { auto r = __builtin_amdgcn_permlane16_swap(__float_as_uint(km), __float_as_uint(km), false, false); km = nmax(__uint_as_float(r[0]), __uint_as_float(r[1])); }
    { auto r = __builtin_amdgcn_permlane32_swap(__float_as_uint(km), __float_as_uint(km), false, false); km = nmax(__uint_as_float(r[0]), __uint_as_float(r[1])); }
    const u32x4 qu = __builtin_bit_cast(u32x4, qf);
    const float qn = sum_x32(dot2(qu[3], qu[3], dot2(qu[2], qu[2], dot2(qu[1], qu[1], dot2(qu[0], qu[0], 0.f)))));
    const float bound = sqrtf(qn * km) * 1.001f;
    const int G = lane >> 4, q = (lane & 15) >> 2, p = lane & 3;
    ldsp kp = lds + L_K + lane * 16;
    ldsp v0 = lds + L_V + ((G & 1) ? 512 + 8 * p : (8 * (G >> 1) + q) * 32 + 8 * p);
    ldsp v1 = lds + L_V + ((G & 1) ? 512 + 8 * p : (8 * (G >> 1) + 4 + q) * 32 + 8 * p);
    f32x16 o = {};
    if (__all(bound <= SAFE_BOUND)) {
        int cnt = 8;
        float r0, r1, r2, r3, r4, r5, r6, r7, r8;
        asm volatile(ATT_ASM
                     : "=v"(r0), "=v"(r1), "=v"(r2), "=v"(r3), "=v"(r4), "=v"(r5), "=v"(r6), "=v"(r7), "=v"(r8), "+s"(cnt)
                     : "v"(qu[0]), "v"(qu[1]), "v"(qu[2]), "v"(qu[3]), "v"(-bound), "v"((unsigned)(uintptr_t)kp), "v"((unsigned)(uintptr_t)v0), "v"((unsigned)(uintptr_t)v1)
                     : "memory", "scc", ATT_CLOBBER);
        o[0] = r0; o[1] = r1; o[2] = r2; o[3] = r3; o[4] = r4; o[5] = r5; o[6] = r6; o[7] = r7; o[8] = r8;
    } else {
        const int idx = (int)0xDDDDDDDDu;
        float mx = -INFINITY;
#pragma unroll 1
        for (int it = 0; it < 64; ++it) {
            f32x16 s = MFMA32(ld_b128(kp), qf, f32x16{});
            const bf16x8 va = ld_tr2(v0, v1), vb = ld_tr2(v0 + VT, v1 + VT);
            float bm = s[0];
#pragma unroll
            for (int r = 1; r < 16; ++r) bm = fmaxf(bm, s[r]);
            const float mn = fmaxf(mx, max_x32(bm));
            const float al = __builtin_amdgcn_exp2f(mx - mn);
            mx = mn;
#pragma unroll
            for (int r = 0; r < 16; ++r) { o[r] *= al; s[r] = __builtin_amdgcn_exp2f(s[r] - mn); }
            o = SMF(va, lo8(s), o, idx); o = SMF(vb, hi8(s), o, idx);
            kp += 1024; v0 += 2 * VT; v1 += 2 * VT;
        }
    }
    const float inv = 1.0f / sum_x32(o[8]);
    const u32x4 d = {pk2(o[0] * inv, o[1] * inv), pk2(o[2] * inv, o[3] * inv), pk2(o[4] * inv, o[5] * inv), pk2(o[6] * inv, o[7] * inv)};
    __builtin_amdgcn_raw_buffer_store_b128(d, wsr, WS_O + (unsigned)((8 * qt + w) * 32 + head) * 1024 + lane * 16, 0, 16);
    CSTAMP(13);
    arrive_publish(lds, 2, 8, ws, WS_F3 + (head * 8 + qt) * 128, lane);
}

struct G2Ctx { ldsp wr, rd0, rd1; };
__device__ __forceinline__ void g2_wfrag(const G2Ctx& c, int half, const u32x4& r0, const u32x4& r1, bf16x8& w0, bf16x8& w1) {
    *(LAS u32x4*)(c.wr + half * 2048) = r0; *(LAS u32x4*)(c.wr + half * 2048 + 1024) = r1;
    w0 = ld_tr2(c.rd0 + half * 2048, c.rd0 + half * 2048 + 512); w1 = ld_tr2(c.rd1 + half * 2048, c.rd1 + half * 2048 + 512);
}
#define G2SLEEP 2
template <int PACE>
__device__ __forceinline__ void g2_mma(f32x16 (&acc)[2][2], const bf16x8& w0, const bf16x8& w1, const u32x4& a0, const u32x4& a1) {
    const bf16x8 h0 = __builtin_bit_cast(bf16x8, a0), h1 = __builtin_bit_cast(bf16x8, a1);
    auto gap = [&]() { if (PACE > 0) { __builtin_amdgcn_sched_barrier(0); __builtin_amdgcn_s_sleep(PACE); __builtin_amdgcn_sched_barrier(0); } };
    acc[0][0] = MFMA32(w0, h0, acc[0][0]); gap(); acc[0][1] = MFMA32(w0, h1, acc[0][1]); gap();
    acc[1][0] = MFMA32(w1, h0, acc[1][0]); gap(); acc[1][1] = MFMA32(w1, h1, acc[1][1]); gap();
}

__global__ __launch_bounds__(768, 3) void fused_block_kernel(const __hip_bfloat16* __restrict__ x, const __hip_bfloat16* __restrict__ wqkv, const __hip_bfloat16* __restrict__ wo,
                                                             const __hip_bfloat16* __restrict__ w1, const __hip_bfloat16* __restrict__ b1, const __hip_bfloat16* __restrict__ w2,
                                                             const __hip_bfloat16* __restrict__ b2, __hip_bfloat16* __restrict__ out, char* __restrict__ ws) {
    extern __shared__ __attribute__((aligned(16))) char smem[];
    ldsp lds = (ldsp)smem;
    const int lane = threadIdx.x & 63, w = __builtin_amdgcn_readfirstlane(threadIdx.x >> 6);
    const int bg = blockIdx.x & 7, br = blockIdx.x >> 3;
    const int mt = (bg & 1) * 8 + (br & 7), nt = (bg >> 1) * 4 + (br >> 3);
    const int head = bg * 4 + (br >> 3), qt = br & 7;
    const int rt = bg * 4 + (br >> 3), ct = br & 7;
    const __amdgpu_buffer_rsrc_t wsr = __builtin_amdgcn_make_buffer_rsrc(ws, 0, WS_BYTES, 0x00020000);
    STAMP(0); CSTAMP(10);
    u32x4 xres; u32x4 bres;
    const int orow = 64 * rt + (threadIdx.x >> 3), ocol = 64 * ct + (threadIdx.x & 7) * 8;
    if (w < 8) {
        g1_rowstat(lds, (const char*)x, w, lane, mt, nt);
        if (w < 4) g1_compute<4>(lds, ws, wsr, b1, w, lane, mt, nt); else g1_compute<3>(lds, ws, wsr, b1, w - 4, lane, mt, nt);
        STAMP(4);
        xres = *(const GAS u32x4*)((const char*)x + (orow * DM + ocol) * 2);
        bres = *(const GAS u32x4*)((const char*)b2 + ocol * 2);
        BAR();
        arrive_publish(lds, 1, 8, ws, WS_F2 + (mt * 16 + nt) * 128, lane);
        STAMP(5); CSTAMP(12);
        attention(lds, ws, wsr, w, lane, head, qt);
        STAMP(6);
        BAR();
    } else {
        const int j = w - 8;
        if (j == 0) {
            unsigned off[16];
#pragma unroll
            for (int i = 0; i < 16; ++i) { const int row = 8 * i + (lane >> 3); off[i] = row * 1024 + (((lane & 7) ^ ((row >> 1) & 7)) << 4); }
            g1_load<16>(lds, (const char*)x + (size_t)mt * 128 * 1024, off, 128, mt >> 1);
        } else if (j == 1) {
            const u64 tg = __hip_atomic_load((const GAS u64*)(ws + WS_EPOCH), RLX_AGENT) * 6364136223846793005ull + 1442695040888963407ull;
            if (lane == 0) { *(LAS u64*)(lds + L_MISC) = tg; *(LAS u32x4*)(lds + L_MISC + 16) = u32x4{0u, 0u, 0u, 0u}; }
            BAR(); BAR();
            if (lane < 54) __builtin_amdgcn_raw_buffer_store_b128(*(const LAS u32x4*)(lds + L_PUBC + lane * 16), wsr, WS_ST + (mt * 16 + nt) * 1024 + lane * 16, 0, 16);
            BAR(); BAR();
            WAITV(0);
            if (lane == 0) __hip_atomic_store((GAS u64*)(ws + WS_F0 + (mt * 16 + nt) * 128), tg, RLX_AGENT);
            BAR(); BAR();
            const unsigned foff = WS_F0 + ((lane & 16) ? mt * 16 + (lane & 15) : (lane & 15) * 16 + nt) * 128;
            const u64 fv = __hip_atomic_load((const GAS u64*)(ws + foff), RLX_AGENT);
            BAR();
            if (!__all(fv == tg)) wait_flags(ws, foff, tg);
            u32x4 cv[16];
            const int cl = (lane < 52) ? lane : 0;
#pragma unroll
            for (int k = 0; k < 16; ++k) cv[k] = __builtin_amdgcn_raw_buffer_load_b128(wsr, WS_ST + nt * 1024 + cl * 16, k * 16384, 16);
            const u32x4 rv = __builtin_amdgcn_raw_buffer_load_b128(wsr, WS_ST + (mt * 16 + ((lane & 31) >> 1)) * 1024 + 832 + (lane & 1) * 16, 0, 16);
            BAR();
            f32x4 c = __builtin_bit_cast(f32x4, cv[0]);
#pragma unroll
            for (int k = 1; k < 16; ++k) c += __builtin_bit_cast(f32x4, cv[k]);
            *(LAS f32x4*)(lds + L_COLS + cl * 16) = c;
            *(LAS u32x4*)(lds + L_ROWST + (lane & 31) * 16) = rv;
            BAR();
        } else if (j == 2) {
            unsigned off[12];
#pragma unroll
            for (int i = 0; i < 12; ++i) { const int e = 64 * i + lane, k = e / 12, pos = e - 12 * k; off[i] = k * 3072 + ((pos ^ (((k >> 3) & 1) << 1)) << 4); }
            g1_load<12>(lds + OFF_B1, (const char*)wqkv + nt * 192, off, 64 * 3072, mt >> 1);
        } else {
            unsigned off[14];
#pragma unroll
            for (int i = 0; i < 14; ++i) { const int e = 64 * i + lane, rho = e / 14, pos = e - 14 * rho; const int k = (rho & ~12) | ((rho & 4) << 1) | ((rho & 8) >> 1); off[i] = k * 3584 + (pos << 4); }
            g1_load<14>(lds + OFF_B2, (const char*)w1 + nt * 224, off, 64 * 3584, mt >> 1);
        }
        STAMP(2);
        const u64 tag = *(const volatile LAS u64*)(lds + L_MISC);
        {
            const int ntk = (512 + 16 * head) / 96, ntv = (1024 + 16 * head) / 96, ntq = (16 * head) / 96;
            const int l16 = lane & 15;
            const int fm = (l16 < 8) ? 4 * j + (l16 & 3) : 2 * qt + (j >> 1), fn = (l16 < 4) ? ntk : (l16 < 8) ? ntv : ntq;
            wait_flags(ws, WS_F1 + (fm * 16 + fn) * 128, tag);
            STAMP(3);
            u32x4 tk4[16], tv4[17];
#pragma unroll
            for (int i = 0; i < 16; ++i) tk4[i] = __builtin_amdgcn_raw_buffer_load_b128(wsr, WS_K + head * 65536 + j * 16384 + i * 1024 + lane * 16, 0, 16);
            const u32x4 tq0 = __builtin_amdgcn_raw_buffer_load_b128(wsr, WS_Q + head * 65536 + qt * 8192 + j * 2048 + lane * 16, 0, 16);
            const u32x4 tq1 = __builtin_amdgcn_raw_buffer_load_b128(wsr, WS_Q + head * 65536 + qt * 8192 + j * 2048 + 1024 + lane * 16, 0, 16);
            const unsigned tk = __builtin_amdgcn_raw_buffer_load_b32(wsr, WS_KMAX + (head * 64 + 16 * j + l16) * 4, 0, 16);
#pragma unroll
            for (int i = 0; i < 17; ++i) tv4[i] = __builtin_amdgcn_raw_buffer_load_b128(wsr, WS_V + head * VHEAD + j * 17408 + i * 1024 + lane * 16, 0, 16);
            __builtin_amdgcn_sched_barrier(0);
#pragma unroll
            for (int i = 0; i < 16; ++i) *(LAS u32x4*)(lds + L_K + j * 16384 + i * 1024 + lane * 16) = tk4[i];
            *(LAS u32x4*)(lds + L_Q + j * 2048 + lane * 16) = tq0; *(LAS u32x4*)(lds + L_Q + j * 2048 + 1024 + lane * 16) = tq1;
            if (lane < 16) *(LAS unsigned*)(lds + L_KMAX + (16 * j + lane) * 4) = tk;
#pragma unroll
            for (int i = 0; i < 17; ++i) *(LAS u32x4*)(lds + L_V + j * 17408 + i * 1024 + lane * 16) = tv4[i];
        }
        STAMP(4);
        BAR();
        f32x16 acc[2][2] = {};
        G2Ctx gc;
        { const int G = lane >> 4, q = (lane & 15) >> 2, p = lane & 3; ldsp stage = lds + L_G2 + j * 4096; ldsp rd = stage + (8 * (G >> 1) + q) * 128 + 32 * (G & 1) + 8 * p;
          const int sw = ((q >> 1) & 1) * 64; gc.wr = stage + lane * 16; gc.rd0 = rd + sw; gc.rd1 = rd + (sw ^ 64); }
        const int wrow = lane >> 3, wch = (lane & 7) ^ (((wrow >> 1) & 1) << 2);
        {
            constexpr int DEPTH = 4;
            const __amdgpu_buffer_rsrc_t w2r = __builtin_amdgcn_make_buffer_rsrc((void*)w2, 0, FF * DM * 2, 0x00020000);
            const unsigned wv = (448 * j + wrow) * 1024 + ct * 128 + wch * 16, av = WS_H + (unsigned)(2 * rt * 112 + 28 * j) * 1024 + lane * 16;
            u32x4 wr[DEPTH][2], ar[DEPTH][2];
#pragma unroll
            for (int d = 0; d < DEPTH; ++d) { wr[d][0] = __builtin_amdgcn_raw_buffer_load_b128(w2r, wv, d * 16384, 0); wr[d][1] = __builtin_amdgcn_raw_buffer_load_b128(w2r, wv + 8192, d * 16384, 0); }
            wait_flags(ws, WS_F2 + ((rt >> 1) * 16 + 4 * j + (lane & 3)) * 128, tag);
            STAMP(5);
#pragma unroll
            for (int d = 0; d < DEPTH; ++d) { ar[d][0] = __builtin_amdgcn_raw_buffer_load_b128(wsr, av, d * 1024, 16); ar[d][1] = __builtin_amdgcn_raw_buffer_load_b128(wsr, av + 112 * 1024, d * 1024, 16); }
#pragma unroll 1
            for (int s0 = DEPTH; s0 < 28 + DEPTH; s0 += DEPTH) {
#pragma unroll
                for (int d = 0; d < DEPTH; ++d) {
                    bf16x8 w0, w1f;
                    g2_wfrag(gc, d & 1, wr[d][0], wr[d][1], w0, w1f);
                    wr[d][0] = __builtin_amdgcn_raw_buffer_load_b128(w2r, wv, (s0 + d) * 16384, 0); wr[d][1] = __builtin_amdgcn_raw_buffer_load_b128(w2r, wv + 8192, (s0 + d) * 16384, 0);
                    __builtin_amdgcn_sched_barrier(0);
                    g2_mma<G2SLEEP>(acc, w0, w1f, ar[d][0], ar[d][1]);
                    __builtin_amdgcn_sched_barrier(0);
                    const int sa = (s0 + d < 28 ? s0 + d : 27) * 1024;
                    ar[d][0] = __builtin_amdgcn_raw_buffer_load_b128(wsr, av, sa, 16); ar[d][1] = __builtin_amdgcn_raw_buffer_load_b128(wsr, av + 112 * 1024, sa, 16);
                    __builtin_amdgcn_sched_barrier(0);
                }
            }
        }
        STAMP(6);
        if (blockIdx.x == 0 && j == 3) {
#pragma unroll
            for (int i = 0; i < 4; ++i) wait_flags(ws, WS_F1 + (i * 64 + lane) * 128, tag);
            if (lane == 0) __hip_atomic_store((GAS u64*)(ws + WS_EPOCH), tag, RLX_AGENT);
        }
        bf16x8 wq[8][2];
        {
            const int d0 = (wrow & 3) | ((wrow & 4) << 1);
            const char* wsrc = (const char*)wo + (size_t)(128 * j + d0) * 1024 + ct * 128 + wch * 16;
            u32x4 t[8][2];
#pragma unroll
            for (int d = 0; d < 8; ++d) { t[d][0] = *(const GAS u32x4*)(wsrc + (size_t)d * 16384); t[d][1] = *(const GAS u32x4*)(wsrc + (size_t)d * 16384 + 4096); }
#pragma unroll
            for (int d = 0; d < 8; ++d) { g2_wfrag(gc, d & 1, t[d][0], t[d][1], wq[d][0], wq[d][1]); asm volatile("" : "+v"(wq[d][0]), "+v"(wq[d][1])); }
        }
        wait_flags(ws, WS_F3 + ((8 * j + (lane & 7)) * 8 + (rt >> 2)) * 128, tag);
        STAMP(7);
        {
            const unsigned ov = WS_O + (unsigned)(2 * rt * 32 + 8 * j) * 1024 + lane * 16;
#pragma unroll
            for (int hf = 0; hf < 2; ++hf) {
                u32x4 t[4][2];
#pragma unroll
                for (int d = 0; d < 4; ++d) { t[d][0] = __builtin_amdgcn_raw_buffer_load_b128(wsr, ov, (4 * hf + d) * 1024, 16); t[d][1] = __builtin_amdgcn_raw_buffer_load_b128(wsr, ov + 32768, (4 * hf + d) * 1024, 16); }
#pragma unroll
                for (int d = 0; d < 4; ++d) g2_mma<0>(acc, wq[4 * hf + d][0], wq[4 * hf + d][1], t[d][0], t[d][1]);
            }
        }
        STAMP(8);
        BAR();
#pragma unroll
        for (int a = 0; a < 2; ++a)
#pragma unroll
            for (int b = 0; b < 2; ++b)
#pragma unroll
                for (int r4 = 0; r4 < 4; ++r4)
                    *(LAS f32x4*)(lds + j * (64 * RED_PITCH) + (32 * b + (lane & 31)) * RED_PITCH + (32 * a + 8 * r4 + 4 * (lane >> 5)) * 4) =
                        f32x4{acc[a][b][4 * r4], acc[a][b][4 * r4 + 1], acc[a][b][4 * r4 + 2], acc[a][b][4 * r4 + 3]};
    }
    BAR();
    if (w < 8) {
        ldsp rp = lds + (threadIdx.x >> 3) * RED_PITCH + (threadIdx.x & 7) * 32;
        f32x4 s0 = *(const LAS f32x4*)rp, s1 = *(const LAS f32x4*)(rp + 16);
#pragma unroll
        for (int k = 1; k < 4; ++k) { s0 += *(const LAS f32x4*)(rp + k * 64 * RED_PITCH); s1 += *(const LAS f32x4*)(rp + k * 64 * RED_PITCH + 16); }
        u32x4 o;
        o[0] = pk2(s0[0] + bf_lo(xres[0]) + bf_lo(bres[0]), s0[1] + bf_hi(xres[0]) + bf_hi(bres[0]));
        o[1] = pk2(s0[2] + bf_lo(xres[1]) + bf_lo(bres[1]), s0[3] + bf_hi(xres[1]) + bf_hi(bres[1]));
        o[2] = pk2(s1[0] + bf_lo(xres[2]) + bf_lo(bres[2]), s1[1] + bf_hi(xres[2]) + bf_hi(bres[2]));
        o[3] = pk2(s1[2] + bf_lo(xres[3]) + bf_lo(bres[3]), s1[3] + bf_hi(xres[3]) + bf_hi(bres[3]));
        *(GAS u32x4*)((char*)out + (orow * DM + ocol) * 2) = o;
    }
    STAMP(9); CSTAMP(11);
}

extern "C" void kernel_launch(void* const* d_in, const int* in_sizes, int n_in, void* d_out, int out_size, void* d_ws, size_t ws_size, hipStream_t stream) {
    (void)in_sizes; (void)n_in; (void)out_size; (void)ws_size;
    (void)hipFuncSetAttribute((const void*)fused_block_kernel, hipFuncAttributeMaxDynamicSharedMemorySize, LDS_BYTES);
    fused_block_kernel<<<256, 768, LDS_BYTES, stream>>>((const __hip_bfloat16*)d_in[0], (const __hip_bfloat16*)d_in[1], (const __hip_bfloat16*)d_in[2],
                                                       (const __hip_bfloat16*)d_in[4], (const __hip_bfloat16*)d_in[5], (const __hip_bfloat16*)d_in[6],
                                                       (const __hip_bfloat16*)d_in[7], (__hip_bfloat16*)d_out, (char*)d_ws);
}
```

```cpp
#include <hip/hip_runtime.h>
#include <hip/hip_bf16.h>
#include <stdint.h>

typedef __bf16 bf16x2 __attribute__((ext_vector_type(2)));
typedef __bf16 bf16x8 __attribute__((ext_vector_type(8)));
typedef __bf16 bf16x16 __attribute__((ext_vector_type(16)));
typedef float f32x2 __attribute__((ext_vector_type(2)));
typedef float f32x4 __attribute__((ext_vector_type(4)));
typedef float f32x8 __attribute__((ext_vector_type(8)));
typedef float f32x16 __attribute__((ext_vector_type(16)));
typedef unsigned u32x2 __attribute__((ext_vector_type(2)));
typedef unsigned u32x4 __attribute__((ext_vector_type(4)));
typedef short s16x4 __attribute__((ext_vector_type(4)));
typedef short s16x8 __attribute__((ext_vector_type(8)));
typedef unsigned long long u64;
#define LAS __attribute__((address_space(3)))
#define GAS __attribute__((address_space(1)))
typedef LAS char* ldsp;

constexpr int SEQ = 2048, DM = 512, NH = 32, FF = 1792;
constexpr unsigned WS_EPOCH = 0, WS_F1 = 4096, WS_F2 = WS_F1 + 32768, WS_F3 = WS_F2 + 32768, WS_KMAX = WS_F3 + 32768;
constexpr unsigned WS_RS = WS_KMAX + 8192;
constexpr unsigned WS_Q = 1u << 20, WS_K = 3u << 20, WS_V = 5u << 20, WS_H = 8u << 20, WS_O = 16u << 20, WS_BYTES = 18u << 20;
constexpr unsigned VT = 544;
constexpr int SLOT = 30720, OFF_B = 16384, NSLOT = 5;
constexpr int L_ROWST = NSLOT * SLOT, L_COLS = L_ROWST + 512, L_BIAS = L_COLS + 1792;
constexpr int L_K = 0, L_V = 65536, L_Q = 135168, L_KMAX = 143360, L_G2 = 143616;
constexpr int L_RED2 = 69632;
constexpr int L_MISC = 163840 - 64, LDS_BYTES = 163840;
constexpr int RED_PITCH = 272;
constexpr float QSCALE = 0.25f * 1.4426950408889634f;
constexpr float SAFE_BOUND = 50.0f;
constexpr unsigned SPIN_CAP = 1u << 18;

#define F1_AFTER 0
#define MFMA16(a, b, c) __builtin_amdgcn_mfma_f32_16x16x32_bf16(a, b, c, 0, 0, 0)
#define MFMA32(a, b, c) __builtin_amdgcn_mfma_f32_32x32x16_bf16(a, b, c, 0, 0, 0)
#define SMF(a, b, c, i) __builtin_amdgcn_smfmac_f32_32x32x32_bf16(a, b, c, i, 0, 0)
#define BAR() asm volatile("s_waitcnt lgkmcnt(0)\n\ts_barrier" ::: "memory")
#define WAITV(n) asm volatile("s_waitcnt vmcnt(" #n ")" ::: "memory")
#define RLX_AGENT __ATOMIC_RELAXED, __HIP_MEMORY_SCOPE_AGENT

#define SSTAMP(i) do {} while (0)
#define CSTAMP(i) do {} while (0)
#define STAMP(i) do {} while (0)

__device__ __forceinline__ bf16x8 ld_b128(ldsp p) { return *(const LAS bf16x8*)p; }
__device__ __forceinline__ bf16x8 ld_tr2(ldsp p0, ldsp p1) {
    s16x4 lo = __builtin_amdgcn_ds_read_tr16_b64_v4i16((LAS s16x4*)p0);
    s16x4 hi = __builtin_amdgcn_ds_read_tr16_b64_v4i16((LAS s16x4*)p1);
    s16x8 t = __builtin_shufflevector(lo, hi, 0, 1, 2, 3, 4, 5, 6, 7);
    return __builtin_bit_cast(bf16x8, t);
}
__device__ __forceinline__ unsigned pk2(float a, float b) { f32x2 v = {a, b}; return __builtin_bit_cast(unsigned, __builtin_convertvector(v, bf16x2)); }
__device__ __forceinline__ float bf_lo(unsigned u) { return __uint_as_float(u << 16); }
__device__ __forceinline__ float bf_hi(unsigned u) { return __uint_as_float(u & 0xffff0000u); }
__device__ __forceinline__ float rbf(float a) { return (float)(__bf16)a; }
__device__ __forceinline__ float dot2(unsigned a, unsigned b, float c) { return __builtin_amdgcn_fdot2_f32_bf16(__builtin_bit_cast(bf16x2, a), __builtin_bit_cast(bf16x2, b), c, false); }
__device__ __forceinline__ float sum_x16(float v) { auto r = __builtin_amdgcn_permlane16_swap(__float_as_uint(v), __float_as_uint(v), false, false); return __uint_as_float(r[0]) + __uint_as_float(r[1]); }
__device__ __forceinline__ float sum_x32(float v) { auto r = __builtin_amdgcn_permlane32_swap(__float_as_uint(v), __float_as_uint(v), false, false); return __uint_as_float(r[0]) + __uint_as_float(r[1]); }
__device__ __forceinline__ float max_x16(float v) { auto r = __builtin_amdgcn_permlane16_swap(__float_as_uint(v), __float_as_uint(v), false, false); return fmaxf(__uint_as_float(r[0]), __uint_as_float(r[1])); }
__device__ __forceinline__ float max_x32(float v) { auto r = __builtin_amdgcn_permlane32_swap(__float_as_uint(v), __float_as_uint(v), false, false); return fmaxf(__uint_as_float(r[0]), __uint_as_float(r[1])); }
template <int N> __device__ __forceinline__ float ror16(float v) { return __uint_as_float(__builtin_amdgcn_update_dpp(0u, __float_as_uint(v), 0x120 + N, 0xf, 0xf, false)); }
template <int N> __device__ __forceinline__ unsigned umax_ror(unsigned v) { const unsigned o = __builtin_amdgcn_update_dpp(0u, v, 0x120 + N, 0xf, 0xf, false); return v > o ? v : o; }
__device__ __forceinline__ unsigned umax_32lanes(unsigned v) {
    v = umax_ror<1>(umax_ror<2>(umax_ror<4>(umax_ror<8>(v))));
    const auto r = __builtin_amdgcn_permlane16_swap(v, v, false, false); return r[0] > r[1] ? r[0] : r[1];
}

__device__ __forceinline__ int lane_id() { int l; asm volatile("v_mbcnt_lo_u32_b32 %0, -1, 0\n\tv_mbcnt_hi_u32_b32 %0, -1, %0" : "=v"(l)); return l; }
__device__ __forceinline__ void lds_count(ldsp lds, int ctr, int lane) { if (lane == 0) __hip_atomic_fetch_add((LAS unsigned*)(lds + L_MISC + 16 + 4 * ctr), 1u, __ATOMIC_RELAXED, __HIP_MEMORY_SCOPE_WORKGROUP); }
__device__ __forceinline__ void lds_wait(ldsp lds, int ctr, unsigned n) {
#pragma unroll 1
    for (unsigned spins = 0; spins < (SPIN_CAP << 4); ++spins) {
        if (__builtin_amdgcn_readfirstlane(*(const volatile LAS unsigned*)(lds + L_MISC + 16 + 4 * ctr)) >= n) break;
        __builtin_amdgcn_s_sleep(1);
    }
    asm volatile("" ::: "memory");
}
__device__ __forceinline__ void glds16(const char* g, ldsp l) { __builtin_amdgcn_global_load_lds((const GAS unsigned*)g, (LAS unsigned*)l, 16, 0, 0); }

__device__ __forceinline__ void wait_flags(const char* ws, unsigned off, u64 tag) {
    const GAS u64* p = (const GAS u64*)(ws + off);
#pragma unroll 1
    for (unsigned spins = 0; spins < SPIN_CAP; ++spins) {
        const u64 v = __hip_atomic_load(p, RLX_AGENT);
        if (__all(v == tag)) break;
        __builtin_amdgcn_s_sleep(1);
    }
    asm volatile("" ::: "memory");
}
__device__ __forceinline__ void arrive_publish(ldsp lds, int ctr, int n, char* ws, unsigned flag_off, int lane) {
    WAITV(0);
    if (lane == 0) {
        const unsigned old = __hip_atomic_fetch_add((LAS unsigned*)(lds + L_MISC + 16 + 4 * ctr), 1u, __ATOMIC_RELAXED, __HIP_MEMORY_SCOPE_WORKGROUP);
        if (old == (unsigned)(n - 1)) {
            const u64 tag = *(const volatile LAS u64*)(lds + L_MISC);
            __hip_atomic_store((GAS u64*)(ws + flag_off), tag, RLX_AGENT);
        }
    }
}

__device__ __forceinline__ float gelu_tanh(float x) {
    constexpr float C1 = 2.0f * 0.7978845608028654f * 1.4426950408889634f, C2 = C1 * 0.044715f;
    const float t = __builtin_fmaf(x * x, -C2, -C1);
    const float e = __builtin_amdgcn_exp2f(x * t);
    return x * __builtin_amdgcn_rcpf(1.0f + e);
}

__device__ __forceinline__ u32x4 widen(u32x2 d0, u32x2 d1) {
    const auto a = __builtin_amdgcn_permlane16_swap(d0[0], d1[0], false, false), b = __builtin_amdgcn_permlane16_swap(d0[1], d1[1], false, false);
    return u32x4{a[0], b[0], a[1], b[1]};
}
__device__ __forceinline__ float sum_row16(float v) { v += ror16<8>(v); v += ror16<4>(v); v += ror16<2>(v); return v + ror16<1>(v); }
__device__ __forceinline__ u64 call_tag(const char* ws) {
    return __hip_atomic_load((const GAS u64*)(ws + WS_EPOCH), RLX_AGENT) * 6364136223846793005ull + 1442695040888963407ull;
}
__device__ __forceinline__ void g1_rowstat(char* ws, const char* x, int row, int lane) {
    const u32x4 xr = *(const GAS u32x4*)(x + (size_t)row * 1024 + lane * 16);
    const u64 tag = call_tag(ws);
    float e[8], s = 0.f;
#pragma unroll
    for (int i = 0; i < 4; ++i) { e[2 * i] = bf_lo(xr[i]); e[2 * i + 1] = bf_hi(xr[i]); s += e[2 * i] + e[2 * i + 1]; }
    const float mu = sum_x32(sum_x16(sum_row16(s))) * (1.0f / DM);
    float v = 0.f;
#pragma unroll
    for (int i = 0; i < 8; ++i) v = __builtin_fmaf(e[i] - mu, e[i] - mu, v);
    v = sum_x32(sum_x16(sum_row16(v))) * (1.0f / DM);
    if (lane == 0) __hip_atomic_store((GAS u64*)(ws + WS_RS) + row, (tag << 32) | pk2(mu, __builtin_amdgcn_sqrtf(rbf(rbf(v) + 1e-5f))), RLX_AGENT);
}

#define G1_QKV0 \
    "v_add_u32 v76, 61440, %9\n" \
    "v_add_u32 v77, 61440, %10\n" \
    "v_add_u32 v78, 61440, %11\n" \
    "v_add_u32 v79, 61440, %12\n" \
    "v_add_u32 v80, 122880, %9\n" \
    "v_add_u32 v81, 122880, %10\n" \
    "v_add_u32 v82, 122880, %11\n" \
    "v_add_u32 v83, 122880, %12\n" \
    "v_mov_b32 v84, 0x3f803f80\n" \
    "v_mov_b32 v85, 0x3f803f80\n" \
    "v_mov_b32 v86, 0x3f803f80\n" \
    "v_mov_b32 v87, 0x3f803f80\n" \
    "s_barrier\n" \
    "ds_read_b128 v[88:91], %9 offset:0\n" \
    "ds_read_b64_tr_b16 v[96:97], %11 offset:0\n" \
    "ds_read_b64_tr_b16 v[98:99], %11 offset:768\n" \
    "ds_read_b128 v[92:95], %9 offset:2048\n" \
    "ds_read_b64_tr_b16 v[100:101], %12 offset:0\n" \
    "ds_read_b64_tr_b16 v[102:103], %12 offset:768\n" \
    "ds_read_b64_tr_b16 v[104:105], %11 offset:64\n" \
    "ds_read_b64_tr_b16 v[106:107], %11 offset:832\n" \
    "ds_read_b128 v[108:111], %10 offset:0\n" \
    "ds_read_b64_tr_b16 v[116:117], %11 offset:6144\n" \
    "ds_read_b64_tr_b16 v[118:119], %11 offset:6912\n" \
    "ds_read_b128 v[112:115], %10 offset:2048\n" \
    "ds_read_b64_tr_b16 v[120:121], %12 offset:6144\n" \
    "ds_read_b64_tr_b16 v[122:123], %12 offset:6912\n" \
    "ds_read_b64_tr_b16 v[124:125], %11 offset:6208\n" \
    "ds_read_b64_tr_b16 v[126:127], %11 offset:6976\n" \
    "s_waitcnt lgkmcnt(0)\n" \
    "s_barrier\n" \
    "v_mfma_f32_16x16x32_bf16 %0, v[96:99], v[88:91], 0\n" \
    "ds_read_b128 v[128:131], %9 offset:30720\n" \
    "ds_read_b64_tr_b16 v[136:137], %11 offset:30720\n" \
    "v_mfma_f32_16x16x32_bf16 %1, v[96:99], v[92:95], 0\n" \
    "ds_read_b64_tr_b16 v[138:139], %11 offset:31488\n" \
    "ds_read_b128 v[132:135], %9 offset:32768\n" \
    "v_mfma_f32_16x16x32_bf16 %2, v[100:103], v[88:91], 0\n" \
    "ds_read_b64_tr_b16 v[140:141], %12 offset:30720\n" \
    "ds_read_b64_tr_b16 v[142:143], %12 offset:31488\n" \
    "v_mfma_f32_16x16x32_bf16 %3, v[100:103], v[92:95], 0\n" \
    "ds_read_b64_tr_b16 v[144:145], %11 offset:30784\n" \
    "ds_read_b64_tr_b16 v[146:147], %11 offset:31552\n" \
    "v_mfma_f32_16x16x32_bf16 %4, v[104:107], v[88:91], 0\n" \
    "ds_read_b128 v[148:151], %10 offset:30720\n" \
    "v_mfma_f32_16x16x32_bf16 %5, v[104:107], v[92:95], 0\n" \
    "ds_read_b64_tr_b16 v[156:157], %11 offset:36864\n" \
    "v_mfma_f32_16x16x32_bf16 %0, v[116:119], v[108:111], %0\n" \
    "ds_read_b64_tr_b16 v[158:159], %11 offset:37632\n" \
    "v_mfma_f32_16x16x32_bf16 %1, v[116:119], v[112:115], %1\n" \
    "ds_read_b128 v[152:155], %10 offset:32768\n" \
    "v_mfma_f32_16x16x32_bf16 %2, v[120:123], v[108:111], %2\n" \
    "ds_read_b64_tr_b16 v[160:161], %12 offset:36864\n" \
    "v_mfma_f32_16x16x32_bf16 %3, v[120:123], v[112:115], %3\n" \
    "ds_read_b64_tr_b16 v[162:163], %12 offset:37632\n" \
    "v_mfma_f32_16x16x32_bf16 %4, v[124:127], v[108:111], %4\n" \
    "ds_read_b64_tr_b16 v[164:165], %11 offset:36928\n" \
    "v_mfma_f32_16x16x32_bf16 %5, v[124:127], v[112:115], %5\n" \
    "ds_read_b64_tr_b16 v[166:167], %11 offset:37696\n" \
    "s_cmp_lg_u32 %13, 0\n" \
    "s_cbranch_scc1 G1C00_%=\n" \
    "v_mfma_f32_16x16x32_bf16 %6, v[96:99], v[84:87], %6\n" \
    "v_mfma_f32_16x16x32_bf16 %7, v[100:103], v[84:87], %7\n" \
    "v_mfma_f32_16x16x32_bf16 %8, v[104:107], v[84:87], %8\n" \
    "G1C00_%=:\n" \
    "s_cmp_lg_u32 %13, 1\n" \
    "s_cbranch_scc1 G1C01_%=\n" \
    "v_mfma_f32_16x16x32_bf16 %6, v[116:119], v[84:87], %6\n" \
    "v_mfma_f32_16x16x32_bf16 %7, v[120:123], v[84:87], %7\n" \
    "v_mfma_f32_16x16x32_bf16 %8, v[124:127], v[84:87], %8\n" \
    "G1C01_%=:\n" \
    "s_waitcnt lgkmcnt(0)\n" \
    "s_barrier\n" \
    "v_mfma_f32_16x16x32_bf16 %0, v[136:139], v[128:131], %0\n" \
    "ds_read_b128 v[88:91], v76 offset:0\n" \
    "ds_read_b64_tr_b16 v[96:97], v78 offset:0\n" \
    "v_mfma_f32_16x16x32_bf16 %1, v[136:139], v[132:135], %1\n" \
    "ds_read_b64_tr_b16 v[98:99], v78 offset:768\n" \
    "ds_read_b128 v[92:95], v76 offset:2048\n" \
    "v_mfma_f32_16x16x32_bf16 %2, v[140:143], v[128:131], %2\n" \
    "ds_read_b64_tr_b16 v[100:101], v79 offset:0\n" \
    "ds_read_b64_tr_b16 v[102:103], v79 offset:768\n" \
    "v_mfma_f32_16x16x32_bf16 %3, v[140:143], v[132:135], %3\n" \
    "ds_read_b64_tr_b16 v[104:105], v78 offset:64\n" \
    "ds_read_b64_tr_b16 v[106:107], v78 offset:832\n" \
    "v_mfma_f32_16x16x32_bf16 %4, v[144:147], v[128:131], %4\n" \
    "ds_read_b128 v[108:111], v77 offset:0\n" \
    "v_mfma_f32_16x16x32_bf16 %5, v[144:147], v[132:135], %5\n" \
    "ds_read_b64_tr_b16 v[116:117], v78 offset:6144\n" \
    "v_mfma_f32_16x16x32_bf16 %0, v[156:159], v[148:151], %0\n" \
    "ds_read_b64_tr_b16 v[118:119], v78 offset:6912\n" \
    "v_mfma_f32_16x16x32_bf16 %1, v[156:159], v[152:155], %1\n" \
    "ds_read_b128 v[112:115], v77 offset:2048\n" \
    "v_mfma_f32_16x16x32_bf16 %2, v[160:163], v[148:151], %2\n" \
    "ds_read_b64_tr_b16 v[120:121], v79 offset:6144\n" \
    "v_mfma_f32_16x16x32_bf16 %3, v[160:163], v[152:155], %3\n" \
    "ds_read_b64_tr_b16 v[122:123], v79 offset:6912\n" \
    "v_mfma_f32_16x16x32_bf16 %4, v[164:167], v[148:151], %4\n" \
    "ds_read_b64_tr_b16 v[124:125], v78 offset:6208\n" \
    "v_mfma_f32_16x16x32_bf16 %5, v[164:167], v[152:155], %5\n" \
    "ds_read_b64_tr_b16 v[126:127], v78 offset:6976\n" \
    "s_cmp_lg_u32 %13, 2\n" \
    "s_cbranch_scc1 G1C10_%=\n" \
    "v_mfma_f32_16x16x32_bf16 %6, v[136:139], v[84:87], %6\n" \
    "v_mfma_f32_16x16x32_bf16 %7, v[140:143], v[84:87], %7\n" \
    "v_mfma_f32_16x16x32_bf16 %8, v[144:147], v[84:87], %8\n" \
    "G1C10_%=:\n" \
    "s_cmp_lg_u32 %13, 3\n" \
    "s_cbranch_scc1 G1C11_%=\n" \
    "v_mfma_f32_16x16x32_bf16 %6, v[156:159], v[84:87], %6\n" \
    "v_mfma_f32_16x16x32_bf16 %7, v[160:163], v[84:87], %7\n" \
    "v_mfma_f32_16x16x32_bf16 %8, v[164:167], v[84:87], %8\n" \
    "G1C11_%=:\n" \
    "s_waitcnt lgkmcnt(0)\n" \
    "s_barrier\n" \
    "v_mfma_f32_16x16x32_bf16 %0, v[96:99], v[88:91], %0\n" \
    "ds_read_b128 v[128:131], v76 offset:30720\n" \
    "ds_read_b64_tr_b16 v[136:137], v78 offset:30720\n" \
    "v_mfma_f32_16x16x32_bf16 %1, v[96:99], v[92:95], %1\n" \
    "ds_read_b64_tr_b16 v[138:139], v78 offset:31488\n" \
    "ds_read_b128 v[132:135], v76 offset:32768\n" \
    "v_mfma_f32_16x16x32_bf16 %2, v[100:103], v[88:91], %2\n" \
    "ds_read_b64_tr_b16 v[140:141], v79 offset:30720\n" \
    "ds_read_b64_tr_b16 v[142:143], v79 offset:31488\n" \
    "v_mfma_f32_16x16x32_bf16 %3, v[100:103], v[92:95], %3\n" \
    "ds_read_b64_tr_b16 v[144:145], v78 offset:30784\n" \
    "ds_read_b64_tr_b16 v[146:147], v78 offset:31552\n" \
    "v_mfma_f32_16x16x32_bf16 %4, v[104:107], v[88:91], %4\n" \
    "ds_read_b128 v[148:151], v77 offset:30720\n" \
    "v_mfma_f32_16x16x32_bf16 %5, v[104:107], v[92:95], %5\n" \
    "ds_read_b64_tr_b16 v[156:157], v78 offset:36864\n" \
    "v_mfma_f32_16x16x32_bf16 %0, v[116:119], v[108:111], %0\n" \
    "ds_read_b64_tr_b16 v[158:159], v78 offset:37632\n" \
    "v_mfma_f32_16x16x32_bf16 %1, v[116:119], v[112:115], %1\n" \
    "ds_read_b128 v[152:155], v77 offset:32768\n" \
    "v_mfma_f32_16x16x32_bf16 %2, v[120:123], v[108:111], %2\n" \
    "ds_read_b64_tr_b16 v[160:161], v79 offset:36864\n" \
    "v_mfma_f32_16x16x32_bf16 %3, v[120:123], v[112:115], %3\n" \
    "ds_read_b64_tr_b16 v[162:163], v79 offset:37632\n" \
    "v_mfma_f32_16x16x32_bf16 %4, v[124:127], v[108:111], %4\n" \
    "ds_read_b64_tr_b16 v[164:165], v78 offset:36928\n" \
    "v_mfma_f32_16x16x32_bf16 %5, v[124:127], v[112:115], %5\n" \
    "ds_read_b64_tr_b16 v[166:167], v78 offset:37696\n" \
    "s_cmp_lg_u32 %13, 0\n" \
    "s_cbranch_scc1 G1C20_%=\n" \
    "v_mfma_f32_16x16x32_bf16 %6, v[96:99], v[84:87], %6\n" \
    "v_mfma_f32_16x16x32_bf16 %7, v[100:103], v[84:87], %7\n" \
    "v_mfma_f32_16x16x32_bf16 %8, v[104:107], v[84:87], %8\n" \
    "G1C20_%=:\n" \
    "s_cmp_lg_u32 %13, 1\n" \
    "s_cbranch_scc1 G1C21_%=\n" \
    "v_mfma_f32_16x16x32_bf16 %6, v[116:119], v[84:87], %6\n" \
    "v_mfma_f32_16x16x32_bf16 %7, v[120:123], v[84:87], %7\n" \
    "v_mfma_f32_16x16x32_bf16 %8, v[124:127], v[84:87], %8\n" \
    "G1C21_%=:\n" \
    "s_waitcnt lgkmcnt(0)\n" \
    "s_barrier\n" \
    "v_mfma_f32_16x16x32_bf16 %0, v[136:139], v[128:131], %0\n" \
    "ds_read_b128 v[88:91], v80 offset:0\n" \
    "ds_read_b64_tr_b16 v[96:97], v82 offset:0\n" \
    "v_mfma_f32_16x16x32_bf16 %1, v[136:139], v[132:135], %1\n" \
    "ds_read_b64_tr_b16 v[98:99], v82 offset:768\n" \
    "ds_read_b128 v[92:95], v80 offset:2048\n" \
    "v_mfma_f32_16x16x32_bf16 %2, v[140:143], v[128:131], %2\n" \
    "ds_read_b64_tr_b16 v[100:101], v83 offset:0\n" \
    "ds_read_b64_tr_b16 v[102:103], v83 offset:768\n" \
    "v_mfma_f32_16x16x32_bf16 %3, v[140:143], v[132:135], %3\n" \
    "ds_read_b64_tr_b16 v[104:105], v82 offset:64\n" \
    "ds_read_b64_tr_b16 v[106:107], v82 offset:832\n" \
    "v_mfma_f32_16x16x32_bf16 %4, v[144:147], v[128:131], %4\n" \
    "ds_read_b128 v[108:111], v81 offset:0\n" \
    "v_mfma_f32_16x16x32_bf16 %5, v[144:147], v[132:135], %5\n" \
    "ds_read_b64_tr_b16 v[116:117], v82 offset:6144\n" \
    "v_mfma_f32_16x16x32_bf16 %0, v[156:159], v[148:151], %0\n" \
    "ds_read_b64_tr_b16 v[118:119], v82 offset:6912\n" \
    "v_mfma_f32_16x16x32_bf16 %1, v[156:159], v[152:155], %1\n" \
    "ds_read_b128 v[112:115], v81 offset:2048\n" \
    "v_mfma_f32_16x16x32_bf16 %2, v[160:163], v[148:151], %2\n" \
    "ds_read_b64_tr_b16 v[120:121], v83 offset:6144\n" \
    "v_mfma_f32_16x16x32_bf16 %3, v[160:163], v[152:155], %3\n" \
    "ds_read_b64_tr_b16 v[122:123], v83 offset:6912\n" \
    "v_mfma_f32_16x16x32_bf16 %4, v[164:167], v[148:151], %4\n" \
    "ds_read_b64_tr_b16 v[124:125], v82 offset:6208\n" \
    "v_mfma_f32_16x16x32_bf16 %5, v[164:167], v[152:155], %5\n" \
    "ds_read_b64_tr_b16 v[126:127], v82 offset:6976\n" \
    "s_cmp_lg_u32 %13, 2\n" \
    "s_cbranch_scc1 G1C30_%=\n" \
    "v_mfma_f32_16x16x32_bf16 %6, v[136:139], v[84:87], %6\n" \
    "v_mfma_f32_16x16x32_bf16 %7, v[140:143], v[84:87], %7\n" \
    "v_mfma_f32_16x16x32_bf16 %8, v[144:147], v[84:87], %8\n" \
    "G1C30_%=:\n" \
    "s_cmp_lg_u32 %13, 3\n" \
    "s_cbranch_scc1 G1C31_%=\n" \
    "v_mfma_f32_16x16x32_bf16 %6, v[156:159], v[84:87], %6\n" \
    "v_mfma_f32_16x16x32_bf16 %7, v[160:163], v[84:87], %7\n" \
    "v_mfma_f32_16x16x32_bf16 %8, v[164:167], v[84:87], %8\n" \
    "G1C31_%=:\n" \
    "s_waitcnt lgkmcnt(0)\n" \
    "s_barrier\n" \
    "v_mfma_f32_16x16x32_bf16 %0, v[96:99], v[88:91], %0\n" \
    "ds_read_b128 v[128:131], %9 offset:0\n" \
    "ds_read_b64_tr_b16 v[136:137], %11 offset:0\n" \
    "v_mfma_f32_16x16x32_bf16 %1, v[96:99], v[92:95], %1\n" \
    "ds_read_b64_tr_b16 v[138:139], %11 offset:768\n" \
    "ds_read_b128 v[132:135], %9 offset:2048\n" \
    "v_mfma_f32_16x16x32_bf16 %2, v[100:103], v[88:91], %2\n" \
    "ds_read_b64_tr_b16 v[140:141], %12 offset:0\n" \
    "ds_read_b64_tr_b16 v[142:143], %12 offset:768\n" \
    "v_mfma_f32_16x16x32_bf16 %3, v[100:103], v[92:95], %3\n" \
    "ds_read_b64_tr_b16 v[144:145], %11 offset:64\n" \
    "ds_read_b64_tr_b16 v[146:147], %11 offset:832\n" \
    "v_mfma_f32_16x16x32_bf16 %4, v[104:107], v[88:91], %4\n" \
    "ds_read_b128 v[148:151], %10 offset:0\n" \
    "v_mfma_f32_16x16x32_bf16 %5, v[104:107], v[92:95], %5\n" \
    "ds_read_b64_tr_b16 v[156:157], %11 offset:6144\n" \
    "v_mfma_f32_16x16x32_bf16 %0, v[116:119], v[108:111], %0\n" \
    "ds_read_b64_tr_b16 v[158:159], %11 offset:6912\n" \
    "v_mfma_f32_16x16x32_bf16 %1, v[116:119], v[112:115], %1\n" \
    "ds_read_b128 v[152:155], %10 offset:2048\n" \
    "v_mfma_f32_16x16x32_bf16 %2, v[120:123], v[108:111], %2\n" \
    "ds_read_b64_tr_b16 v[160:161], %12 offset:6144\n" \
    "v_mfma_f32_16x16x32_bf16 %3, v[120:123], v[112:115], %3\n" \
    "ds_read_b64_tr_b16 v[162:163], %12 offset:6912\n" \
    "v_mfma_f32_16x16x32_bf16 %4, v[124:127], v[108:111], %4\n" \
    "ds_read_b64_tr_b16 v[164:165], %11 offset:6208\n" \
    "v_mfma_f32_16x16x32_bf16 %5, v[124:127], v[112:115], %5\n" \
    "ds_read_b64_tr_b16 v[166:167], %11 offset:6976\n" \
    "s_cmp_lg_u32 %13, 0\n" \
    "s_cbranch_scc1 G1C40_%=\n" \
    "v_mfma_f32_16x16x32_bf16 %6, v[96:99], v[84:87], %6\n" \
    "v_mfma_f32_16x16x32_bf16 %7, v[100:103], v[84:87], %7\n" \
    "v_mfma_f32_16x16x32_bf16 %8, v[104:107], v[84:87], %8\n" \
    "G1C40_%=:\n" \
    "s_cmp_lg_u32 %13, 1\n" \
    "s_cbranch_scc1 G1C41_%=\n" \
    "v_mfma_f32_16x16x32_bf16 %6, v[116:119], v[84:87], %6\n" \
    "v_mfma_f32_16x16x32_bf16 %7, v[120:123], v[84:87], %7\n" \
    "v_mfma_f32_16x16x32_bf16 %8, v[124:127], v[84:87], %8\n" \
    "G1C41_%=:\n" \
    "s_waitcnt lgkmcnt(0)\n" \
    "s_barrier\n" \
    "v_mfma_f32_16x16x32_bf16 %0, v[136:139], v[128:131], %0\n" \
    "ds_read_b128 v[88:91], %9 offset:30720\n" \
    "ds_read_b64_tr_b16 v[96:97], %11 offset:30720\n" \
    "v_mfma_f32_16x16x32_bf16 %1, v[136:139], v[132:135], %1\n" \
    "ds_read_b64_tr_b16 v[98:99], %11 offset:31488\n" \
    "ds_read_b128 v[92:95], %9 offset:32768\n" \
    "v_mfma_f32_16x16x32_bf16 %2, v[140:143], v[128:131], %2\n" \
    "ds_read_b64_tr_b16 v[100:101], %12 offset:30720\n" \
    "ds_read_b64_tr_b16 v[102:103], %12 offset:31488\n" \
    "v_mfma_f32_16x16x32_bf16 %3, v[140:143], v[132:135], %3\n" \
    "ds_read_b64_tr_b16 v[104:105], %11 offset:30784\n" \
    "ds_read_b64_tr_b16 v[106:107], %11 offset:31552\n" \
    "v_mfma_f32_16x16x32_bf16 %4, v[144:147], v[128:131], %4\n" \
    "ds_read_b128 v[108:111], %10 offset:30720\n" \
    "v_mfma_f32_16x16x32_bf16 %5, v[144:147], v[132:135], %5\n" \
    "ds_read_b64_tr_b16 v[116:117], %11 offset:36864\n" \
    "v_mfma_f32_16x16x32_bf16 %0, v[156:159], v[148:151], %0\n" \
    "ds_read_b64_tr_b16 v[118:119], %11 offset:37632\n" \
    "v_mfma_f32_16x16x32_bf16 %1, v[156:159], v[152:155], %1\n" \
    "ds_read_b128 v[112:115], %10 offset:32768\n" \
    "v_mfma_f32_16x16x32_bf16 %2, v[160:163], v[148:151], %2\n" \
    "ds_read_b64_tr_b16 v[120:121], %12 offset:36864\n" \
    "v_mfma_f32_16x16x32_bf16 %3, v[160:163], v[152:155], %3\n" \
    "ds_read_b64_tr_b16 v[122:123], %12 offset:37632\n" \
    "v_mfma_f32_16x16x32_bf16 %4, v[164:167], v[148:151], %4\n" \
    "ds_read_b64_tr_b16 v[124:125], %11 offset:36928\n" \
    "v_mfma_f32_16x16x32_bf16 %5, v[164:167], v[152:155], %5\n" \
    "ds_read_b64_tr_b16 v[126:127], %11 offset:37696\n" \
    "s_cmp_lg_u32 %13, 2\n" \
    "s_cbranch_scc1 G1C50_%=\n" \
    "v_mfma_f32_16x16x32_bf16 %6, v[136:139], v[84:87], %6\n" \
    "v_mfma_f32_16x16x32_bf16 %7, v[140:143], v[84:87], %7\n" \
    "v_mfma_f32_16x16x32_bf16 %8, v[144:147], v[84:87], %8\n" \
    "G1C50_%=:\n" \
    "s_cmp_lg_u32 %13, 3\n" \
    "s_cbranch_scc1 G1C51_%=\n" \
    "v_mfma_f32_16x16x32_bf16 %6, v[156:159], v[84:87], %6\n" \
    "v_mfma_f32_16x16x32_bf16 %7, v[160:163], v[84:87], %7\n" \
    "v_mfma_f32_16x16x32_bf16 %8, v[164:167], v[84:87], %8\n" \
    "G1C51_%=:\n" \
    "s_waitcnt lgkmcnt(0)\n" \
    "s_barrier\n" \
    "v_mfma_f32_16x16x32_bf16 %0, v[96:99], v[88:91], %0\n" \
    "ds_read_b128 v[128:131], v76 offset:0\n" \
    "ds_read_b64_tr_b16 v[136:137], v78 offset:0\n" \
    "v_mfma_f32_16x16x32_bf16 %1, v[96:99], v[92:95], %1\n" \
    "ds_read_b64_tr_b16 v[138:139], v78 offset:768\n" \
    "ds_read_b128 v[132:135], v76 offset:2048\n" \
    "v_mfma_f32_16x16x32_bf16 %2, v[100:103], v[88:91], %2\n" \
    "ds_read_b64_tr_b16 v[140:141], v79 offset:0\n" \
    "ds_read_b64_tr_b16 v[142:143], v79 offset:768\n" \
    "v_mfma_f32_16x16x32_bf16 %3, v[100:103], v[92:95], %3\n" \
    "ds_read_b64_tr_b16 v[144:145], v78 offset:64\n" \
    "ds_read_b64_tr_b16 v[146:147], v78 offset:832\n" \
    "v_mfma_f32_16x16x32_bf16 %4, v[104:107], v[88:91], %4\n" \
    "ds_read_b128 v[148:151], v77 offset:0\n" \
    "v_mfma_f32_16x16x32_bf16 %5, v[104:107], v[92:95], %5\n" \
    "ds_read_b64_tr_b16 v[156:157], v78 offset:6144\n" \
    "v_mfma_f32_16x16x32_bf16 %0, v[116:119], v[108:111], %0\n" \
    "ds_read_b64_tr_b16 v[158:159], v78 offset:6912\n" \
    "v_mfma_f32_16x16x32_bf16 %1, v[116:119], v[112:115], %1\n" \
    "ds_read_b128 v[152:155], v77 offset:2048\n" \
    "v_mfma_f32_16x16x32_bf16 %2, v[120:123], v[108:111], %2\n" \
    "ds_read_b64_tr_b16 v[160:161], v79 offset:6144\n" \
    "v_mfma_f32_16x16x32_bf16 %3, v[120:123], v[112:115], %3\n" \
    "ds_read_b64_tr_b16 v[162:163], v79 offset:6912\n" \
    "v_mfma_f32_16x16x32_bf16 %4, v[124:127], v[108:111], %4\n" \
    "ds_read_b64_tr_b16 v[164:165], v78 offset:6208\n" \
    "v_mfma_f32_16x16x32_bf16 %5, v[124:127], v[112:115], %5\n" \
    "ds_read_b64_tr_b16 v[166:167], v78 offset:6976\n" \
    "s_cmp_lg_u32 %13, 0\n" \
    "s_cbranch_scc1 G1C60_%=\n" \
    "v_mfma_f32_16x16x32_bf16 %6, v[96:99], v[84:87], %6\n" \
    "v_mfma_f32_16x16x32_bf16 %7, v[100:103], v[84:87], %7\n" \
    "v_mfma_f32_16x16x32_bf16 %8, v[104:107], v[84:87], %8\n" \
    "G1C60_%=:\n" \
    "s_cmp_lg_u32 %13, 1\n" \
    "s_cbranch_scc1 G1C61_%=\n" \
    "v_mfma_f32_16x16x32_bf16 %6, v[116:119], v[84:87], %6\n" \
    "v_mfma_f32_16x16x32_bf16 %7, v[120:123], v[84:87], %7\n" \
    "v_mfma_f32_16x16x32_bf16 %8, v[124:127], v[84:87], %8\n" \
    "G1C61_%=:\n" \
    "s_waitcnt lgkmcnt(0)\n" \
    "v_mfma_f32_16x16x32_bf16 %0, v[136:139], v[128:131], %0\n" \
    "v_mfma_f32_16x16x32_bf16 %1, v[136:139], v[132:135], %1\n" \
    "v_mfma_f32_16x16x32_bf16 %2, v[140:143], v[128:131], %2\n" \
    "v_mfma_f32_16x16x32_bf16 %3, v[140:143], v[132:135], %3\n" \
    "v_mfma_f32_16x16x32_bf16 %4, v[144:147], v[128:131], %4\n" \
    "v_mfma_f32_16x16x32_bf16 %5, v[144:147], v[132:135], %5\n" \
    "v_mfma_f32_16x16x32_bf16 %0, v[156:159], v[148:151], %0\n" \
    "v_mfma_f32_16x16x32_bf16 %1, v[156:159], v[152:155], %1\n" \
    "v_mfma_f32_16x16x32_bf16 %2, v[160:163], v[148:151], %2\n" \
    "v_mfma_f32_16x16x32_bf16 %3, v[160:163], v[152:155], %3\n" \
    "v_mfma_f32_16x16x32_bf16 %4, v[164:167], v[148:151], %4\n" \
    "v_mfma_f32_16x16x32_bf16 %5, v[164:167], v[152:155], %5\n" \
    "s_cmp_lg_u32 %13, 2\n" \
    "s_cbranch_scc1 G1C70_%=\n" \
    "v_mfma_f32_16x16x32_bf16 %6, v[136:139], v[84:87], %6\n" \
    "v_mfma_f32_16x16x32_bf16 %7, v[140:143], v[84:87], %7\n" \
    "v_mfma_f32_16x16x32_bf16 %8, v[144:147], v[84:87], %8\n" \
    "G1C70_%=:\n" \
    "s_cmp_lg_u32 %13, 3\n" \
    "s_cbranch_scc1 G1C71_%=\n" \
    "v_mfma_f32_16x16x32_bf16 %6, v[156:159], v[84:87], %6\n" \
    "v_mfma_f32_16x16x32_bf16 %7, v[160:163], v[84:87], %7\n" \
    "v_mfma_f32_16x16x32_bf16 %8, v[164:167], v[84:87], %8\n" \
    "G1C71_%=:\n" \
    "s_nop 7\n" \
    ""
#define G1_QKV0_CLOB "v76", "v77", "v78", "v79", "v80", "v81", "v82", "v83", "v84", "v85", "v86", "v87", "v88", "v89", "v90", "v91", "v92", "v93", "v94", "v95", "v96", "v97", "v98", "v99", "v100", "v101", "v102", "v103", "v104", "v105", "v106", "v107", "v108", "v109", "v110", "v111", "v112", "v113", "v114", "v115", "v116", "v117", "v118", "v119", "v120", "v121", "v122", "v123", "v124", "v125", "v126", "v127", "v128", "v129", "v130", "v131", "v132", "v133", "v134", "v135", "v136", "v137", "v138", "v139", "v140", "v141", "v142", "v143", "v144", "v145", "v146", "v147", "v148", "v149", "v150", "v151", "v152", "v153", "v154", "v155", "v156", "v157", "v158", "v159", "v160", "v161", "v162", "v163", "v164", "v165", "v166", "v167"
#define G1_QKV1 \
    "v_add_u32 v76, 61440, %9\n" \
    "v_add_u32 v77, 61440, %10\n" \
    "v_add_u32 v78, 61440, %11\n" \
    "v_add_u32 v79, 61440, %12\n" \
    "v_add_u32 v80, 122880, %9\n" \
    "v_add_u32 v81, 122880, %10\n" \
    "v_add_u32 v82, 122880, %11\n" \
    "v_add_u32 v83, 122880, %12\n" \
    "v_mov_b32 v84, 0x3f803f80\n" \
    "v_mov_b32 v85, 0x3f803f80\n" \
    "v_mov_b32 v86, 0x3f803f80\n" \
    "v_mov_b32 v87, 0x3f803f80\n" \
    "s_barrier\n" \
    "ds_read_b128 v[88:91], %9 offset:0\n" \
    "ds_read_b64_tr_b16 v[96:97], %11 offset:0\n" \
    "ds_read_b64_tr_b16 v[98:99], %11 offset:768\n" \
    "ds_read_b128 v[92:95], %9 offset:2048\n" \
    "ds_read_b64_tr_b16 v[100:101], %12 offset:0\n" \
    "ds_read_b64_tr_b16 v[102:103], %12 offset:768\n" \
    "ds_read_b64_tr_b16 v[104:105], %11 offset:64\n" \
    "ds_read_b64_tr_b16 v[106:107], %11 offset:832\n" \
    "ds_read_b128 v[108:111], %10 offset:0\n" \
    "ds_read_b64_tr_b16 v[116:117], %11 offset:6144\n" \
    "ds_read_b64_tr_b16 v[118:119], %11 offset:6912\n" \
    "ds_read_b128 v[112:115], %10 offset:2048\n" \
    "ds_read_b64_tr_b16 v[120:121], %12 offset:6144\n" \
    "ds_read_b64_tr_b16 v[122:123], %12 offset:6912\n" \
    "ds_read_b64_tr_b16 v[124:125], %11 offset:6208\n" \
    "ds_read_b64_tr_b16 v[126:127], %11 offset:6976\n" \
    "s_waitcnt lgkmcnt(0)\n" \
    "s_barrier\n" \
    "ds_read_b128 v[128:131], %9 offset:30720\n" \
    "ds_read_b64_tr_b16 v[136:137], %11 offset:30720\n" \
    "v_mfma_f32_16x16x32_bf16 %0, v[96:99], v[88:91], 0\n" \
    "ds_read_b64_tr_b16 v[138:139], %11 offset:31488\n" \
    "ds_read_b128 v[132:135], %9 offset:32768\n" \
    "v_mfma_f32_16x16x32_bf16 %1, v[96:99], v[92:95], 0\n" \
    "ds_read_b64_tr_b16 v[140:141], %12 offset:30720\n" \
    "ds_read_b64_tr_b16 v[142:143], %12 offset:31488\n" \
    "v_mfma_f32_16x16x32_bf16 %2, v[100:103], v[88:91], 0\n" \
    "ds_read_b64_tr_b16 v[144:145], %11 offset:30784\n" \
    "ds_read_b64_tr_b16 v[146:147], %11 offset:31552\n" \
    "v_mfma_f32_16x16x32_bf16 %3, v[100:103], v[92:95], 0\n" \
    "ds_read_b128 v[148:151], %10 offset:30720\n" \
    "v_mfma_f32_16x16x32_bf16 %4, v[104:107], v[88:91], 0\n" \
    "ds_read_b64_tr_b16 v[156:157], %11 offset:36864\n" \
    "v_mfma_f32_16x16x32_bf16 %5, v[104:107], v[92:95], 0\n" \
    "ds_read_b64_tr_b16 v[158:159], %11 offset:37632\n" \
    "v_mfma_f32_16x16x32_bf16 %0, v[116:119], v[108:111], %0\n" \
    "ds_read_b128 v[152:155], %10 offset:32768\n" \
    "v_mfma_f32_16x16x32_bf16 %1, v[116:119], v[112:115], %1\n" \
    "ds_read_b64_tr_b16 v[160:161], %12 offset:36864\n" \
    "v_mfma_f32_16x16x32_bf16 %2, v[120:123], v[108:111], %2\n" \
    "ds_read_b64_tr_b16 v[162:163], %12 offset:37632\n" \
    "v_mfma_f32_16x16x32_bf16 %3, v[120:123], v[112:115], %3\n" \
    "ds_read_b64_tr_b16 v[164:165], %11 offset:36928\n" \
    "v_mfma_f32_16x16x32_bf16 %4, v[124:127], v[108:111], %4\n" \
    "ds_read_b64_tr_b16 v[166:167], %11 offset:37696\n" \
    "v_mfma_f32_16x16x32_bf16 %5, v[124:127], v[112:115], %5\n" \
    "s_cmp_lg_u32 %13, 0\n" \
    "s_cbranch_scc1 G1C00_%=\n" \
    "v_mfma_f32_16x16x32_bf16 %6, v[96:99], v[84:87], %6\n" \
    "v_mfma_f32_16x16x32_bf16 %7, v[100:103], v[84:87], %7\n" \
    "v_mfma_f32_16x16x32_bf16 %8, v[104:107], v[84:87], %8\n" \
    "G1C00_%=:\n" \
    "s_cmp_lg_u32 %13, 1\n" \
    "s_cbranch_scc1 G1C01_%=\n" \
    "v_mfma_f32_16x16x32_bf16 %6, v[116:119], v[84:87], %6\n" \
    "v_mfma_f32_16x16x32_bf16 %7, v[120:123], v[84:87], %7\n" \
    "v_mfma_f32_16x16x32_bf16 %8, v[124:127], v[84:87], %8\n" \
    "G1C01_%=:\n" \
    "s_waitcnt lgkmcnt(0)\n" \
    "s_barrier\n" \
    "ds_read_b128 v[88:91], v76 offset:0\n" \
    "ds_read_b64_tr_b16 v[96:97], v78 offset:0\n" \
    "v_mfma_f32_16x16x32_bf16 %0, v[136:139], v[128:131], %0\n" \
    "ds_read_b64_tr_b16 v[98:99], v78 offset:768\n" \
    "ds_read_b128 v[92:95], v76 offset:2048\n" \
    "v_mfma_f32_16x16x32_bf16 %1, v[136:139], v[132:135], %1\n" \
    "ds_read_b64_tr_b16 v[100:101], v79 offset:0\n" \
    "ds_read_b64_tr_b16 v[102:103], v79 offset:768\n" \
    "v_mfma_f32_16x16x32_bf16 %2, v[140:143], v[128:131], %2\n" \
    "ds_read_b64_tr_b16 v[104:105], v78 offset:64\n" \
    "ds_read_b64_tr_b16 v[106:107], v78 offset:832\n" \
    "v_mfma_f32_16x16x32_bf16 %3, v[140:143], v[132:135], %3\n" \
    "ds_read_b128 v[108:111], v77 offset:0\n" \
    "v_mfma_f32_16x16x32_bf16 %4, v[144:147], v[128:131], %4\n" \
    "ds_read_b64_tr_b16 v[116:117], v78 offset:6144\n" \
    "v_mfma_f32_16x16x32_bf16 %5, v[144:147], v[132:135], %5\n" \
    "ds_read_b64_tr_b16 v[118:119], v78 offset:6912\n" \
    "v_mfma_f32_16x16x32_bf16 %0, v[156:159], v[148:151], %0\n" \
    "ds_read_b128 v[112:115], v77 offset:2048\n" \
    "v_mfma_f32_16x16x32_bf16 %1, v[156:159], v[152:155], %1\n" \
    "ds_read_b64_tr_b16 v[120:121], v79 offset:6144\n" \
    "v_mfma_f32_16x16x32_bf16 %2, v[160:163], v[148:151], %2\n" \
    "ds_read_b64_tr_b16 v[122:123], v79 offset:6912\n" \
    "v_mfma_f32_16x16x32_bf16 %3, v[160:163], v[152:155], %3\n" \
    "ds_read_b64_tr_b16 v[124:125], v78 offset:6208\n" \
    "v_mfma_f32_16x16x32_bf16 %4, v[164:167], v[148:151], %4\n" \
    "ds_read_b64_tr_b16 v[126:127], v78 offset:6976\n" \
    "v_mfma_f32_16x16x32_bf16 %5, v[164:167], v[152:155], %5\n" \
    "s_cmp_lg_u32 %13, 2\n" \
    "s_cbranch_scc1 G1C10_%=\n" \
    "v_mfma_f32_16x16x32_bf16 %6, v[136:139], v[84:87], %6\n" \
    "v_mfma_f32_16x16x32_bf16 %7, v[140:143], v[84:87], %7\n" \
    "v_mfma_f32_16x16x32_bf16 %8, v[144:147], v[84:87], %8\n" \
    "G1C10_%=:\n" \
    "s_cmp_lg_u32 %13, 3\n" \
    "s_cbranch_scc1 G1C11_%=\n" \
    "v_mfma_f32_16x16x32_bf16 %6, v[156:159], v[84:87], %6\n" \
    "v_mfma_f32_16x16x32_bf16 %7, v[160:163], v[84:87], %7\n" \
    "v_mfma_f32_16x16x32_bf16 %8, v[164:167], v[84:87], %8\n" \
    "G1C11_%=:\n" \
    "s_waitcnt lgkmcnt(0)\n" \
    "s_barrier\n" \
    "ds_read_b128 v[128:131], v76 offset:30720\n" \
    "ds_read_b64_tr_b16 v[136:137], v78 offset:30720\n" \
    "v_mfma_f32_16x16x32_bf16 %0, v[96:99], v[88:91], %0\n" \
    "ds_read_b64_tr_b16 v[138:139], v78 offset:31488\n" \
    "ds_read_b128 v[132:135], v76 offset:32768\n" \
    "v_mfma_f32_16x16x32_bf16 %1, v[96:99], v[92:95], %1\n" \
    "ds_read_b64_tr_b16 v[140:141], v79 offset:30720\n" \
    "ds_read_b64_tr_b16 v[142:143], v79 offset:31488\n" \
    "v_mfma_f32_16x16x32_bf16 %2, v[100:103], v[88:91], %2\n" \
    "ds_read_b64_tr_b16 v[144:145], v78 offset:30784\n" \
    "ds_read_b64_tr_b16 v[146:147], v78 offset:31552\n" \
    "v_mfma_f32_16x16x32_bf16 %3, v[100:103], v[92:95], %3\n" \
    "ds_read_b128 v[148:151], v77 offset:30720\n" \
    "v_mfma_f32_16x16x32_bf16 %4, v[104:107], v[88:91], %4\n" \
    "ds_read_b64_tr_b16 v[156:157], v78 offset:36864\n" \
    "v_mfma_f32_16x16x32_bf16 %5, v[104:107], v[92:95], %5\n" \
    "ds_read_b64_tr_b16 v[158:159], v78 offset:37632\n" \
    "v_mfma_f32_16x16x32_bf16 %0, v[116:119], v[108:111], %0\n" \
    "ds_read_b128 v[152:155], v77 offset:32768\n" \
    "v_mfma_f32_16x16x32_bf16 %1, v[116:119], v[112:115], %1\n" \
    "ds_read_b64_tr_b16 v[160:161], v79 offset:36864\n" \
    "v_mfma_f32_16x16x32_bf16 %2, v[120:123], v[108:111], %2\n" \
    "ds_read_b64_tr_b16 v[162:163], v79 offset:37632\n" \
    "v_mfma_f32_16x16x32_bf16 %3, v[120:123], v[112:115], %3\n" \
    "ds_read_b64_tr_b16 v[164:165], v78 offset:36928\n" \
    "v_mfma_f32_16x16x32_bf16 %4, v[124:127], v[108:111], %4\n" \
    "ds_read_b64_tr_b16 v[166:167], v78 offset:37696\n" \
    "v_mfma_f32_16x16x32_bf16 %5, v[124:127], v[112:115], %5\n" \
    "s_cmp_lg_u32 %13, 0\n" \
    "s_cbranch_scc1 G1C20_%=\n" \
    "v_mfma_f32_16x16x32_bf16 %6, v[96:99], v[84:87], %6\n" \
    "v_mfma_f32_16x16x32_bf16 %7, v[100:103], v[84:87], %7\n" \
    "v_mfma_f32_16x16x32_bf16 %8, v[104:107], v[84:87], %8\n" \
    "G1C20_%=:\n" \
    "s_cmp_lg_u32 %13, 1\n" \
    "s_cbranch_scc1 G1C21_%=\n" \
    "v_mfma_f32_16x16x32_bf16 %6, v[116:119], v[84:87], %6\n" \
    "v_mfma_f32_16x16x32_bf16 %7, v[120:123], v[84:87], %7\n" \
    "v_mfma_f32_16x16x32_bf16 %8, v[124:127], v[84:87], %8\n" \
    "G1C21_%=:\n" \
    "s_waitcnt lgkmcnt(0)\n" \
    "s_barrier\n" \
    "ds_read_b128 v[88:91], v80 offset:0\n" \
    "ds_read_b64_tr_b16 v[96:97], v82 offset:0\n" \
    "v_mfma_f32_16x16x32_bf16 %0, v[136:139], v[128:131], %0\n" \
    "ds_read_b64_tr_b16 v[98:99], v82 offset:768\n" \
    "ds_read_b128 v[92:95], v80 offset:2048\n" \
    "v_mfma_f32_16x16x32_bf16 %1, v[136:139], v[132:135], %1\n" \
    "ds_read_b64_tr_b16 v[100:101], v83 offset:0\n" \
    "ds_read_b64_tr_b16 v[102:103], v83 offset:768\n" \
    "v_mfma_f32_16x16x32_bf16 %2, v[140:143], v[128:131], %2\n" \
    "ds_read_b64_tr_b16 v[104:105], v82 offset:64\n" \
    "ds_read_b64_tr_b16 v[106:107], v82 offset:832\n" \
    "v_mfma_f32_16x16x32_bf16 %3, v[140:143], v[132:135], %3\n" \
    "ds_read_b128 v[108:111], v81 offset:0\n" \
    "v_mfma_f32_16x16x32_bf16 %4, v[144:147], v[128:131], %4\n" \
    "ds_read_b64_tr_b16 v[116:117], v82 offset:6144\n" \
    "v_mfma_f32_16x16x32_bf16 %5, v[144:147], v[132:135], %5\n" \
    "ds_read_b64_tr_b16 v[118:119], v82 offset:6912\n" \
    "v_mfma_f32_16x16x32_bf16 %0, v[156:159], v[148:151], %0\n" \
    "ds_read_b128 v[112:115], v81 offset:2048\n" \
    "v_mfma_f32_16x16x32_bf16 %1, v[156:159], v[152:155], %1\n" \
    "ds_read_b64_tr_b16 v[120:121], v83 offset:6144\n" \
    "v_mfma_f32_16x16x32_bf16 %2, v[160:163], v[148:151], %2\n" \
    "ds_read_b64_tr_b16 v[122:123], v83 offset:6912\n" \
    "v_mfma_f32_16x16x32_bf16 %3, v[160:163], v[152:155], %3\n" \
    "ds_read_b64_tr_b16 v[124:125], v82 offset:6208\n" \
    "v_mfma_f32_16x16x32_bf16 %4, v[164:167], v[148:151], %4\n" \
    "ds_read_b64_tr_b16 v[126:127], v82 offset:6976\n" \
    "v_mfma_f32_16x16x32_bf16 %5, v[164:167], v[152:155], %5\n" \
    "s_cmp_lg_u32 %13, 2\n" \
    "s_cbranch_scc1 G1C30_%=\n" \
    "v_mfma_f32_16x16x32_bf16 %6, v[136:139], v[84:87], %6\n" \
    "v_mfma_f32_16x16x32_bf16 %7, v[140:143], v[84:87], %7\n" \
    "v_mfma_f32_16x16x32_bf16 %8, v[144:147], v[84:87], %8\n" \
    "G1C30_%=:\n" \
    "s_cmp_lg_u32 %13, 3\n" \
    "s_cbranch_scc1 G1C31_%=\n" \
    "v_mfma_f32_16x16x32_bf16 %6, v[156:159], v[84:87], %6\n" \
    "v_mfma_f32_16x16x32_bf16 %7, v[160:163], v[84:87], %7\n" \
    "v_mfma_f32_16x16x32_bf16 %8, v[164:167], v[84:87], %8\n" \
    "G1C31_%=:\n" \
    "s_waitcnt lgkmcnt(0)\n" \
    "s_barrier\n" \
    "ds_read_b128 v[128:131], %9 offset:0\n" \
    "ds_read_b64_tr_b16 v[136:137], %11 offset:0\n" \
    "v_mfma_f32_16x16x32_bf16 %0, v[96:99], v[88:91], %0\n" \
    "ds_read_b64_tr_b16 v[138:139], %11 offset:768\n" \
    "ds_read_b128 v[132:135], %9 offset:2048\n" \
    "v_mfma_f32_16x16x32_bf16 %1, v[96:99], v[92:95], %1\n" \
    "ds_read_b64_tr_b16 v[140:141], %12 offset:0\n" \
    "ds_read_b64_tr_b16 v[142:143], %12 offset:768\n" \
    "v_mfma_f32_16x16x32_bf16 %2, v[100:103], v[88:91], %2\n" \
    "ds_read_b64_tr_b16 v[144:145], %11 offset:64\n" \
    "ds_read_b64_tr_b16 v[146:147], %11 offset:832\n" \
    "v_mfma_f32_16x16x32_bf16 %3, v[100:103], v[92:95], %3\n" \
    "ds_read_b128 v[148:151], %10 offset:0\n" \
    "v_mfma_f32_16x16x32_bf16 %4, v[104:107], v[88:91], %4\n" \
    "ds_read_b64_tr_b16 v[156:157], %11 offset:6144\n" \
    "v_mfma_f32_16x16x32_bf16 %5, v[104:107], v[92:95], %5\n" \
    "ds_read_b64_tr_b16 v[158:159], %11 offset:6912\n" \
    "v_mfma_f32_16x16x32_bf16 %0, v[116:119], v[108:111], %0\n" \
    "ds_read_b128 v[152:155], %10 offset:2048\n" \
    "v_mfma_f32_16x16x32_bf16 %1, v[116:119], v[112:115], %1\n" \
    "ds_read_b64_tr_b16 v[160:161], %12 offset:6144\n" \
    "v_mfma_f32_16x16x32_bf16 %2, v[120:123], v[108:111], %2\n" \
    "ds_read_b64_tr_b16 v[162:163], %12 offset:6912\n" \
    "v_mfma_f32_16x16x32_bf16 %3, v[120:123], v[112:115], %3\n" \
    "ds_read_b64_tr_b16 v[164:165], %11 offset:6208\n" \
    "v_mfma_f32_16x16x32_bf16 %4, v[124:127], v[108:111], %4\n" \
    "ds_read_b64_tr_b16 v[166:167], %11 offset:6976\n" \
    "v_mfma_f32_16x16x32_bf16 %5, v[124:127], v[112:115], %5\n" \
    "s_cmp_lg_u32 %13, 0\n" \
    "s_cbranch_scc1 G1C40_%=\n" \
    "v_mfma_f32_16x16x32_bf16 %6, v[96:99], v[84:87], %6\n" \
    "v_mfma_f32_16x16x32_bf16 %7, v[100:103], v[84:87], %7\n" \
    "v_mfma_f32_16x16x32_bf16 %8, v[104:107], v[84:87], %8\n" \
    "G1C40_%=:\n" \
    "s_cmp_lg_u32 %13, 1\n" \
    "s_cbranch_scc1 G1C41_%=\n" \
    "v_mfma_f32_16x16x32_bf16 %6, v[116:119], v[84:87], %6\n" \
    "v_mfma_f32_16x16x32_bf16 %7, v[120:123], v[84:87], %7\n" \
    "v_mfma_f32_16x16x32_bf16 %8, v[124:127], v[84:87], %8\n" \
    "G1C41_%=:\n" \
    "s_waitcnt lgkmcnt(0)\n" \
    "s_barrier\n" \
    "ds_read_b128 v[88:91], %9 offset:30720\n" \
    "ds_read_b64_tr_b16 v[96:97], %11 offset:30720\n" \
    "v_mfma_f32_16x16x32_bf16 %0, v[136:139], v[128:131], %0\n" \
    "ds_read_b64_tr_b16 v[98:99], %11 offset:31488\n" \
    "ds_read_b128 v[92:95], %9 offset:32768\n" \
    "v_mfma_f32_16x16x32_bf16 %1, v[136:139], v[132:135], %1\n" \
    "ds_read_b64_tr_b16 v[100:101], %12 offset:30720\n" \
    "ds_read_b64_tr_b16 v[102:103], %12 offset:31488\n" \
    "v_mfma_f32_16x16x32_bf16 %2, v[140:143], v[128:131], %2\n" \
    "ds_read_b64_tr_b16 v[104:105], %11 offset:30784\n" \
    "ds_read_b64_tr_b16 v[106:107], %11 offset:31552\n" \
    "v_mfma_f32_16x16x32_bf16 %3, v[140:143], v[132:135], %3\n" \
    "ds_read_b128 v[108:111], %10 offset:30720\n" \
    "v_mfma_f32_16x16x32_bf16 %4, v[144:147], v[128:131], %4\n" \
    "ds_read_b64_tr_b16 v[116:117], %11 offset:36864\n" \
    "v_mfma_f32_16x16x32_bf16 %5, v[144:147], v[132:135], %5\n" \
    "ds_read_b64_tr_b16 v[118:119], %11 offset:37632\n" \
    "v_mfma_f32_16x16x32_bf16 %0, v[156:159], v[148:151], %0\n" \
    "ds_read_b128 v[112:115], %10 offset:32768\n" \
    "v_mfma_f32_16x16x32_bf16 %1, v[156:159], v[152:155], %1\n" \
    "ds_read_b64_tr_b16 v[120:121], %12 offset:36864\n" \
    "v_mfma_f32_16x16x32_bf16 %2, v[160:163], v[148:151], %2\n" \
    "ds_read_b64_tr_b16 v[122:123], %12 offset:37632\n" \
    "v_mfma_f32_16x16x32_bf16 %3, v[160:163], v[152:155], %3\n" \
    "ds_read_b64_tr_b16 v[124:125], %11 offset:36928\n" \
    "v_mfma_f32_16x16x32_bf16 %4, v[164:167], v[148:151], %4\n" \
    "ds_read_b64_tr_b16 v[126:127], %11 offset:37696\n" \
    "v_mfma_f32_16x16x32_bf16 %5, v[164:167], v[152:155], %5\n" \
    "s_cmp_lg_u32 %13, 2\n" \
    "s_cbranch_scc1 G1C50_%=\n" \
    "v_mfma_f32_16x16x32_bf16 %6, v[136:139], v[84:87], %6\n" \
    "v_mfma_f32_16x16x32_bf16 %7, v[140:143], v[84:87], %7\n" \
    "v_mfma_f32_16x16x32_bf16 %8, v[144:147], v[84:87], %8\n" \
    "G1C50_%=:\n" \
    "s_cmp_lg_u32 %13, 3\n" \
    "s_cbranch_scc1 G1C51_%=\n" \
    "v_mfma_f32_16x16x32_bf16 %6, v[156:159], v[84:87], %6\n" \
    "v_mfma_f32_16x16x32_bf16 %7, v[160:163], v[84:87], %7\n" \
    "v_mfma_f32_16x16x32_bf16 %8, v[164:167], v[84:87], %8\n" \
    "G1C51_%=:\n" \
    "s_waitcnt lgkmcnt(0)\n" \
    "s_barrier\n" \
    "ds_read_b128 v[128:131], v76 offset:0\n" \
    "ds_read_b64_tr_b16 v[136:137], v78 offset:0\n" \
    "v_mfma_f32_16x16x32_bf16 %0, v[96:99], v[88:91], %0\n" \
    "ds_read_b64_tr_b16 v[138:139], v78 offset:768\n" \
    "ds_read_b128 v[132:135], v76 offset:2048\n" \
    "v_mfma_f32_16x16x32_bf16 %1, v[96:99], v[92:95], %1\n" \
    "ds_read_b64_tr_b16 v[140:141], v79 offset:0\n" \
    "ds_read_b64_tr_b16 v[142:143], v79 offset:768\n" \
    "v_mfma_f32_16x16x32_bf16 %2, v[100:103], v[88:91], %2\n" \
    "ds_read_b64_tr_b16 v[144:145], v78 offset:64\n" \
    "ds_read_b64_tr_b16 v[146:147], v78 offset:832\n" \
    "v_mfma_f32_16x16x32_bf16 %3, v[100:103], v[92:95], %3\n" \
    "ds_read_b128 v[148:151], v77 offset:0\n" \
    "v_mfma_f32_16x16x32_bf16 %4, v[104:107], v[88:91], %4\n" \
    "ds_read_b64_tr_b16 v[156:157], v78 offset:6144\n" \
    "v_mfma_f32_16x16x32_bf16 %5, v[104:107], v[92:95], %5\n" \
    "ds_read_b64_tr_b16 v[158:159], v78 offset:6912\n" \
    "v_mfma_f32_16x16x32_bf16 %0, v[116:119], v[108:111], %0\n" \
    "ds_read_b128 v[152:155], v77 offset:2048\n" \
    "v_mfma_f32_16x16x32_bf16 %1, v[116:119], v[112:115], %1\n" \
    "ds_read_b64_tr_b16 v[160:161], v79 offset:6144\n" \
    "v_mfma_f32_16x16x32_bf16 %2, v[120:123], v[108:111], %2\n" \
    "ds_read_b64_tr_b16 v[162:163], v79 offset:6912\n" \
    "v_mfma_f32_16x16x32_bf16 %3, v[120:123], v[112:115], %3\n" \
    "ds_read_b64_tr_b16 v[164:165], v78 offset:6208\n" \
    "v_mfma_f32_16x16x32_bf16 %4, v[124:127], v[108:111], %4\n" \
    "ds_read_b64_tr_b16 v[166:167], v78 offset:6976\n" \
    "v_mfma_f32_16x16x32_bf16 %5, v[124:127], v[112:115], %5\n" \
    "s_cmp_lg_u32 %13, 0\n" \
    "s_cbranch_scc1 G1C60_%=\n" \
    "v_mfma_f32_16x16x32_bf16 %6, v[96:99], v[84:87], %6\n" \
    "v_mfma_f32_16x16x32_bf16 %7, v[100:103], v[84:87], %7\n" \
    "v_mfma_f32_16x16x32_bf16 %8, v[104:107], v[84:87], %8\n" \
    "G1C60_%=:\n" \
    "s_cmp_lg_u32 %13, 1\n" \
    "s_cbranch_scc1 G1C61_%=\n" \
    "v_mfma_f32_16x16x32_bf16 %6, v[116:119], v[84:87], %6\n" \
    "v_mfma_f32_16x16x32_bf16 %7, v[120:123], v[84:87], %7\n" \
    "v_mfma_f32_16x16x32_bf16 %8, v[124:127], v[84:87], %8\n" \
    "G1C61_%=:\n" \
    "s_waitcnt lgkmcnt(0)\n" \
    "v_mfma_f32_16x16x32_bf16 %0, v[136:139], v[128:131], %0\n" \
    "v_mfma_f32_16x16x32_bf16 %1, v[136:139], v[132:135], %1\n" \
    "v_mfma_f32_16x16x32_bf16 %2, v[140:143], v[128:131], %2\n" \
    "v_mfma_f32_16x16x32_bf16 %3, v[140:143], v[132:135], %3\n" \
    "v_mfma_f32_16x16x32_bf16 %4, v[144:147], v[128:131], %4\n" \
    "v_mfma_f32_16x16x32_bf16 %5, v[144:147], v[132:135], %5\n" \
    "v_mfma_f32_16x16x32_bf16 %0, v[156:159], v[148:151], %0\n" \
    "v_mfma_f32_16x16x32_bf16 %1, v[156:159], v[152:155], %1\n" \
    "v_mfma_f32_16x16x32_bf16 %2, v[160:163], v[148:151], %2\n" \
    "v_mfma_f32_16x16x32_bf16 %3, v[160:163], v[152:155], %3\n" \
    "v_mfma_f32_16x16x32_bf16 %4, v[164:167], v[148:151], %4\n" \
    "v_mfma_f32_16x16x32_bf16 %5, v[164:167], v[152:155], %5\n" \
    "s_cmp_lg_u32 %13, 2\n" \
    "s_cbranch_scc1 G1C70_%=\n" \
    "v_mfma_f32_16x16x32_bf16 %6, v[136:139], v[84:87], %6\n" \
    "v_mfma_f32_16x16x32_bf16 %7, v[140:143], v[84:87], %7\n" \
    "v_mfma_f32_16x16x32_bf16 %8, v[144:147], v[84:87], %8\n" \
    "G1C70_%=:\n" \
    "s_cmp_lg_u32 %13, 3\n" \
    "s_cbranch_scc1 G1C71_%=\n" \
    "v_mfma_f32_16x16x32_bf16 %6, v[156:159], v[84:87], %6\n" \
    "v_mfma_f32_16x16x32_bf16 %7, v[160:163], v[84:87], %7\n" \
    "v_mfma_f32_16x16x32_bf16 %8, v[164:167], v[84:87], %8\n" \
    "G1C71_%=:\n" \
    "s_nop 7\n" \
    ""
#define G1_QKV1_CLOB "v76", "v77", "v78", "v79", "v80", "v81", "v82", "v83", "v84", "v85", "v86", "v87", "v88", "v89", "v90", "v91", "v92", "v93", "v94", "v95", "v96", "v97", "v98", "v99", "v100", "v101", "v102", "v103", "v104", "v105", "v106", "v107", "v108", "v109", "v110", "v111", "v112", "v113", "v114", "v115", "v116", "v117", "v118", "v119", "v120", "v121", "v122", "v123", "v124", "v125", "v126", "v127", "v128", "v129", "v130", "v131", "v132", "v133", "v134", "v135", "v136", "v137", "v138", "v139", "v140", "v141", "v142", "v143", "v144", "v145", "v146", "v147", "v148", "v149", "v150", "v151", "v152", "v153", "v154", "v155", "v156", "v157", "v158", "v159", "v160", "v161", "v162", "v163", "v164", "v165", "v166", "v167"
#define G1_FFN0 \
    "v_add_u32 v62, 61440, %12\n" \
    "v_add_u32 v63, 61440, %13\n" \
    "v_add_u32 v64, 61440, %14\n" \
    "v_add_u32 v65, 122880, %12\n" \
    "v_add_u32 v66, 122880, %13\n" \
    "v_add_u32 v67, 122880, %14\n" \
    "v_mov_b32 v68, 0x3f803f80\n" \
    "v_mov_b32 v69, 0x3f803f80\n" \
    "v_mov_b32 v70, 0x3f803f80\n" \
    "v_mov_b32 v71, 0x3f803f80\n" \
    "ds_read_b128 v[72:75], v62 offset:30720\n" \
    "ds_read_b64_tr_b16 v[80:81], v64 offset:30720\n" \
    "ds_read_b64_tr_b16 v[82:83], v64 offset:32512\n" \
    "ds_read_b128 v[76:79], v62 offset:32768\n" \
    "ds_read_b64_tr_b16 v[84:85], v64 offset:30752\n" \
    "ds_read_b64_tr_b16 v[86:87], v64 offset:32544\n" \
    "ds_read_b64_tr_b16 v[88:89], v64 offset:30784\n" \
    "ds_read_b64_tr_b16 v[90:91], v64 offset:32576\n" \
    "ds_read_b64_tr_b16 v[92:93], v64 offset:30816\n" \
    "ds_read_b64_tr_b16 v[94:95], v64 offset:32608\n" \
    "ds_read_b128 v[96:99], v63 offset:30720\n" \
    "ds_read_b64_tr_b16 v[104:105], v64 offset:37888\n" \
    "ds_read_b64_tr_b16 v[106:107], v64 offset:39680\n" \
    "ds_read_b128 v[100:103], v63 offset:32768\n" \
    "ds_read_b64_tr_b16 v[108:109], v64 offset:37920\n" \
    "ds_read_b64_tr_b16 v[110:111], v64 offset:39712\n" \
    "ds_read_b64_tr_b16 v[112:113], v64 offset:37952\n" \
    "ds_read_b64_tr_b16 v[114:115], v64 offset:39744\n" \
    "ds_read_b64_tr_b16 v[116:117], v64 offset:37984\n" \
    "ds_read_b64_tr_b16 v[118:119], v64 offset:39776\n" \
    "s_waitcnt lgkmcnt(0)\n" \
    "s_barrier\n" \
    "v_mfma_f32_16x16x32_bf16 %0, v[80:83], v[72:75], 0\n" \
    "ds_read_b128 v[120:123], v65 offset:0\n" \
    "ds_read_b64_tr_b16 v[128:129], v67 offset:0\n" \
    "v_mfma_f32_16x16x32_bf16 %1, v[80:83], v[76:79], 0\n" \
    "ds_read_b64_tr_b16 v[130:131], v67 offset:1792\n" \
    "ds_read_b128 v[124:127], v65 offset:2048\n" \
    "v_mfma_f32_16x16x32_bf16 %2, v[84:87], v[72:75], 0\n" \
    "ds_read_b64_tr_b16 v[132:133], v67 offset:32\n" \
    "ds_read_b64_tr_b16 v[134:135], v67 offset:1824\n" \
    "v_mfma_f32_16x16x32_bf16 %3, v[84:87], v[76:79], 0\n" \
    "ds_read_b64_tr_b16 v[136:137], v67 offset:64\n" \
    "ds_read_b64_tr_b16 v[138:139], v67 offset:1856\n" \
    "v_mfma_f32_16x16x32_bf16 %4, v[88:91], v[72:75], 0\n" \
    "ds_read_b64_tr_b16 v[140:141], v67 offset:96\n" \
    "v_mfma_f32_16x16x32_bf16 %5, v[88:91], v[76:79], 0\n" \
    "ds_read_b64_tr_b16 v[142:143], v67 offset:1888\n" \
    "v_mfma_f32_16x16x32_bf16 %6, v[92:95], v[72:75], 0\n" \
    "ds_read_b128 v[144:147], v66 offset:0\n" \
    "v_mfma_f32_16x16x32_bf16 %7, v[92:95], v[76:79], 0\n" \
    "ds_read_b64_tr_b16 v[152:153], v67 offset:7168\n" \
    "v_mfma_f32_16x16x32_bf16 %0, v[104:107], v[96:99], %0\n" \
    "ds_read_b64_tr_b16 v[154:155], v67 offset:8960\n" \
    "v_mfma_f32_16x16x32_bf16 %1, v[104:107], v[100:103], %1\n" \
    "ds_read_b128 v[148:151], v66 offset:2048\n" \
    "v_mfma_f32_16x16x32_bf16 %2, v[108:111], v[96:99], %2\n" \
    "ds_read_b64_tr_b16 v[156:157], v67 offset:7200\n" \
    "v_mfma_f32_16x16x32_bf16 %3, v[108:111], v[100:103], %3\n" \
    "ds_read_b64_tr_b16 v[158:159], v67 offset:8992\n" \
    "v_mfma_f32_16x16x32_bf16 %4, v[112:115], v[96:99], %4\n" \
    "ds_read_b64_tr_b16 v[160:161], v67 offset:7232\n" \
    "v_mfma_f32_16x16x32_bf16 %5, v[112:115], v[100:103], %5\n" \
    "ds_read_b64_tr_b16 v[162:163], v67 offset:9024\n" \
    "v_mfma_f32_16x16x32_bf16 %6, v[116:119], v[96:99], %6\n" \
    "ds_read_b64_tr_b16 v[164:165], v67 offset:7264\n" \
    "v_mfma_f32_16x16x32_bf16 %7, v[116:119], v[100:103], %7\n" \
    "ds_read_b64_tr_b16 v[166:167], v67 offset:9056\n" \
    "s_cmp_lg_u32 %15, 0\n" \
    "s_cbranch_scc1 G1C00_%=\n" \
    "v_mfma_f32_16x16x32_bf16 %8, v[80:83], v[68:71], %8\n" \
    "v_mfma_f32_16x16x32_bf16 %9, v[84:87], v[68:71], %9\n" \
    "v_mfma_f32_16x16x32_bf16 %10, v[88:91], v[68:71], %10\n" \
    "v_mfma_f32_16x16x32_bf16 %11, v[92:95], v[68:71], %11\n" \
    "G1C00_%=:\n" \
    "s_cmp_lg_u32 %15, 1\n" \
    "s_cbranch_scc1 G1C01_%=\n" \
    "v_mfma_f32_16x16x32_bf16 %8, v[104:107], v[68:71], %8\n" \
    "v_mfma_f32_16x16x32_bf16 %9, v[108:111], v[68:71], %9\n" \
    "v_mfma_f32_16x16x32_bf16 %10, v[112:115], v[68:71], %10\n" \
    "v_mfma_f32_16x16x32_bf16 %11, v[116:119], v[68:71], %11\n" \
    "G1C01_%=:\n" \
    "s_waitcnt lgkmcnt(0)\n" \
    "s_barrier\n" \
    "v_mfma_f32_16x16x32_bf16 %0, v[128:131], v[120:123], %0\n" \
    "ds_read_b128 v[72:75], %12 offset:0\n" \
    "ds_read_b64_tr_b16 v[80:81], %14 offset:0\n" \
    "v_mfma_f32_16x16x32_bf16 %1, v[128:131], v[124:127], %1\n" \
    "ds_read_b64_tr_b16 v[82:83], %14 offset:1792\n" \
    "ds_read_b128 v[76:79], %12 offset:2048\n" \
    "v_mfma_f32_16x16x32_bf16 %2, v[132:135], v[120:123], %2\n" \
    "ds_read_b64_tr_b16 v[84:85], %14 offset:32\n" \
    "ds_read_b64_tr_b16 v[86:87], %14 offset:1824\n" \
    "v_mfma_f32_16x16x32_bf16 %3, v[132:135], v[124:127], %3\n" \
    "ds_read_b64_tr_b16 v[88:89], %14 offset:64\n" \
    "ds_read_b64_tr_b16 v[90:91], %14 offset:1856\n" \
    "v_mfma_f32_16x16x32_bf16 %4, v[136:139], v[120:123], %4\n" \
    "ds_read_b64_tr_b16 v[92:93], %14 offset:96\n" \
    "v_mfma_f32_16x16x32_bf16 %5, v[136:139], v[124:127], %5\n" \
    "ds_read_b64_tr_b16 v[94:95], %14 offset:1888\n" \
    "v_mfma_f32_16x16x32_bf16 %6, v[140:143], v[120:123], %6\n" \
    "ds_read_b128 v[96:99], %13 offset:0\n" \
    "v_mfma_f32_16x16x32_bf16 %7, v[140:143], v[124:127], %7\n" \
    "ds_read_b64_tr_b16 v[104:105], %14 offset:7168\n" \
    "v_mfma_f32_16x16x32_bf16 %0, v[152:155], v[144:147], %0\n" \
    "ds_read_b64_tr_b16 v[106:107], %14 offset:8960\n" \
    "v_mfma_f32_16x16x32_bf16 %1, v[152:155], v[148:151], %1\n" \
    "ds_read_b128 v[100:103], %13 offset:2048\n" \
    "v_mfma_f32_16x16x32_bf16 %2, v[156:159], v[144:147], %2\n" \
    "ds_read_b64_tr_b16 v[108:109], %14 offset:7200\n" \
    "v_mfma_f32_16x16x32_bf16 %3, v[156:159], v[148:151], %3\n" \
    "ds_read_b64_tr_b16 v[110:111], %14 offset:8992\n" \
    "v_mfma_f32_16x16x32_bf16 %4, v[160:163], v[144:147], %4\n" \
    "ds_read_b64_tr_b16 v[112:113], %14 offset:7232\n" \
    "v_mfma_f32_16x16x32_bf16 %5, v[160:163], v[148:151], %5\n" \
    "ds_read_b64_tr_b16 v[114:115], %14 offset:9024\n" \
    "v_mfma_f32_16x16x32_bf16 %6, v[164:167], v[144:147], %6\n" \
    "ds_read_b64_tr_b16 v[116:117], %14 offset:7264\n" \
    "v_mfma_f32_16x16x32_bf16 %7, v[164:167], v[148:151], %7\n" \
    "ds_read_b64_tr_b16 v[118:119], %14 offset:9056\n" \
    "s_cmp_lg_u32 %15, 2\n" \
    "s_cbranch_scc1 G1C10_%=\n" \
    "v_mfma_f32_16x16x32_bf16 %8, v[128:131], v[68:71], %8\n" \
    "v_mfma_f32_16x16x32_bf16 %9, v[132:135], v[68:71], %9\n" \
    "v_mfma_f32_16x16x32_bf16 %10, v[136:139], v[68:71], %10\n" \
    "v_mfma_f32_16x16x32_bf16 %11, v[140:143], v[68:71], %11\n" \
    "G1C10_%=:\n" \
    "s_cmp_lg_u32 %15, 3\n" \
    "s_cbranch_scc1 G1C11_%=\n" \
    "v_mfma_f32_16x16x32_bf16 %8, v[152:155], v[68:71], %8\n" \
    "v_mfma_f32_16x16x32_bf16 %9, v[156:159], v[68:71], %9\n" \
    "v_mfma_f32_16x16x32_bf16 %10, v[160:163], v[68:71], %10\n" \
    "v_mfma_f32_16x16x32_bf16 %11, v[164:167], v[68:71], %11\n" \
    "G1C11_%=:\n" \
    "s_waitcnt lgkmcnt(0)\n" \
    "s_barrier\n" \
    "v_mfma_f32_16x16x32_bf16 %0, v[80:83], v[72:75], %0\n" \
    "ds_read_b128 v[120:123], %12 offset:30720\n" \
    "ds_read_b64_tr_b16 v[128:129], %14 offset:30720\n" \
    "v_mfma_f32_16x16x32_bf16 %1, v[80:83], v[76:79], %1\n" \
    "ds_read_b64_tr_b16 v[130:131], %14 offset:32512\n" \
    "ds_read_b128 v[124:127], %12 offset:32768\n" \
    "v_mfma_f32_16x16x32_bf16 %2, v[84:87], v[72:75], %2\n" \
    "ds_read_b64_tr_b16 v[132:133], %14 offset:30752\n" \
    "ds_read_b64_tr_b16 v[134:135], %14 offset:32544\n" \
    "v_mfma_f32_16x16x32_bf16 %3, v[84:87], v[76:79], %3\n" \
    "ds_read_b64_tr_b16 v[136:137], %14 offset:30784\n" \
    "ds_read_b64_tr_b16 v[138:139], %14 offset:32576\n" \
    "v_mfma_f32_16x16x32_bf16 %4, v[88:91], v[72:75], %4\n" \
    "ds_read_b64_tr_b16 v[140:141], %14 offset:30816\n" \
    "v_mfma_f32_16x16x32_bf16 %5, v[88:91], v[76:79], %5\n" \
    "ds_read_b64_tr_b16 v[142:143], %14 offset:32608\n" \
    "v_mfma_f32_16x16x32_bf16 %6, v[92:95], v[72:75], %6\n" \
    "ds_read_b128 v[144:147], %13 offset:30720\n" \
    "v_mfma_f32_16x16x32_bf16 %7, v[92:95], v[76:79], %7\n" \
    "ds_read_b64_tr_b16 v[152:153], %14 offset:37888\n" \
    "v_mfma_f32_16x16x32_bf16 %0, v[104:107], v[96:99], %0\n" \
    "ds_read_b64_tr_b16 v[154:155], %14 offset:39680\n" \
    "v_mfma_f32_16x16x32_bf16 %1, v[104:107], v[100:103], %1\n" \
    "ds_read_b128 v[148:151], %13 offset:32768\n" \
    "v_mfma_f32_16x16x32_bf16 %2, v[108:111], v[96:99], %2\n" \
    "ds_read_b64_tr_b16 v[156:157], %14 offset:37920\n" \
    "v_mfma_f32_16x16x32_bf16 %3, v[108:111], v[100:103], %3\n" \
    "ds_read_b64_tr_b16 v[158:159], %14 offset:39712\n" \
    "v_mfma_f32_16x16x32_bf16 %4, v[112:115], v[96:99], %4\n" \
    "ds_read_b64_tr_b16 v[160:161], %14 offset:37952\n" \
    "v_mfma_f32_16x16x32_bf16 %5, v[112:115], v[100:103], %5\n" \
    "ds_read_b64_tr_b16 v[162:163], %14 offset:39744\n" \
    "v_mfma_f32_16x16x32_bf16 %6, v[116:119], v[96:99], %6\n" \
    "ds_read_b64_tr_b16 v[164:165], %14 offset:37984\n" \
    "v_mfma_f32_16x16x32_bf16 %7, v[116:119], v[100:103], %7\n" \
    "ds_read_b64_tr_b16 v[166:167], %14 offset:39776\n" \
    "s_cmp_lg_u32 %15, 0\n" \
    "s_cbranch_scc1 G1C20_%=\n" \
    "v_mfma_f32_16x16x32_bf16 %8, v[80:83], v[68:71], %8\n" \
    "v_mfma_f32_16x16x32_bf16 %9, v[84:87], v[68:71], %9\n" \
    "v_mfma_f32_16x16x32_bf16 %10, v[88:91], v[68:71], %10\n" \
    "v_mfma_f32_16x16x32_bf16 %11, v[92:95], v[68:71], %11\n" \
    "G1C20_%=:\n" \
    "s_cmp_lg_u32 %15, 1\n" \
    "s_cbranch_scc1 G1C21_%=\n" \
    "v_mfma_f32_16x16x32_bf16 %8, v[104:107], v[68:71], %8\n" \
    "v_mfma_f32_16x16x32_bf16 %9, v[108:111], v[68:71], %9\n" \
    "v_mfma_f32_16x16x32_bf16 %10, v[112:115], v[68:71], %10\n" \
    "v_mfma_f32_16x16x32_bf16 %11, v[116:119], v[68:71], %11\n" \
    "G1C21_%=:\n" \
    "s_waitcnt vmcnt(0) lgkmcnt(0)\n" \
    "s_barrier\n" \
    "v_mfma_f32_16x16x32_bf16 %0, v[128:131], v[120:123], %0\n" \
    "ds_read_b128 v[72:75], v62 offset:0\n" \
    "ds_read_b64_tr_b16 v[80:81], v64 offset:0\n" \
    "v_mfma_f32_16x16x32_bf16 %1, v[128:131], v[124:127], %1\n" \
    "ds_read_b64_tr_b16 v[82:83], v64 offset:1792\n" \
    "ds_read_b128 v[76:79], v62 offset:2048\n" \
    "v_mfma_f32_16x16x32_bf16 %2, v[132:135], v[120:123], %2\n" \
    "ds_read_b64_tr_b16 v[84:85], v64 offset:32\n" \
    "ds_read_b64_tr_b16 v[86:87], v64 offset:1824\n" \
    "v_mfma_f32_16x16x32_bf16 %3, v[132:135], v[124:127], %3\n" \
    "ds_read_b64_tr_b16 v[88:89], v64 offset:64\n" \
    "ds_read_b64_tr_b16 v[90:91], v64 offset:1856\n" \
    "v_mfma_f32_16x16x32_bf16 %4, v[136:139], v[120:123], %4\n" \
    "ds_read_b64_tr_b16 v[92:93], v64 offset:96\n" \
    "v_mfma_f32_16x16x32_bf16 %5, v[136:139], v[124:127], %5\n" \
    "ds_read_b64_tr_b16 v[94:95], v64 offset:1888\n" \
    "v_mfma_f32_16x16x32_bf16 %6, v[140:143], v[120:123], %6\n" \
    "ds_read_b128 v[96:99], v63 offset:0\n" \
    "v_mfma_f32_16x16x32_bf16 %7, v[140:143], v[124:127], %7\n" \
    "ds_read_b64_tr_b16 v[104:105], v64 offset:7168\n" \
    "v_mfma_f32_16x16x32_bf16 %0, v[152:155], v[144:147], %0\n" \
    "ds_read_b64_tr_b16 v[106:107], v64 offset:8960\n" \
    "v_mfma_f32_16x16x32_bf16 %1, v[152:155], v[148:151], %1\n" \
    "ds_read_b128 v[100:103], v63 offset:2048\n" \
    "v_mfma_f32_16x16x32_bf16 %2, v[156:159], v[144:147], %2\n" \
    "ds_read_b64_tr_b16 v[108:109], v64 offset:7200\n" \
    "v_mfma_f32_16x16x32_bf16 %3, v[156:159], v[148:151], %3\n" \
    "ds_read_b64_tr_b16 v[110:111], v64 offset:8992\n" \
    "v_mfma_f32_16x16x32_bf16 %4, v[160:163], v[144:147], %4\n" \
    "ds_read_b64_tr_b16 v[112:113], v64 offset:7232\n" \
    "v_mfma_f32_16x16x32_bf16 %5, v[160:163], v[148:151], %5\n" \
    "ds_read_b64_tr_b16 v[114:115], v64 offset:9024\n" \
    "v_mfma_f32_16x16x32_bf16 %6, v[164:167], v[144:147], %6\n" \
    "ds_read_b64_tr_b16 v[116:117], v64 offset:7264\n" \
    "v_mfma_f32_16x16x32_bf16 %7, v[164:167], v[148:151], %7\n" \
    "ds_read_b64_tr_b16 v[118:119], v64 offset:9056\n" \
    "s_cmp_lg_u32 %15, 2\n" \
    "s_cbranch_scc1 G1C30_%=\n" \
    "v_mfma_f32_16x16x32_bf16 %8, v[128:131], v[68:71], %8\n" \
    "v_mfma_f32_16x16x32_bf16 %9, v[132:135], v[68:71], %9\n" \
    "v_mfma_f32_16x16x32_bf16 %10, v[136:139], v[68:71], %10\n" \
    "v_mfma_f32_16x16x32_bf16 %11, v[140:143], v[68:71], %11\n" \
    "G1C30_%=:\n" \
    "s_cmp_lg_u32 %15, 3\n" \
    "s_cbranch_scc1 G1C31_%=\n" \
    "v_mfma_f32_16x16x32_bf16 %8, v[152:155], v[68:71], %8\n" \
    "v_mfma_f32_16x16x32_bf16 %9, v[156:159], v[68:71], %9\n" \
    "v_mfma_f32_16x16x32_bf16 %10, v[160:163], v[68:71], %10\n" \
    "v_mfma_f32_16x16x32_bf16 %11, v[164:167], v[68:71], %11\n" \
    "G1C31_%=:\n" \
    "s_waitcnt lgkmcnt(0)\n" \
    "s_barrier\n" \
    "v_mfma_f32_16x16x32_bf16 %0, v[80:83], v[72:75], %0\n" \
    "ds_read_b128 v[120:123], v62 offset:30720\n" \
    "ds_read_b64_tr_b16 v[128:129], v64 offset:30720\n" \
    "v_mfma_f32_16x16x32_bf16 %1, v[80:83], v[76:79], %1\n" \
    "ds_read_b64_tr_b16 v[130:131], v64 offset:32512\n" \
    "ds_read_b128 v[124:127], v62 offset:32768\n" \
    "v_mfma_f32_16x16x32_bf16 %2, v[84:87], v[72:75], %2\n" \
    "ds_read_b64_tr_b16 v[132:133], v64 offset:30752\n" \
    "ds_read_b64_tr_b16 v[134:135], v64 offset:32544\n" \
    "v_mfma_f32_16x16x32_bf16 %3, v[84:87], v[76:79], %3\n" \
    "ds_read_b64_tr_b16 v[136:137], v64 offset:30784\n" \
    "ds_read_b64_tr_b16 v[138:139], v64 offset:32576\n" \
    "v_mfma_f32_16x16x32_bf16 %4, v[88:91], v[72:75], %4\n" \
    "ds_read_b64_tr_b16 v[140:141], v64 offset:30816\n" \
    "v_mfma_f32_16x16x32_bf16 %5, v[88:91], v[76:79], %5\n" \
    "ds_read_b64_tr_b16 v[142:143], v64 offset:32608\n" \
    "v_mfma_f32_16x16x32_bf16 %6, v[92:95], v[72:75], %6\n" \
    "ds_read_b128 v[144:147], v63 offset:30720\n" \
    "v_mfma_f32_16x16x32_bf16 %7, v[92:95], v[76:79], %7\n" \
    "ds_read_b64_tr_b16 v[152:153], v64 offset:37888\n" \
    "v_mfma_f32_16x16x32_bf16 %0, v[104:107], v[96:99], %0\n" \
    "ds_read_b64_tr_b16 v[154:155], v64 offset:39680\n" \
    "v_mfma_f32_16x16x32_bf16 %1, v[104:107], v[100:103], %1\n" \
    "ds_read_b128 v[148:151], v63 offset:32768\n" \
    "v_mfma_f32_16x16x32_bf16 %2, v[108:111], v[96:99], %2\n" \
    "ds_read_b64_tr_b16 v[156:157], v64 offset:37920\n" \
    "v_mfma_f32_16x16x32_bf16 %3, v[108:111], v[100:103], %3\n" \
    "ds_read_b64_tr_b16 v[158:159], v64 offset:39712\n" \
    "v_mfma_f32_16x16x32_bf16 %4, v[112:115], v[96:99], %4\n" \
    "ds_read_b64_tr_b16 v[160:161], v64 offset:37952\n" \
    "v_mfma_f32_16x16x32_bf16 %5, v[112:115], v[100:103], %5\n" \
    "ds_read_b64_tr_b16 v[162:163], v64 offset:39744\n" \
    "v_mfma_f32_16x16x32_bf16 %6, v[116:119], v[96:99], %6\n" \
    "ds_read_b64_tr_b16 v[164:165], v64 offset:37984\n" \
    "v_mfma_f32_16x16x32_bf16 %7, v[116:119], v[100:103], %7\n" \
    "ds_read_b64_tr_b16 v[166:167], v64 offset:39776\n" \
    "s_cmp_lg_u32 %15, 0\n" \
    "s_cbranch_scc1 G1C40_%=\n" \
    "v_mfma_f32_16x16x32_bf16 %8, v[80:83], v[68:71], %8\n" \
    "v_mfma_f32_16x16x32_bf16 %9, v[84:87], v[68:71], %9\n" \
    "v_mfma_f32_16x16x32_bf16 %10, v[88:91], v[68:71], %10\n" \
    "v_mfma_f32_16x16x32_bf16 %11, v[92:95], v[68:71], %11\n" \
    "G1C40_%=:\n" \
    "s_cmp_lg_u32 %15, 1\n" \
    "s_cbranch_scc1 G1C41_%=\n" \
    "v_mfma_f32_16x16x32_bf16 %8, v[104:107], v[68:71], %8\n" \
    "v_mfma_f32_16x16x32_bf16 %9, v[108:111], v[68:71], %9\n" \
    "v_mfma_f32_16x16x32_bf16 %10, v[112:115], v[68:71], %10\n" \
    "v_mfma_f32_16x16x32_bf16 %11, v[116:119], v[68:71], %11\n" \
    "G1C41_%=:\n" \
    "s_waitcnt lgkmcnt(0)\n" \
    "v_mfma_f32_16x16x32_bf16 %0, v[128:131], v[120:123], %0\n" \
    "ds_read_b128 v[72:75], v65 offset:0\n" \
    "ds_read_b64_tr_b16 v[80:81], v67 offset:0\n" \
    "v_mfma_f32_16x16x32_bf16 %1, v[128:131], v[124:127], %1\n" \
    "ds_read_b64_tr_b16 v[82:83], v67 offset:1792\n" \
    "ds_read_b128 v[76:79], v65 offset:2048\n" \
    "v_mfma_f32_16x16x32_bf16 %2, v[132:135], v[120:123], %2\n" \
    "ds_read_b64_tr_b16 v[84:85], v67 offset:32\n" \
    "ds_read_b64_tr_b16 v[86:87], v67 offset:1824\n" \
    "v_mfma_f32_16x16x32_bf16 %3, v[132:135], v[124:127], %3\n" \
    "ds_read_b64_tr_b16 v[88:89], v67 offset:64\n" \
    "ds_read_b64_tr_b16 v[90:91], v67 offset:1856\n" \
    "v_mfma_f32_16x16x32_bf16 %4, v[136:139], v[120:123], %4\n" \
    "ds_read_b64_tr_b16 v[92:93], v67 offset:96\n" \
    "v_mfma_f32_16x16x32_bf16 %5, v[136:139], v[124:127], %5\n" \
    "ds_read_b64_tr_b16 v[94:95], v67 offset:1888\n" \
    "v_mfma_f32_16x16x32_bf16 %6, v[140:143], v[120:123], %6\n" \
    "ds_read_b128 v[96:99], v66 offset:0\n" \
    "v_mfma_f32_16x16x32_bf16 %7, v[140:143], v[124:127], %7\n" \
    "ds_read_b64_tr_b16 v[104:105], v67 offset:7168\n" \
    "v_mfma_f32_16x16x32_bf16 %0, v[152:155], v[144:147], %0\n" \
    "ds_read_b64_tr_b16 v[106:107], v67 offset:8960\n" \
    "v_mfma_f32_16x16x32_bf16 %1, v[152:155], v[148:151], %1\n" \
    "ds_read_b128 v[100:103], v66 offset:2048\n" \
    "v_mfma_f32_16x16x32_bf16 %2, v[156:159], v[144:147], %2\n" \
    "ds_read_b64_tr_b16 v[108:109], v67 offset:7200\n" \
    "v_mfma_f32_16x16x32_bf16 %3, v[156:159], v[148:151], %3\n" \
    "ds_read_b64_tr_b16 v[110:111], v67 offset:8992\n" \
    "v_mfma_f32_16x16x32_bf16 %4, v[160:163], v[144:147], %4\n" \
    "ds_read_b64_tr_b16 v[112:113], v67 offset:7232\n" \
    "v_mfma_f32_16x16x32_bf16 %5, v[160:163], v[148:151], %5\n" \
    "ds_read_b64_tr_b16 v[114:115], v67 offset:9024\n" \
    "v_mfma_f32_16x16x32_bf16 %6, v[164:167], v[144:147], %6\n" \
    "ds_read_b64_tr_b16 v[116:117], v67 offset:7264\n" \
    "v_mfma_f32_16x16x32_bf16 %7, v[164:167], v[148:151], %7\n" \
    "ds_read_b64_tr_b16 v[118:119], v67 offset:9056\n" \
    "s_cmp_lg_u32 %15, 2\n" \
    "s_cbranch_scc1 G1C50_%=\n" \
    "v_mfma_f32_16x16x32_bf16 %8, v[128:131], v[68:71], %8\n" \
    "v_mfma_f32_16x16x32_bf16 %9, v[132:135], v[68:71], %9\n" \
    "v_mfma_f32_16x16x32_bf16 %10, v[136:139], v[68:71], %10\n" \
    "v_mfma_f32_16x16x32_bf16 %11, v[140:143], v[68:71], %11\n" \
    "G1C50_%=:\n" \
    "s_cmp_lg_u32 %15, 3\n" \
    "s_cbranch_scc1 G1C51_%=\n" \
    "v_mfma_f32_16x16x32_bf16 %8, v[152:155], v[68:71], %8\n" \
    "v_mfma_f32_16x16x32_bf16 %9, v[156:159], v[68:71], %9\n" \
    "v_mfma_f32_16x16x32_bf16 %10, v[160:163], v[68:71], %10\n" \
    "v_mfma_f32_16x16x32_bf16 %11, v[164:167], v[68:71], %11\n" \
    "G1C51_%=:\n" \
    "s_waitcnt lgkmcnt(0)\n" \
    "v_mfma_f32_16x16x32_bf16 %0, v[80:83], v[72:75], %0\n" \
    "ds_read_b128 v[120:123], %12 offset:0\n" \
    "ds_read_b64_tr_b16 v[128:129], %14 offset:0\n" \
    "v_mfma_f32_16x16x32_bf16 %1, v[80:83], v[76:79], %1\n" \
    "ds_read_b64_tr_b16 v[130:131], %14 offset:1792\n" \
    "ds_read_b128 v[124:127], %12 offset:2048\n" \
    "v_mfma_f32_16x16x32_bf16 %2, v[84:87], v[72:75], %2\n" \
    "ds_read_b64_tr_b16 v[132:133], %14 offset:32\n" \
    "ds_read_b64_tr_b16 v[134:135], %14 offset:1824\n" \
    "v_mfma_f32_16x16x32_bf16 %3, v[84:87], v[76:79], %3\n" \
    "ds_read_b64_tr_b16 v[136:137], %14 offset:64\n" \
    "ds_read_b64_tr_b16 v[138:139], %14 offset:1856\n" \
    "v_mfma_f32_16x16x32_bf16 %4, v[88:91], v[72:75], %4\n" \
    "ds_read_b64_tr_b16 v[140:141], %14 offset:96\n" \
    "v_mfma_f32_16x16x32_bf16 %5, v[88:91], v[76:79], %5\n" \
    "ds_read_b64_tr_b16 v[142:143], %14 offset:1888\n" \
    "v_mfma_f32_16x16x32_bf16 %6, v[92:95], v[72:75], %6\n" \
    "ds_read_b128 v[144:147], %13 offset:0\n" \
    "v_mfma_f32_16x16x32_bf16 %7, v[92:95], v[76:79], %7\n" \
    "ds_read_b64_tr_b16 v[152:153], %14 offset:7168\n" \
    "v_mfma_f32_16x16x32_bf16 %0, v[104:107], v[96:99], %0\n" \
    "ds_read_b64_tr_b16 v[154:155], %14 offset:8960\n" \
    "v_mfma_f32_16x16x32_bf16 %1, v[104:107], v[100:103], %1\n" \
    "ds_read_b128 v[148:151], %13 offset:2048\n" \
    "v_mfma_f32_16x16x32_bf16 %2, v[108:111], v[96:99], %2\n" \
    "ds_read_b64_tr_b16 v[156:157], %14 offset:7200\n" \
    "v_mfma_f32_16x16x32_bf16 %3, v[108:111], v[100:103], %3\n" \
    "ds_read_b64_tr_b16 v[158:159], %14 offset:8992\n" \
    "v_mfma_f32_16x16x32_bf16 %4, v[112:115], v[96:99], %4\n" \
    "ds_read_b64_tr_b16 v[160:161], %14 offset:7232\n" \
    "v_mfma_f32_16x16x32_bf16 %5, v[112:115], v[100:103], %5\n" \
    "ds_read_b64_tr_b16 v[162:163], %14 offset:9024\n" \
    "v_mfma_f32_16x16x32_bf16 %6, v[116:119], v[96:99], %6\n" \
    "ds_read_b64_tr_b16 v[164:165], %14 offset:7264\n" \
    "v_mfma_f32_16x16x32_bf16 %7, v[116:119], v[100:103], %7\n" \
    "ds_read_b64_tr_b16 v[166:167], %14 offset:9056\n" \
    "s_cmp_lg_u32 %15, 0\n" \
    "s_cbranch_scc1 G1C60_%=\n" \
    "v_mfma_f32_16x16x32_bf16 %8, v[80:83], v[68:71], %8\n" \
    "v_mfma_f32_16x16x32_bf16 %9, v[84:87], v[68:71], %9\n" \
    "v_mfma_f32_16x16x32_bf16 %10, v[88:91], v[68:71], %10\n" \
    "v_mfma_f32_16x16x32_bf16 %11, v[92:95], v[68:71], %11\n" \
    "G1C60_%=:\n" \
    "s_cmp_lg_u32 %15, 1\n" \
    "s_cbranch_scc1 G1C61_%=\n" \
    "v_mfma_f32_16x16x32_bf16 %8, v[104:107], v[68:71], %8\n" \
    "v_mfma_f32_16x16x32_bf16 %9, v[108:111], v[68:71], %9\n" \
    "v_mfma_f32_16x16x32_bf16 %10, v[112:115], v[68:71], %10\n" \
    "v_mfma_f32_16x16x32_bf16 %11, v[116:119], v[68:71], %11\n" \
    "G1C61_%=:\n" \
    "s_waitcnt lgkmcnt(0)\n" \
    "v_mfma_f32_16x16x32_bf16 %0, v[128:131], v[120:123], %0\n" \
    "v_mfma_f32_16x16x32_bf16 %1, v[128:131], v[124:127], %1\n" \
    "v_mfma_f32_16x16x32_bf16 %2, v[132:135], v[120:123], %2\n" \
    "v_mfma_f32_16x16x32_bf16 %3, v[132:135], v[124:127], %3\n" \
    "v_mfma_f32_16x16x32_bf16 %4, v[136:139], v[120:123], %4\n" \
    "v_mfma_f32_16x16x32_bf16 %5, v[136:139], v[124:127], %5\n" \
    "v_mfma_f32_16x16x32_bf16 %6, v[140:143], v[120:123], %6\n" \
    "v_mfma_f32_16x16x32_bf16 %7, v[140:143], v[124:127], %7\n" \
    "v_mfma_f32_16x16x32_bf16 %0, v[152:155], v[144:147], %0\n" \
    "v_mfma_f32_16x16x32_bf16 %1, v[152:155], v[148:151], %1\n" \
    "v_mfma_f32_16x16x32_bf16 %2, v[156:159], v[144:147], %2\n" \
    "v_mfma_f32_16x16x32_bf16 %3, v[156:159], v[148:151], %3\n" \
    "v_mfma_f32_16x16x32_bf16 %4, v[160:163], v[144:147], %4\n" \
    "v_mfma_f32_16x16x32_bf16 %5, v[160:163], v[148:151], %5\n" \
    "v_mfma_f32_16x16x32_bf16 %6, v[164:167], v[144:147], %6\n" \
    "v_mfma_f32_16x16x32_bf16 %7, v[164:167], v[148:151], %7\n" \
    "s_cmp_lg_u32 %15, 2\n" \
    "s_cbranch_scc1 G1C70_%=\n" \
    "v_mfma_f32_16x16x32_bf16 %8, v[128:131], v[68:71], %8\n" \
    "v_mfma_f32_16x16x32_bf16 %9, v[132:135], v[68:71], %9\n" \
    "v_mfma_f32_16x16x32_bf16 %10, v[136:139], v[68:71], %10\n" \
    "v_mfma_f32_16x16x32_bf16 %11, v[140:143], v[68:71], %11\n" \
    "G1C70_%=:\n" \
    "s_cmp_lg_u32 %15, 3\n" \
    "s_cbranch_scc1 G1C71_%=\n" \
    "v_mfma_f32_16x16x32_bf16 %8, v[152:155], v[68:71], %8\n" \
    "v_mfma_f32_16x16x32_bf16 %9, v[156:159], v[68:71], %9\n" \
    "v_mfma_f32_16x16x32_bf16 %10, v[160:163], v[68:71], %10\n" \
    "v_mfma_f32_16x16x32_bf16 %11, v[164:167], v[68:71], %11\n" \
    "G1C71_%=:\n" \
    "s_nop 7\n" \
    ""
#define G1_FFN0_CLOB "v62", "v63", "v64", "v65", "v66", "v67", "v68", "v69", "v70", "v71", "v72", "v73", "v74", "v75", "v76", "v77", "v78", "v79", "v80", "v81", "v82", "v83", "v84", "v85", "v86", "v87", "v88", "v89", "v90", "v91", "v92", "v93", "v94", "v95", "v96", "v97", "v98", "v99", "v100", "v101", "v102", "v103", "v104", "v105", "v106", "v107", "v108", "v109", "v110", "v111", "v112", "v113", "v114", "v115", "v116", "v117", "v118", "v119", "v120", "v121", "v122", "v123", "v124", "v125", "v126", "v127", "v128", "v129", "v130", "v131", "v132", "v133", "v134", "v135", "v136", "v137", "v138", "v139", "v140", "v141", "v142", "v143", "v144", "v145", "v146", "v147", "v148", "v149", "v150", "v151", "v152", "v153", "v154", "v155", "v156", "v157", "v158", "v159", "v160", "v161", "v162", "v163", "v164", "v165", "v166", "v167"
#define G1_FFN1 \
    "v_add_u32 v78, 61440, %9\n" \
    "v_add_u32 v79, 61440, %10\n" \
    "v_add_u32 v80, 61440, %11\n" \
    "v_add_u32 v81, 122880, %9\n" \
    "v_add_u32 v82, 122880, %10\n" \
    "v_add_u32 v83, 122880, %11\n" \
    "v_mov_b32 v84, 0x3f803f80\n" \
    "v_mov_b32 v85, 0x3f803f80\n" \
    "v_mov_b32 v86, 0x3f803f80\n" \
    "v_mov_b32 v87, 0x3f803f80\n" \
    "ds_read_b128 v[88:91], v78 offset:30720\n" \
    "ds_read_b64_tr_b16 v[96:97], v80 offset:30720\n" \
    "ds_read_b64_tr_b16 v[98:99], v80 offset:32512\n" \
    "ds_read_b128 v[92:95], v78 offset:32768\n" \
    "ds_read_b64_tr_b16 v[100:101], v80 offset:30752\n" \
    "ds_read_b64_tr_b16 v[102:103], v80 offset:32544\n" \
    "ds_read_b64_tr_b16 v[104:105], v80 offset:30784\n" \
    "ds_read_b64_tr_b16 v[106:107], v80 offset:32576\n" \
    "ds_read_b128 v[108:111], v79 offset:30720\n" \
    "ds_read_b64_tr_b16 v[116:117], v80 offset:37888\n" \
    "ds_read_b64_tr_b16 v[118:119], v80 offset:39680\n" \
    "ds_read_b128 v[112:115], v79 offset:32768\n" \
    "ds_read_b64_tr_b16 v[120:121], v80 offset:37920\n" \
    "ds_read_b64_tr_b16 v[122:123], v80 offset:39712\n" \
    "ds_read_b64_tr_b16 v[124:125], v80 offset:37952\n" \
    "ds_read_b64_tr_b16 v[126:127], v80 offset:39744\n" \
    "s_waitcnt lgkmcnt(0)\n" \
    "s_barrier\n" \
    "ds_read_b128 v[128:131], v81 offset:0\n" \
    "ds_read_b64_tr_b16 v[136:137], v83 offset:0\n" \
    "v_mfma_f32_16x16x32_bf16 %0, v[96:99], v[88:91], 0\n" \
    "ds_read_b64_tr_b16 v[138:139], v83 offset:1792\n" \
    "ds_read_b128 v[132:135], v81 offset:2048\n" \
    "v_mfma_f32_16x16x32_bf16 %1, v[96:99], v[92:95], 0\n" \
    "ds_read_b64_tr_b16 v[140:141], v83 offset:32\n" \
    "ds_read_b64_tr_b16 v[142:143], v83 offset:1824\n" \
    "v_mfma_f32_16x16x32_bf16 %2, v[100:103], v[88:91], 0\n" \
    "ds_read_b64_tr_b16 v[144:145], v83 offset:64\n" \
    "ds_read_b64_tr_b16 v[146:147], v83 offset:1856\n" \
    "v_mfma_f32_16x16x32_bf16 %3, v[100:103], v[92:95], 0\n" \
    "ds_read_b128 v[148:151], v82 offset:0\n" \
    "v_mfma_f32_16x16x32_bf16 %4, v[104:107], v[88:91], 0\n" \
    "ds_read_b64_tr_b16 v[156:157], v83 offset:7168\n" \
    "v_mfma_f32_16x16x32_bf16 %5, v[104:107], v[92:95], 0\n" \
    "ds_read_b64_tr_b16 v[158:159], v83 offset:8960\n" \
    "v_mfma_f32_16x16x32_bf16 %0, v[116:119], v[108:111], %0\n" \
    "ds_read_b128 v[152:155], v82 offset:2048\n" \
    "v_mfma_f32_16x16x32_bf16 %1, v[116:119], v[112:115], %1\n" \
    "ds_read_b64_tr_b16 v[160:161], v83 offset:7200\n" \
    "v_mfma_f32_16x16x32_bf16 %2, v[120:123], v[108:111], %2\n" \
    "ds_read_b64_tr_b16 v[162:163], v83 offset:8992\n" \
    "v_mfma_f32_16x16x32_bf16 %3, v[120:123], v[112:115], %3\n" \
    "ds_read_b64_tr_b16 v[164:165], v83 offset:7232\n" \
    "v_mfma_f32_16x16x32_bf16 %4, v[124:127], v[108:111], %4\n" \
    "ds_read_b64_tr_b16 v[166:167], v83 offset:9024\n" \
    "v_mfma_f32_16x16x32_bf16 %5, v[124:127], v[112:115], %5\n" \
    "s_cmp_lg_u32 %12, 0\n" \
    "s_cbranch_scc1 G1C00_%=\n" \
    "v_mfma_f32_16x16x32_bf16 %6, v[96:99], v[84:87], %6\n" \
    "v_mfma_f32_16x16x32_bf16 %7, v[100:103], v[84:87], %7\n" \
    "v_mfma_f32_16x16x32_bf16 %8, v[104:107], v[84:87], %8\n" \
    "G1C00_%=:\n" \
    "s_cmp_lg_u32 %12, 1\n" \
    "s_cbranch_scc1 G1C01_%=\n" \
    "v_mfma_f32_16x16x32_bf16 %6, v[116:119], v[84:87], %6\n" \
    "v_mfma_f32_16x16x32_bf16 %7, v[120:123], v[84:87], %7\n" \
    "v_mfma_f32_16x16x32_bf16 %8, v[124:127], v[84:87], %8\n" \
    "G1C01_%=:\n" \
    "s_waitcnt lgkmcnt(0)\n" \
    "s_barrier\n" \
    "ds_read_b128 v[88:91], %9 offset:0\n" \
    "ds_read_b64_tr_b16 v[96:97], %11 offset:0\n" \
    "v_mfma_f32_16x16x32_bf16 %0, v[136:139], v[128:131], %0\n" \
    "ds_read_b64_tr_b16 v[98:99], %11 offset:1792\n" \
    "ds_read_b128 v[92:95], %9 offset:2048\n" \
    "v_mfma_f32_16x16x32_bf16 %1, v[136:139], v[132:135], %1\n" \
    "ds_read_b64_tr_b16 v[100:101], %11 offset:32\n" \
    "ds_read_b64_tr_b16 v[102:103], %11 offset:1824\n" \
    "v_mfma_f32_16x16x32_bf16 %2, v[140:143], v[128:131], %2\n" \
    "ds_read_b64_tr_b16 v[104:105], %11 offset:64\n" \
    "ds_read_b64_tr_b16 v[106:107], %11 offset:1856\n" \
    "v_mfma_f32_16x16x32_bf16 %3, v[140:143], v[132:135], %3\n" \
    "ds_read_b128 v[108:111], %10 offset:0\n" \
    "v_mfma_f32_16x16x32_bf16 %4, v[144:147], v[128:131], %4\n" \
    "ds_read_b64_tr_b16 v[116:117], %11 offset:7168\n" \
    "v_mfma_f32_16x16x32_bf16 %5, v[144:147], v[132:135], %5\n" \
    "ds_read_b64_tr_b16 v[118:119], %11 offset:8960\n" \
    "v_mfma_f32_16x16x32_bf16 %0, v[156:159], v[148:151], %0\n" \
    "ds_read_b128 v[112:115], %10 offset:2048\n" \
    "v_mfma_f32_16x16x32_bf16 %1, v[156:159], v[152:155], %1\n" \
    "ds_read_b64_tr_b16 v[120:121], %11 offset:7200\n" \
    "v_mfma_f32_16x16x32_bf16 %2, v[160:163], v[148:151], %2\n" \
    "ds_read_b64_tr_b16 v[122:123], %11 offset:8992\n" \
    "v_mfma_f32_16x16x32_bf16 %3, v[160:163], v[152:155], %3\n" \
    "ds_read_b64_tr_b16 v[124:125], %11 offset:7232\n" \
    "v_mfma_f32_16x16x32_bf16 %4, v[164:167], v[148:151], %4\n" \
    "ds_read_b64_tr_b16 v[126:127], %11 offset:9024\n" \
    "v_mfma_f32_16x16x32_bf16 %5, v[164:167], v[152:155], %5\n" \
    "s_cmp_lg_u32 %12, 2\n" \
    "s_cbranch_scc1 G1C10_%=\n" \
    "v_mfma_f32_16x16x32_bf16 %6, v[136:139], v[84:87], %6\n" \
    "v_mfma_f32_16x16x32_bf16 %7, v[140:143], v[84:87], %7\n" \
    "v_mfma_f32_16x16x32_bf16 %8, v[144:147], v[84:87], %8\n" \
    "G1C10_%=:\n" \
    "s_cmp_lg_u32 %12, 3\n" \
    "s_cbranch_scc1 G1C11_%=\n" \
    "v_mfma_f32_16x16x32_bf16 %6, v[156:159], v[84:87], %6\n" \
    "v_mfma_f32_16x16x32_bf16 %7, v[160:163], v[84:87], %7\n" \
    "v_mfma_f32_16x16x32_bf16 %8, v[164:167], v[84:87], %8\n" \
    "G1C11_%=:\n" \
    "s_waitcnt lgkmcnt(0)\n" \
    "s_barrier\n" \
    "ds_read_b128 v[128:131], %9 offset:30720\n" \
    "ds_read_b64_tr_b16 v[136:137], %11 offset:30720\n" \
    "v_mfma_f32_16x16x32_bf16 %0, v[96:99], v[88:91], %0\n" \
    "ds_read_b64_tr_b16 v[138:139], %11 offset:32512\n" \
    "ds_read_b128 v[132:135], %9 offset:32768\n" \
    "v_mfma_f32_16x16x32_bf16 %1, v[96:99], v[92:95], %1\n" \
    "ds_read_b64_tr_b16 v[140:141], %11 offset:30752\n" \
    "ds_read_b64_tr_b16 v[142:143], %11 offset:32544\n" \
    "v_mfma_f32_16x16x32_bf16 %2, v[100:103], v[88:91], %2\n" \
    "ds_read_b64_tr_b16 v[144:145], %11 offset:30784\n" \
    "ds_read_b64_tr_b16 v[146:147], %11 offset:32576\n" \
    "v_mfma_f32_16x16x32_bf16 %3, v[100:103], v[92:95], %3\n" \
    "ds_read_b128 v[148:151], %10 offset:30720\n" \
    "v_mfma_f32_16x16x32_bf16 %4, v[104:107], v[88:91], %4\n" \
    "ds_read_b64_tr_b16 v[156:157], %11 offset:37888\n" \
    "v_mfma_f32_16x16x32_bf16 %5, v[104:107], v[92:95], %5\n" \
    "ds_read_b64_tr_b16 v[158:159], %11 offset:39680\n" \
    "v_mfma_f32_16x16x32_bf16 %0, v[116:119], v[108:111], %0\n" \
    "ds_read_b128 v[152:155], %10 offset:32768\n" \
    "v_mfma_f32_16x16x32_bf16 %1, v[116:119], v[112:115], %1\n" \
    "ds_read_b64_tr_b16 v[160:161], %11 offset:37920\n" \
    "v_mfma_f32_16x16x32_bf16 %2, v[120:123], v[108:111], %2\n" \
    "ds_read_b64_tr_b16 v[162:163], %11 offset:39712\n" \
    "v_mfma_f32_16x16x32_bf16 %3, v[120:123], v[112:115], %3\n" \
    "ds_read_b64_tr_b16 v[164:165], %11 offset:37952\n" \
    "v_mfma_f32_16x16x32_bf16 %4, v[124:127], v[108:111], %4\n" \
    "ds_read_b64_tr_b16 v[166:167], %11 offset:39744\n" \
    "v_mfma_f32_16x16x32_bf16 %5, v[124:127], v[112:115], %5\n" \
    "s_cmp_lg_u32 %12, 0\n" \
    "s_cbranch_scc1 G1C20_%=\n" \
    "v_mfma_f32_16x16x32_bf16 %6, v[96:99], v[84:87], %6\n" \
    "v_mfma_f32_16x16x32_bf16 %7, v[100:103], v[84:87], %7\n" \
    "v_mfma_f32_16x16x32_bf16 %8, v[104:107], v[84:87], %8\n" \
    "G1C20_%=:\n" \
    "s_cmp_lg_u32 %12, 1\n" \
    "s_cbranch_scc1 G1C21_%=\n" \
    "v_mfma_f32_16x16x32_bf16 %6, v[116:119], v[84:87], %6\n" \
    "v_mfma_f32_16x16x32_bf16 %7, v[120:123], v[84:87], %7\n" \
    "v_mfma_f32_16x16x32_bf16 %8, v[124:127], v[84:87], %8\n" \
    "G1C21_%=:\n" \
    "s_waitcnt vmcnt(0) lgkmcnt(0)\n" \
    "s_barrier\n" \
    "ds_read_b128 v[88:91], v78 offset:0\n" \
    "ds_read_b64_tr_b16 v[96:97], v80 offset:0\n" \
    "v_mfma_f32_16x16x32_bf16 %0, v[136:139], v[128:131], %0\n" \
    "ds_read_b64_tr_b16 v[98:99], v80 offset:1792\n" \
    "ds_read_b128 v[92:95], v78 offset:2048\n" \
    "v_mfma_f32_16x16x32_bf16 %1, v[136:139], v[132:135], %1\n" \
    "ds_read_b64_tr_b16 v[100:101], v80 offset:32\n" \
    "ds_read_b64_tr_b16 v[102:103], v80 offset:1824\n" \
    "v_mfma_f32_16x16x32_bf16 %2, v[140:143], v[128:131], %2\n" \
    "ds_read_b64_tr_b16 v[104:105], v80 offset:64\n" \
    "ds_read_b64_tr_b16 v[106:107], v80 offset:1856\n" \
    "v_mfma_f32_16x16x32_bf16 %3, v[140:143], v[132:135], %3\n" \
    "ds_read_b128 v[108:111], v79 offset:0\n" \
    "v_mfma_f32_16x16x32_bf16 %4, v[144:147], v[128:131], %4\n" \
    "ds_read_b64_tr_b16 v[116:117], v80 offset:7168\n" \
    "v_mfma_f32_16x16x32_bf16 %5, v[144:147], v[132:135], %5\n" \
    "ds_read_b64_tr_b16 v[118:119], v80 offset:8960\n" \
    "v_mfma_f32_16x16x32_bf16 %0, v[156:159], v[148:151], %0\n" \
    "ds_read_b128 v[112:115], v79 offset:2048\n" \
    "v_mfma_f32_16x16x32_bf16 %1, v[156:159], v[152:155], %1\n" \
    "ds_read_b64_tr_b16 v[120:121], v80 offset:7200\n" \
    "v_mfma_f32_16x16x32_bf16 %2, v[160:163], v[148:151], %2\n" \
    "ds_read_b64_tr_b16 v[122:123], v80 offset:8992\n" \
    "v_mfma_f32_16x16x32_bf16 %3, v[160:163], v[152:155], %3\n" \
    "ds_read_b64_tr_b16 v[124:125], v80 offset:7232\n" \
    "v_mfma_f32_16x16x32_bf16 %4, v[164:167], v[148:151], %4\n" \
    "ds_read_b64_tr_b16 v[126:127], v80 offset:9024\n" \
    "v_mfma_f32_16x16x32_bf16 %5, v[164:167], v[152:155], %5\n" \
    "s_cmp_lg_u32 %12, 2\n" \
    "s_cbranch_scc1 G1C30_%=\n" \
    "v_mfma_f32_16x16x32_bf16 %6, v[136:139], v[84:87], %6\n" \
    "v_mfma_f32_16x16x32_bf16 %7, v[140:143], v[84:87], %7\n" \
    "v_mfma_f32_16x16x32_bf16 %8, v[144:147], v[84:87], %8\n" \
    "G1C30_%=:\n" \
    "s_cmp_lg_u32 %12, 3\n" \
    "s_cbranch_scc1 G1C31_%=\n" \
    "v_mfma_f32_16x16x32_bf16 %6, v[156:159], v[84:87], %6\n" \
    "v_mfma_f32_16x16x32_bf16 %7, v[160:163], v[84:87], %7\n" \
    "v_mfma_f32_16x16x32_bf16 %8, v[164:167], v[84:87], %8\n" \
    "G1C31_%=:\n" \
    "s_waitcnt lgkmcnt(0)\n" \
    "s_barrier\n" \
    "ds_read_b128 v[128:131], v78 offset:30720\n" \
    "ds_read_b64_tr_b16 v[136:137], v80 offset:30720\n" \
    "v_mfma_f32_16x16x32_bf16 %0, v[96:99], v[88:91], %0\n" \
    "ds_read_b64_tr_b16 v[138:139], v80 offset:32512\n" \
    "ds_read_b128 v[132:135], v78 offset:32768\n" \
    "v_mfma_f32_16x16x32_bf16 %1, v[96:99], v[92:95], %1\n" \
    "ds_read_b64_tr_b16 v[140:141], v80 offset:30752\n" \
    "ds_read_b64_tr_b16 v[142:143], v80 offset:32544\n" \
    "v_mfma_f32_16x16x32_bf16 %2, v[100:103], v[88:91], %2\n" \
    "ds_read_b64_tr_b16 v[144:145], v80 offset:30784\n" \
    "ds_read_b64_tr_b16 v[146:147], v80 offset:32576\n" \
    "v_mfma_f32_16x16x32_bf16 %3, v[100:103], v[92:95], %3\n" \
    "ds_read_b128 v[148:151], v79 offset:30720\n" \
    "v_mfma_f32_16x16x32_bf16 %4, v[104:107], v[88:91], %4\n" \
    "ds_read_b64_tr_b16 v[156:157], v80 offset:37888\n" \
    "v_mfma_f32_16x16x32_bf16 %5, v[104:107], v[92:95], %5\n" \
    "ds_read_b64_tr_b16 v[158:159], v80 offset:39680\n" \
    "v_mfma_f32_16x16x32_bf16 %0, v[116:119], v[108:111], %0\n" \
    "ds_read_b128 v[152:155], v79 offset:32768\n" \
    "v_mfma_f32_16x16x32_bf16 %1, v[116:119], v[112:115], %1\n" \
    "ds_read_b64_tr_b16 v[160:161], v80 offset:37920\n" \
    "v_mfma_f32_16x16x32_bf16 %2, v[120:123], v[108:111], %2\n" \
    "ds_read_b64_tr_b16 v[162:163], v80 offset:39712\n" \
    "v_mfma_f32_16x16x32_bf16 %3, v[120:123], v[112:115], %3\n" \
    "ds_read_b64_tr_b16 v[164:165], v80 offset:37952\n" \
    "v_mfma_f32_16x16x32_bf16 %4, v[124:127], v[108:111], %4\n" \
    "ds_read_b64_tr_b16 v[166:167], v80 offset:39744\n" \
    "v_mfma_f32_16x16x32_bf16 %5, v[124:127], v[112:115], %5\n" \
    "s_cmp_lg_u32 %12, 0\n" \
    "s_cbranch_scc1 G1C40_%=\n" \
    "v_mfma_f32_16x16x32_bf16 %6, v[96:99], v[84:87], %6\n" \
    "v_mfma_f32_16x16x32_bf16 %7, v[100:103], v[84:87], %7\n" \
    "v_mfma_f32_16x16x32_bf16 %8, v[104:107], v[84:87], %8\n" \
    "G1C40_%=:\n" \
    "s_cmp_lg_u32 %12, 1\n" \
    "s_cbranch_scc1 G1C41_%=\n" \
    "v_mfma_f32_16x16x32_bf16 %6, v[116:119], v[84:87], %6\n" \
    "v_mfma_f32_16x16x32_bf16 %7, v[120:123], v[84:87], %7\n" \
    "v_mfma_f32_16x16x32_bf16 %8, v[124:127], v[84:87], %8\n" \
    "G1C41_%=:\n" \
    "s_waitcnt lgkmcnt(0)\n" \
    "ds_read_b128 v[88:91], v81 offset:0\n" \
    "ds_read_b64_tr_b16 v[96:97], v83 offset:0\n" \
    "v_mfma_f32_16x16x32_bf16 %0, v[136:139], v[128:131], %0\n" \
    "ds_read_b64_tr_b16 v[98:99], v83 offset:1792\n" \
    "ds_read_b128 v[92:95], v81 offset:2048\n" \
    "v_mfma_f32_16x16x32_bf16 %1, v[136:139], v[132:135], %1\n" \
    "ds_read_b64_tr_b16 v[100:101], v83 offset:32\n" \
    "ds_read_b64_tr_b16 v[102:103], v83 offset:1824\n" \
    "v_mfma_f32_16x16x32_bf16 %2, v[140:143], v[128:131], %2\n" \
    "ds_read_b64_tr_b16 v[104:105], v83 offset:64\n" \
    "ds_read_b64_tr_b16 v[106:107], v83 offset:1856\n" \
    "v_mfma_f32_16x16x32_bf16 %3, v[140:143], v[132:135], %3\n" \
    "ds_read_b128 v[108:111], v82 offset:0\n" \
    "v_mfma_f32_16x16x32_bf16 %4, v[144:147], v[128:131], %4\n" \
    "ds_read_b64_tr_b16 v[116:117], v83 offset:7168\n" \
    "v_mfma_f32_16x16x32_bf16 %5, v[144:147], v[132:135], %5\n" \
    "ds_read_b64_tr_b16 v[118:119], v83 offset:8960\n" \
    "v_mfma_f32_16x16x32_bf16 %0, v[156:159], v[148:151], %0\n" \
    "ds_read_b128 v[112:115], v82 offset:2048\n" \
    "v_mfma_f32_16x16x32_bf16 %1, v[156:159], v[152:155], %1\n" \
    "ds_read_b64_tr_b16 v[120:121], v83 offset:7200\n" \
    "v_mfma_f32_16x16x32_bf16 %2, v[160:163], v[148:151], %2\n" \
    "ds_read_b64_tr_b16 v[122:123], v83 offset:8992\n" \
    "v_mfma_f32_16x16x32_bf16 %3, v[160:163], v[152:155], %3\n" \
    "ds_read_b64_tr_b16 v[124:125], v83 offset:7232\n" \
    "v_mfma_f32_16x16x32_bf16 %4, v[164:167], v[148:151], %4\n" \
    "ds_read_b64_tr_b16 v[126:127], v83 offset:9024\n" \
    "v_mfma_f32_16x16x32_bf16 %5, v[164:167], v[152:155], %5\n" \
    "s_cmp_lg_u32 %12, 2\n" \
    "s_cbranch_scc1 G1C50_%=\n" \
    "v_mfma_f32_16x16x32_bf16 %6, v[136:139], v[84:87], %6\n" \
    "v_mfma_f32_16x16x32_bf16 %7, v[140:143], v[84:87], %7\n" \
    "v_mfma_f32_16x16x32_bf16 %8, v[144:147], v[84:87], %8\n" \
    "G1C50_%=:\n" \
    "s_cmp_lg_u32 %12, 3\n" \
    "s_cbranch_scc1 G1C51_%=\n" \
    "v_mfma_f32_16x16x32_bf16 %6, v[156:159], v[84:87], %6\n" \
    "v_mfma_f32_16x16x32_bf16 %7, v[160:163], v[84:87], %7\n" \
    "v_mfma_f32_16x16x32_bf16 %8, v[164:167], v[84:87], %8\n" \
    "G1C51_%=:\n" \
    "s_waitcnt lgkmcnt(0)\n" \
    "ds_read_b128 v[128:131], %9 offset:0\n" \
    "ds_read_b64_tr_b16 v[136:137], %11 offset:0\n" \
    "v_mfma_f32_16x16x32_bf16 %0, v[96:99], v[88:91], %0\n" \
    "ds_read_b64_tr_b16 v[138:139], %11 offset:1792\n" \
    "ds_read_b128 v[132:135], %9 offset:2048\n" \
    "v_mfma_f32_16x16x32_bf16 %1, v[96:99], v[92:95], %1\n" \
    "ds_read_b64_tr_b16 v[140:141], %11 offset:32\n" \
    "ds_read_b64_tr_b16 v[142:143], %11 offset:1824\n" \
    "v_mfma_f32_16x16x32_bf16 %2, v[100:103], v[88:91], %2\n" \
    "ds_read_b64_tr_b16 v[144:145], %11 offset:64\n" \
    "ds_read_b64_tr_b16 v[146:147], %11 offset:1856\n" \
    "v_mfma_f32_16x16x32_bf16 %3, v[100:103], v[92:95], %3\n" \
    "ds_read_b128 v[148:151], %10 offset:0\n" \
    "v_mfma_f32_16x16x32_bf16 %4, v[104:107], v[88:91], %4\n" \
    "ds_read_b64_tr_b16 v[156:157], %11 offset:7168\n" \
    "v_mfma_f32_16x16x32_bf16 %5, v[104:107], v[92:95], %5\n" \
    "ds_read_b64_tr_b16 v[158:159], %11 offset:8960\n" \
    "v_mfma_f32_16x16x32_bf16 %0, v[116:119], v[108:111], %0\n" \
    "ds_read_b128 v[152:155], %10 offset:2048\n" \
    "v_mfma_f32_16x16x32_bf16 %1, v[116:119], v[112:115], %1\n" \
    "ds_read_b64_tr_b16 v[160:161], %11 offset:7200\n" \
    "v_mfma_f32_16x16x32_bf16 %2, v[120:123], v[108:111], %2\n" \
    "ds_read_b64_tr_b16 v[162:163], %11 offset:8992\n" \
    "v_mfma_f32_16x16x32_bf16 %3, v[120:123], v[112:115], %3\n" \
    "ds_read_b64_tr_b16 v[164:165], %11 offset:7232\n" \
    "v_mfma_f32_16x16x32_bf16 %4, v[124:127], v[108:111], %4\n" \
    "ds_read_b64_tr_b16 v[166:167], %11 offset:9024\n" \
    "v_mfma_f32_16x16x32_bf16 %5, v[124:127], v[112:115], %5\n" \
    "s_cmp_lg_u32 %12, 0\n" \
    "s_cbranch_scc1 G1C60_%=\n" \
    "v_mfma_f32_16x16x32_bf16 %6, v[96:99], v[84:87], %6\n" \
    "v_mfma_f32_16x16x32_bf16 %7, v[100:103], v[84:87], %7\n" \
    "v_mfma_f32_16x16x32_bf16 %8, v[104:107], v[84:87], %8\n" \
    "G1C60_%=:\n" \
    "s_cmp_lg_u32 %12, 1\n" \
    "s_cbranch_scc1 G1C61_%=\n" \
    "v_mfma_f32_16x16x32_bf16 %6, v[116:119], v[84:87], %6\n" \
    "v_mfma_f32_16x16x32_bf16 %7, v[120:123], v[84:87], %7\n" \
    "v_mfma_f32_16x16x32_bf16 %8, v[124:127], v[84:87], %8\n" \
    "G1C61_%=:\n" \
    "s_waitcnt lgkmcnt(0)\n" \
    "v_mfma_f32_16x16x32_bf16 %0, v[136:139], v[128:131], %0\n" \
    "v_mfma_f32_16x16x32_bf16 %1, v[136:139], v[132:135], %1\n" \
    "v_mfma_f32_16x16x32_bf16 %2, v[140:143], v[128:131], %2\n" \
    "v_mfma_f32_16x16x32_bf16 %3, v[140:143], v[132:135], %3\n" \
    "v_mfma_f32_16x16x32_bf16 %4, v[144:147], v[128:131], %4\n" \
    "v_mfma_f32_16x16x32_bf16 %5, v[144:147], v[132:135], %5\n" \
    "v_mfma_f32_16x16x32_bf16 %0, v[156:159], v[148:151], %0\n" \
    "v_mfma_f32_16x16x32_bf16 %1, v[156:159], v[152:155], %1\n" \
    "v_mfma_f32_16x16x32_bf16 %2, v[160:163], v[148:151], %2\n" \
    "v_mfma_f32_16x16x32_bf16 %3, v[160:163], v[152:155], %3\n" \
    "v_mfma_f32_16x16x32_bf16 %4, v[164:167], v[148:151], %4\n" \
    "v_mfma_f32_16x16x32_bf16 %5, v[164:167], v[152:155], %5\n" \
    "s_cmp_lg_u32 %12, 2\n" \
    "s_cbranch_scc1 G1C70_%=\n" \
    "v_mfma_f32_16x16x32_bf16 %6, v[136:139], v[84:87], %6\n" \
    "v_mfma_f32_16x16x32_bf16 %7, v[140:143], v[84:87], %7\n" \
    "v_mfma_f32_16x16x32_bf16 %8, v[144:147], v[84:87], %8\n" \
    "G1C70_%=:\n" \
    "s_cmp_lg_u32 %12, 3\n" \
    "s_cbranch_scc1 G1C71_%=\n" \
    "v_mfma_f32_16x16x32_bf16 %6, v[156:159], v[84:87], %6\n" \
    "v_mfma_f32_16x16x32_bf16 %7, v[160:163], v[84:87], %7\n" \
    "v_mfma_f32_16x16x32_bf16 %8, v[164:167], v[84:87], %8\n" \
    "G1C71_%=:\n" \
    "s_nop 7\n" \
    ""
#define G1_FFN1_CLOB "v78", "v79", "v80", "v81", "v82", "v83", "v84", "v85", "v86", "v87", "v88", "v89", "v90", "v91", "v92", "v93", "v94", "v95", "v96", "v97", "v98", "v99", "v100", "v101", "v102", "v103", "v104", "v105", "v106", "v107", "v108", "v109", "v110", "v111", "v112", "v113", "v114", "v115", "v116", "v117", "v118", "v119", "v120", "v121", "v122", "v123", "v124", "v125", "v126", "v127", "v128", "v129", "v130", "v131", "v132", "v133", "v134", "v135", "v136", "v137", "v138", "v139", "v140", "v141", "v142", "v143", "v144", "v145", "v146", "v147", "v148", "v149", "v150", "v151", "v152", "v153", "v154", "v155", "v156", "v157", "v158", "v159", "v160", "v161", "v162", "v163", "v164", "v165", "v166", "v167"
template <int NT, bool FFN, int WN>
__device__ __forceinline__ void g1_pass(ldsp lds, int wm, int lane, f32x4 (&acc)[NT][2]) {
    constexpr int t0 = FFN ? 4 * WN : 3 * WN;
    const int m = lane & 15, g = lane >> 4, q = (lane & 15) >> 2, p = lane & 3;
    const int arow = 32 * wm + m, swz = (arow >> 1) & 7;
    const unsigned l0 = (unsigned)(uintptr_t)lds;
    const unsigned a0 = l0 + arow * 128 + ((g ^ swz) << 4), a1 = l0 + arow * 128 + (((4 + g) ^ swz) << 4);
    f32x4 cs[NT];
#pragma unroll
    for (int t = 0; t < NT; ++t) cs[t] = f32x4{};
    if constexpr (FFN) {
        const unsigned b0 = l0 + OFF_B + (16 * (g >> 1) + 4 * (g & 1) + q) * 224 + 8 * p + t0 * 32;
        if constexpr (WN == 0)
            asm volatile(G1_FFN0 : "=&v"(acc[0][0]), "=&v"(acc[0][1]), "=&v"(acc[1][0]), "=&v"(acc[1][1]), "=&v"(acc[2][0]), "=&v"(acc[2][1]), "=&v"(acc[3][0]), "=&v"(acc[3][1]),
                         "+v"(cs[0]), "+v"(cs[1]), "+v"(cs[2]), "+v"(cs[3]) : "v"(a0), "v"(a1), "v"(b0), "s"(wm) : "memory", "scc", G1_FFN0_CLOB);
        else
            asm volatile(G1_FFN1 : "=&v"(acc[0][0]), "=&v"(acc[0][1]), "=&v"(acc[1][0]), "=&v"(acc[1][1]), "=&v"(acc[2][0]), "=&v"(acc[2][1]),
                         "+v"(cs[0]), "+v"(cs[1]), "+v"(cs[2]) : "v"(a0), "v"(a1), "v"(b0), "s"((wm + 2) & 3) : "memory", "scc", G1_FFN1_CLOB);
    } else {
        const unsigned r = l0 + OFF_B + (8 * g + q) * 192 + 8 * p, b0 = r + ((t0 ^ (g & 1)) << 5), b1 = r + (((t0 + 1) ^ (g & 1)) << 5);
        if constexpr (WN == 0)
            asm volatile(G1_QKV0 : "=&v"(acc[0][0]), "=&v"(acc[0][1]), "=&v"(acc[1][0]), "=&v"(acc[1][1]), "=&v"(acc[2][0]), "=&v"(acc[2][1]),
                         "+v"(cs[0]), "+v"(cs[1]), "+v"(cs[2]) : "v"(a0), "v"(a1), "v"(b0), "v"(b1), "s"(wm) : "memory", "scc", G1_QKV0_CLOB);
        else
            asm volatile(G1_QKV1 : "=&v"(acc[0][0]), "=&v"(acc[0][1]), "=&v"(acc[1][0]), "=&v"(acc[1][1]), "=&v"(acc[2][0]), "=&v"(acc[2][1]),
                         "+v"(cs[0]), "+v"(cs[1]), "+v"(cs[2]) : "v"(a0), "v"(a1), "v"(b0), "v"(b1), "s"((wm + 2) & 3) : "memory", "scc", G1_QKV1_CLOB);
    }
    lane = lane_id();
    if ((lane & 15) == 0) {
#pragma unroll
        for (int t = 0; t < NT; ++t) *(LAS f32x4*)(lds + L_COLS + (wm * 112 + (t0 + t) * 16) * 4 + (lane & 48)) = cs[t];
    }
    if (FFN) {
        asm volatile("s_waitcnt lgkmcnt(0)" ::: "memory");
        lds_count(lds, 0, lane);
        lds_wait(lds, 0, 8);
    } else BAR();
}
__device__ __forceinline__ f32x4 g1_colsum(ldsp lds, int col) {
    f32x4 c = *(const LAS f32x4*)(lds + L_COLS + col * 4);
#pragma unroll
    for (int k = 1; k < 4; ++k) c += *(const LAS f32x4*)(lds + L_COLS + (k * 112 + col) * 4);
    return c;
}

template <int NF>
__device__ __forceinline__ void g1_ffn(ldsp lds, char* ws, const __amdgpu_buffer_rsrc_t wsr, int wm, int lane, u32x4 (&hv)[4]) {
    constexpr int T0 = (NF == 4) ? 0 : 4;
    f32x4 acc[NF][2];
    g1_pass<NF, true, T0 / 4>(lds, wm, lane_id(), acc);
    lane = lane_id();
    const int m = lane & 15, g = lane >> 4;
    STAMP(14); CSTAMP(18);
    float mean[2], rstd[2];
#pragma unroll
    for (int i = 0; i < 2; ++i) { const unsigned a = *(const LAS unsigned*)(lds + L_ROWST + (32 * wm + 16 * i + m) * 4); mean[i] = bf_lo(a); rstd[i] = __builtin_amdgcn_rcpf(bf_hi(a)); }
    f32x4 c[NF]; u32x2 bias[NF];
#pragma unroll
    for (int t = 0; t < NF; ++t) { c[t] = g1_colsum(lds, (T0 + t) * 16 + 4 * g); bias[t] = *(const LAS u32x2*)(lds + L_BIAS + (16 * (T0 + t) + 4 * g) * 2); }
#pragma unroll
    for (int t = 0; t < NF; ++t) {
        const float bb[4] = {bf_lo(bias[t][0]), bf_hi(bias[t][0]), bf_lo(bias[t][1]), bf_hi(bias[t][1])};
        u32x2 d[2];
#pragma unroll
        for (int i = 0; i < 2; ++i) { float v[4];
#pragma unroll
            for (int r = 0; r < 4; ++r) v[r] = gelu_tanh(__builtin_fmaf(__builtin_fmaf(-mean[i], c[t][r], acc[t][i][r]), rstd[i], bb[r]));
            d[i] = u32x2{pk2(v[0], v[1]), pk2(v[2], v[3])}; }
        hv[t] = widen(d[0], d[1]);
    }
}

template <int wn>
__device__ __forceinline__ void g1_compute(ldsp lds, char* ws, const __amdgpu_buffer_rsrc_t wsr, int wm, int lane, int mt, int nt, u32x4 (&hv)[4]) {
    {
        float mean[2], rstd[2];
        f32x4 acc[3][2];
        STAMP(16); CSTAMP(17);
        g1_pass<3, false, wn>(lds, wm, lane, acc);
        lane = lane_id();
        const int m = lane & 15, g = lane >> 4;
        STAMP(2);
        f32x4 c[3];
#pragma unroll
        for (int i = 0; i < 2; ++i) { const unsigned a = *(const LAS unsigned*)(lds + L_ROWST + (32 * wm + 16 * i + m) * 4); mean[i] = bf_lo(a); rstd[i] = __builtin_amdgcn_rcpf(bf_hi(a)); }
#pragma unroll
        for (int t = 0; t < 3; ++t) c[t] = g1_colsum(lds, 16 * (3 * wn + t) + 4 * g);
        const int blk32 = 4 * mt + wm, ht0 = 6 * nt + 3 * wn;
        u32x4 e[3];
#pragma unroll
        for (int t = 0; t < 3; ++t) {
            const float sc = (ht0 + t < 32) ? QSCALE : 1.0f;
            u32x2 d[2];
#pragma unroll
            for (int i = 0; i < 2; ++i) {
                const float rs = rstd[i] * sc; float v[4];
#pragma unroll
                for (int r = 0; r < 4; ++r) v[r] = __builtin_fmaf(-mean[i], c[t][r], acc[t][i][r]) * rs;
                d[i] = u32x2{pk2(v[0], v[1]), pk2(v[2], v[3])};
            }
            e[t] = widen(d[0], d[1]);
            __builtin_amdgcn_raw_buffer_store_b128(e[t], wsr, WS_Q + blk32 * 1024 + (g >> 1) * 512 + (16 * (g & 1) + m) * 16, (ht0 + t) * 65536, 16);
        }
        if (ht0 + 2 >= 32 && ht0 < 64) {
            unsigned kn[3];
#pragma unroll
            for (int t = 0; t < 3; ++t) kn[t] = umax_32lanes(__float_as_uint(sum_x32(dot2(e[t][3], e[t][3], dot2(e[t][2], e[t][2], dot2(e[t][1], e[t][1], dot2(e[t][0], e[t][0], 0.f)))))));
            const int ht = ht0 + lane;
            if (lane < 3 && ht >= 32 && ht < 64) __builtin_amdgcn_raw_buffer_store_b32(lane == 0 ? kn[0] : lane == 1 ? kn[1] : kn[2], wsr, WS_KMAX + ((ht - 32) * 64 + blk32) * 4, 0, 16);
        }
        STAMP(3);
    }
    g1_ffn<4 - wn>(lds, ws, wsr, wm, lane, hv);
}

#define WAIT_BAR(n) asm volatile("s_waitcnt vmcnt(%0)\n\ts_barrier" ::"n"(n) : "memory")
template <int NA, int NB, class F>
__device__ __forceinline__ void g1_load(F issue) {
#pragma unroll 1
    for (int s = 0; s < NSLOT; ++s) issue(s, s * SLOT);
    WAIT_BAR(4 * NA);
    WAIT_BAR(3 * NA);
    int slot = 0;
#pragma unroll 1
    for (int s = NSLOT; s < 16; ++s) {
        issue(s, slot); slot = (slot == (NSLOT - 1) * SLOT) ? 0 : slot + SLOT;
        if (s < 8) WAIT_BAR(3 * NA); else if (NB != 0) WAIT_BAR(3 * NB);
        else if (s == 8) WAIT_BAR(2 * NA); else if (s == 9) WAIT_BAR(NA); else if (s < 13) WAIT_BAR(0); else asm volatile("s_barrier" ::: "memory");
    }
    WAIT_BAR(0);
}

__device__ __forceinline__ bf16x16 lo8(const f32x16& s) { return __builtin_bit_cast(bf16x16, __builtin_shufflevector(s, s, 0, 1, 2, 3, 4, 5, 6, 7)); }
__device__ __forceinline__ bf16x16 hi8(const f32x16& s) { return __builtin_bit_cast(bf16x16, __builtin_shufflevector(s, s, 8, 9, 10, 11, 12, 13, 14, 15)); }

#define ATT_PA 1
#define ATT_PB 0
#define ATT_PC 0
#define ATT_B1 3
#define ATT_STR2(x) #x
#define ATT_STR(x) ATT_STR2(x)
#define G2PRIO 0
#define ATT_ASM \
    "v_mov_b32 v96, %10\n" \
    "v_mov_b32 v97, %11\n" \
    "v_mov_b32 v98, %12\n" \
    "v_mov_b32 v99, %13\n" \
    "v_mov_b32 v125, %15\n" \
    "v_mov_b32 v126, %16\n" \
    "v_mov_b32 v127, %17\n" \
    "ds_read_b128 v[100:103], v125\n" \
    "ds_read_b128 v[104:107], v125 offset:1024\n" \
    "ds_read_b64_tr_b16 v[108:109], v126\n" \
    "ds_read_b64_tr_b16 v[110:111], v127\n" \
    "ds_read_b64_tr_b16 v[116:117], v126 offset:544\n" \
    "ds_read_b64_tr_b16 v[118:119], v127 offset:544\n" \
    "v_mov_b32 v124, 0xdddddddd\n" \
    "v_mov_b32 v80, %14\n" \
    "v_mov_b32 v81, %14\n" \
    "v_mov_b32 v82, %14\n" \
    "v_mov_b32 v83, %14\n" \
    "v_mov_b32 v84, %14\n" \
    "v_mov_b32 v85, %14\n" \
    "v_mov_b32 v86, %14\n" \
    "v_mov_b32 v87, %14\n" \
    "v_mov_b32 v88, %14\n" \
    "v_mov_b32 v89, %14\n" \
    "v_mov_b32 v90, %14\n" \
    "v_mov_b32 v91, %14\n" \
    "v_mov_b32 v92, %14\n" \
    "v_mov_b32 v93, %14\n" \
    "v_mov_b32 v94, %14\n" \
    "v_mov_b32 v95, %14\n" \
    "v_mov_b32 v32, 0\n" \
    "v_mov_b32 v33, 0\n" \
    "v_mov_b32 v34, 0\n" \
    "v_mov_b32 v35, 0\n" \
    "v_mov_b32 v36, 0\n" \
    "v_mov_b32 v37, 0\n" \
    "v_mov_b32 v38, 0\n" \
    "v_mov_b32 v39, 0\n" \
    "v_mov_b32 v40, 0\n" \
    "v_mov_b32 v41, 0\n" \
    "v_mov_b32 v42, 0\n" \
    "v_mov_b32 v43, 0\n" \
    "v_mov_b32 v44, 0\n" \
    "v_mov_b32 v45, 0\n" \
    "v_mov_b32 v46, 0\n" \
    "v_mov_b32 v47, 0\n" \
    "v_mov_b32 v72, 0\n" \
    "v_mov_b32 v73, 0\n" \
    "v_mov_b32 v74, 0\n" \
    "v_mov_b32 v75, 0\n" \
    "v_mov_b32 v76, 0\n" \
    "v_mov_b32 v77, 0\n" \
    "v_mov_b32 v78, 0\n" \
    "v_mov_b32 v79, 0\n" \
    "v_mov_b32 v120, 0\n" \
    "v_mov_b32 v121, 0\n" \
    "v_mov_b32 v122, 0\n" \
    "v_mov_b32 v123, 0\n" \
    "s_waitcnt lgkmcnt(0)\n" \
    "v_mfma_f32_32x32x16_bf16 v[48:63], v[100:103], v[96:99], v[80:95]\n" \
    "s_nop 7\n" \
    "s_nop 3\n" \
    "s_setprio 3\n" \
    "ATT_LOOP_%=:\n" \
    "s_waitcnt lgkmcnt(0)\n" \
    "v_exp_f32 v48, v48\n" \
    "v_exp_f32 v49, v49\n" \
    "v_smfmac_f32_32x32x32_bf16 v[32:47], v[120:123], v[72:79], v124\n" \
    "ds_read_b64_tr_b16 v[112:113], v126 offset:1088\n" \
    "ds_read_b64_tr_b16 v[114:115], v127 offset:1088\n" \
    "v_exp_f32 v50, v50\n" \
    "v_exp_f32 v51, v51\n" \
    "v_exp_f32 v52, v52\n" \
    "v_exp_f32 v53, v53\n" \
    "v_mfma_f32_32x32x16_bf16 v[64:79], v[104:107], v[96:99], v[80:95]\n" \
    "ds_read_b128 v[100:103], v125 offset:2048\n" \
    "ds_read_b64_tr_b16 v[120:121], v126 offset:1632\n" \
    "ds_read_b64_tr_b16 v[122:123], v127 offset:1632\n" \
    "v_exp_f32 v54, v54\n" \
    "v_exp_f32 v55, v55\n" \
    "v_exp_f32 v56, v56\n" \
    "v_exp_f32 v57, v57\n" \
    "v_smfmac_f32_32x32x32_bf16 v[32:47], v[108:111], v[48:55], v124\n" \
    "v_exp_f32 v58, v58\n" \
    "v_exp_f32 v59, v59\n" \
    "v_exp_f32 v60, v60\n" \
    "v_exp_f32 v61, v61\n" \
    "v_exp_f32 v62, v62\n" \
    "v_exp_f32 v63, v63\n" \
    "s_waitcnt lgkmcnt(0)\n" \
    "v_exp_f32 v64, v64\n" \
    "v_exp_f32 v65, v65\n" \
    "v_smfmac_f32_32x32x32_bf16 v[32:47], v[116:119], v[56:63], v124\n" \
    "ds_read_b64_tr_b16 v[108:109], v126 offset:2176\n" \
    "ds_read_b64_tr_b16 v[110:111], v127 offset:2176\n" \
    "v_exp_f32 v66, v66\n" \
    "v_exp_f32 v67, v67\n" \
    "v_exp_f32 v68, v68\n" \
    "v_exp_f32 v69, v69\n" \
    "v_mfma_f32_32x32x16_bf16 v[48:63], v[100:103], v[96:99], v[80:95]\n" \
    "ds_read_b128 v[104:107], v125 offset:3072\n" \
    "ds_read_b64_tr_b16 v[116:117], v126 offset:2720\n" \
    "ds_read_b64_tr_b16 v[118:119], v127 offset:2720\n" \
    "v_exp_f32 v70, v70\n" \
    "v_exp_f32 v71, v71\n" \
    "v_exp_f32 v72, v72\n" \
    "v_exp_f32 v73, v73\n" \
    "v_smfmac_f32_32x32x32_bf16 v[32:47], v[112:115], v[64:71], v124\n" \
    "v_exp_f32 v74, v74\n" \
    "v_exp_f32 v75, v75\n" \
    "v_exp_f32 v76, v76\n" \
    "v_exp_f32 v77, v77\n" \
    "v_exp_f32 v78, v78\n" \
    "v_exp_f32 v79, v79\n" \
    "s_waitcnt lgkmcnt(0)\n" \
    "v_exp_f32 v48, v48\n" \
    "v_exp_f32 v49, v49\n" \
    "v_smfmac_f32_32x32x32_bf16 v[32:47], v[120:123], v[72:79], v124\n" \
    "ds_read_b64_tr_b16 v[112:113], v126 offset:3264\n" \
    "ds_read_b64_tr_b16 v[114:115], v127 offset:3264\n" \
    "v_exp_f32 v50, v50\n" \
    "v_exp_f32 v51, v51\n" \
    "v_exp_f32 v52, v52\n" \
    "v_exp_f32 v53, v53\n" \
    "v_mfma_f32_32x32x16_bf16 v[64:79], v[104:107], v[96:99], v[80:95]\n" \
    "ds_read_b128 v[100:103], v125 offset:4096\n" \
    "ds_read_b64_tr_b16 v[120:121], v126 offset:3808\n" \
    "ds_read_b64_tr_b16 v[122:123], v127 offset:3808\n" \
    "v_exp_f32 v54, v54\n" \
    "v_exp_f32 v55, v55\n" \
    "v_exp_f32 v56, v56\n" \
    "v_exp_f32 v57, v57\n" \
    "v_smfmac_f32_32x32x32_bf16 v[32:47], v[108:111], v[48:55], v124\n" \
    "v_exp_f32 v58, v58\n" \
    "v_exp_f32 v59, v59\n" \
    "v_exp_f32 v60, v60\n" \
    "v_exp_f32 v61, v61\n" \
    "v_exp_f32 v62, v62\n" \
    "v_exp_f32 v63, v63\n" \
    "s_waitcnt lgkmcnt(0)\n" \
    "v_exp_f32 v64, v64\n" \
    "v_exp_f32 v65, v65\n" \
    "v_smfmac_f32_32x32x32_bf16 v[32:47], v[116:119], v[56:63], v124\n" \
    "ds_read_b64_tr_b16 v[108:109], v126 offset:4352\n" \
    "ds_read_b64_tr_b16 v[110:111], v127 offset:4352\n" \
    "v_exp_f32 v66, v66\n" \
    "v_exp_f32 v67, v67\n" \
    "v_exp_f32 v68, v68\n" \
    "v_exp_f32 v69, v69\n" \
    "v_mfma_f32_32x32x16_bf16 v[48:63], v[100:103], v[96:99], v[80:95]\n" \
    "ds_read_b128 v[104:107], v125 offset:5120\n" \
    "ds_read_b64_tr_b16 v[116:117], v126 offset:4896\n" \
    "ds_read_b64_tr_b16 v[118:119], v127 offset:4896\n" \
    "v_exp_f32 v70, v70\n" \
    "v_exp_f32 v71, v71\n" \
    "v_exp_f32 v72, v72\n" \
    "v_exp_f32 v73, v73\n" \
    "v_smfmac_f32_32x32x32_bf16 v[32:47], v[112:115], v[64:71], v124\n" \
    "v_exp_f32 v74, v74\n" \
    "v_exp_f32 v75, v75\n" \
    "v_exp_f32 v76, v76\n" \
    "v_exp_f32 v77, v77\n" \
    "v_exp_f32 v78, v78\n" \
    "v_exp_f32 v79, v79\n" \
    "s_waitcnt lgkmcnt(0)\n" \
    "v_exp_f32 v48, v48\n" \
    "v_exp_f32 v49, v49\n" \
    "v_smfmac_f32_32x32x32_bf16 v[32:47], v[120:123], v[72:79], v124\n" \
    "ds_read_b64_tr_b16 v[112:113], v126 offset:5440\n" \
    "ds_read_b64_tr_b16 v[114:115], v127 offset:5440\n" \
    "v_exp_f32 v50, v50\n" \
    "v_exp_f32 v51, v51\n" \
    "v_exp_f32 v52, v52\n" \
    "v_exp_f32 v53, v53\n" \
    "v_mfma_f32_32x32x16_bf16 v[64:79], v[104:107], v[96:99], v[80:95]\n" \
    "ds_read_b128 v[100:103], v125 offset:6144\n" \
    "ds_read_b64_tr_b16 v[120:121], v126 offset:5984\n" \
    "ds_read_b64_tr_b16 v[122:123], v127 offset:5984\n" \
    "v_exp_f32 v54, v54\n" \
    "v_exp_f32 v55, v55\n" \
    "v_exp_f32 v56, v56\n" \
    "v_exp_f32 v57, v57\n" \
    "v_smfmac_f32_32x32x32_bf16 v[32:47], v[108:111], v[48:55], v124\n" \
    "v_exp_f32 v58, v58\n" \
    "v_exp_f32 v59, v59\n" \
    "v_exp_f32 v60, v60\n" \
    "v_exp_f32 v61, v61\n" \
    "v_exp_f32 v62, v62\n" \
    "v_exp_f32 v63, v63\n" \
    "s_waitcnt lgkmcnt(0)\n" \
    "v_exp_f32 v64, v64\n" \
    "v_exp_f32 v65, v65\n" \
    "v_smfmac_f32_32x32x32_bf16 v[32:47], v[116:119], v[56:63], v124\n" \
    "ds_read_b64_tr_b16 v[108:109], v126 offset:6528\n" \
    "ds_read_b64_tr_b16 v[110:111], v127 offset:6528\n" \
    "v_exp_f32 v66, v66\n" \
    "v_exp_f32 v67, v67\n" \
    "v_exp_f32 v68, v68\n" \
    "v_exp_f32 v69, v69\n" \
    "v_mfma_f32_32x32x16_bf16 v[48:63], v[100:103], v[96:99], v[80:95]\n" \
    "ds_read_b128 v[104:107], v125 offset:7168\n" \
    "ds_read_b64_tr_b16 v[116:117], v126 offset:7072\n" \
    "ds_read_b64_tr_b16 v[118:119], v127 offset:7072\n" \
    "v_exp_f32 v70, v70\n" \
    "v_exp_f32 v71, v71\n" \
    "v_exp_f32 v72, v72\n" \
    "v_exp_f32 v73, v73\n" \
    "v_smfmac_f32_32x32x32_bf16 v[32:47], v[112:115], v[64:71], v124\n" \
    "v_exp_f32 v74, v74\n" \
    "v_exp_f32 v75, v75\n" \
    "v_exp_f32 v76, v76\n" \
    "v_exp_f32 v77, v77\n" \
    "v_exp_f32 v78, v78\n" \
    "v_exp_f32 v79, v79\n" \
    "s_waitcnt lgkmcnt(0)\n" \
    "v_exp_f32 v48, v48\n" \
    "v_exp_f32 v49, v49\n" \
    "v_smfmac_f32_32x32x32_bf16 v[32:47], v[120:123], v[72:79], v124\n" \
    "ds_read_b64_tr_b16 v[112:113], v126 offset:7616\n" \
    "ds_read_b64_tr_b16 v[114:115], v127 offset:7616\n" \
    "v_exp_f32 v50, v50\n" \
    "v_exp_f32 v51, v51\n" \
    "v_exp_f32 v52, v52\n" \
    "v_exp_f32 v53, v53\n" \
    "v_mfma_f32_32x32x16_bf16 v[64:79], v[104:107], v[96:99], v[80:95]\n" \
    "ds_read_b128 v[100:103], v125 offset:8192\n" \
    "ds_read_b64_tr_b16 v[120:121], v126 offset:8160\n" \
    "ds_read_b64_tr_b16 v[122:123], v127 offset:8160\n" \
    "v_exp_f32 v54, v54\n" \
    "v_exp_f32 v55, v55\n" \
    "v_exp_f32 v56, v56\n" \
    "v_exp_f32 v57, v57\n" \
    "v_smfmac_f32_32x32x32_bf16 v[32:47], v[108:111], v[48:55], v124\n" \
    "v_exp_f32 v58, v58\n" \
    "v_exp_f32 v59, v59\n" \
    "v_exp_f32 v60, v60\n" \
    "v_exp_f32 v61, v61\n" \
    "v_exp_f32 v62, v62\n" \
    "v_exp_f32 v63, v63\n" \
    "s_waitcnt lgkmcnt(0)\n" \
    "v_exp_f32 v64, v64\n" \
    "v_exp_f32 v65, v65\n" \
    "v_smfmac_f32_32x32x32_bf16 v[32:47], v[116:119], v[56:63], v124\n" \
    "ds_read_b64_tr_b16 v[108:109], v126 offset:8704\n" \
    "ds_read_b64_tr_b16 v[110:111], v127 offset:8704\n" \
    "v_exp_f32 v66, v66\n" \
    "v_exp_f32 v67, v67\n" \
    "v_exp_f32 v68, v68\n" \
    "v_exp_f32 v69, v69\n" \
    "v_mfma_f32_32x32x16_bf16 v[48:63], v[100:103], v[96:99], v[80:95]\n" \
    "ds_read_b128 v[104:107], v125 offset:9216\n" \
    "ds_read_b64_tr_b16 v[116:117], v126 offset:9248\n" \
    "ds_read_b64_tr_b16 v[118:119], v127 offset:9248\n" \
    "v_exp_f32 v70, v70\n" \
    "v_exp_f32 v71, v71\n" \
    "v_exp_f32 v72, v72\n" \
    "v_exp_f32 v73, v73\n" \
    "v_smfmac_f32_32x32x32_bf16 v[32:47], v[112:115], v[64:71], v124\n" \
    "v_exp_f32 v74, v74\n" \
    "v_exp_f32 v75, v75\n" \
    "v_exp_f32 v76, v76\n" \
    "v_exp_f32 v77, v77\n" \
    "v_exp_f32 v78, v78\n" \
    "v_exp_f32 v79, v79\n" \
    "v_add_u32 v125, 8192, v125\n" \
    "v_add_u32 v126, 8704, v126\n" \
    "v_add_u32 v127, 8704, v127\n" \
    "s_add_i32 %9, %9, -1\n" \
    "s_cmp_lg_u32 %9, 7\n" \
    "s_cbranch_scc1 ATT_KV_%=\n" \
    "s_mov_b32 s40, 0x100000\n" \
    "v_mov_b32 v128, 1\n" \
    "s_waitcnt vmcnt(0)\n" \
    "ds_add_u32 %18, v128 offset:4\n" \
    "s_setprio " ATT_STR(ATT_PA) "\n" \
    "ATT_SP_%=:\n" \
    "ds_read_b32 v128, %18\n" \
    "s_waitcnt lgkmcnt(0)\n" \
    "v_readfirstlane_b32 vcc_lo, v128\n" \
    "s_add_i32 s40, s40, -1\n" \
    "s_cmp_eq_u32 s40, 0\n" \
    "s_cbranch_scc1 ATT_KV_%=\n" \
    "s_cmp_lt_u32 vcc_lo, 4\n" \
    "s_cbranch_scc1 ATT_SP_%=\n" \
    "ATT_KV_%=:\n" \
    "s_cmp_lg_u32 %9, " ATT_STR(ATT_B1) "\n" \
    "s_cbranch_scc1 ATT_P1_%=\n" \
    "s_setprio " ATT_STR(ATT_PB) "\n" \
    "ATT_P1_%=:\n" \
    "s_cmp_lg_u32 %9, 1\n" \
    "s_cbranch_scc1 ATT_P0_%=\n" \
    "s_setprio " ATT_STR(ATT_PC) "\n" \
    "ATT_P0_%=:\n" \
    "s_cmp_lg_u32 %9, 0\n" \
    "s_cbranch_scc1 ATT_LOOP_%=\n" \
    "v_smfmac_f32_32x32x32_bf16 v[32:47], v[120:123], v[72:79], v124\n" \
    "s_setprio 0\n" \
    "s_waitcnt lgkmcnt(0)\n" \
    "s_nop 7\n" \
    "s_nop 3\n" \
    "v_mov_b32 %0, v32\n" \
    "v_mov_b32 %1, v33\n" \
    "v_mov_b32 %2, v34\n" \
    "v_mov_b32 %3, v35\n" \
    "v_mov_b32 %4, v36\n" \
    "v_mov_b32 %5, v37\n" \
    "v_mov_b32 %6, v38\n" \
    "v_mov_b32 %7, v39\n" \
    "v_mov_b32 %8, v40\n" \
    ""
#define ATT_CLOBBER "v32", "v33", "v34", "v35", "v36", "v37", "v38", "v39", "v40", "v41", "v42", "v43", "v44", "v45", "v46", "v47", "v48", "v49", "v50", "v51", "v52", "v53", "v54", "v55", "v56", "v57", "v58", "v59", "v60", "v61", "v62", "v63", "v64", "v65", "v66", "v67", "v68", "v69", "v70", "v71", "v72", "v73", "v74", "v75", "v76", "v77", "v78", "v79", "v80", "v81", "v82", "v83", "v84", "v85", "v86", "v87", "v88", "v89", "v90", "v91", "v92", "v93", "v94", "v95", "v96", "v97", "v98", "v99", "v100", "v101", "v102", "v103", "v104", "v105", "v106", "v107", "v108", "v109", "v110", "v111", "v112", "v113", "v114", "v115", "v116", "v117", "v118", "v119", "v120", "v121", "v122", "v123", "v124", "v125", "v126", "v127", "v128"
__device__ __forceinline__ void attention(ldsp lds, char* ws, const __amdgpu_buffer_rsrc_t wsr, int w, int lane, int head, int qt) {
    const bf16x8 qf = ld_b128(lds + L_Q + w * 1024 + lane * 16);
    unsigned kb = umax_32lanes(*(const LAS unsigned*)(lds + L_KMAX + lane * 4));
    { const auto r = __builtin_amdgcn_permlane32_swap(kb, kb, false, false); kb = r[0] > r[1] ? r[0] : r[1]; }
    const float km = __uint_as_float(kb);
    const u32x4 qu = __builtin_bit_cast(u32x4, qf);
    const float qn = sum_x32(dot2(qu[3], qu[3], dot2(qu[2], qu[2], dot2(qu[1], qu[1], dot2(qu[0], qu[0], 0.f)))));
    const float bound = sqrtf(qn * km) * 1.001f;
    const int G = lane >> 4, q = (lane & 15) >> 2, p = lane & 3;
    ldsp kp = lds + L_K + lane * 16;
    ldsp v0 = lds + L_V + ((G & 1) ? 512 + 8 * p : (8 * (G >> 1) + q) * 32 + 8 * p);
    ldsp v1 = lds + L_V + ((G & 1) ? 512 + 8 * p : (8 * (G >> 1) + 4 + q) * 32 + 8 * p);
    f32x16 o = {};
    if (__all(bound <= SAFE_BOUND)) {
        int cnt = 8;
        float r0, r1, r2, r3, r4, r5, r6, r7, r8;
        asm volatile(ATT_ASM
                     : "=v"(r0), "=v"(r1), "=v"(r2), "=v"(r3), "=v"(r4), "=v"(r5), "=v"(r6), "=v"(r7), "=v"(r8), "+s"(cnt)
                     : "v"(qu[0]), "v"(qu[1]), "v"(qu[2]), "v"(qu[3]), "v"(-bound), "v"((unsigned)(uintptr_t)kp), "v"((unsigned)(uintptr_t)v0), "v"((unsigned)(uintptr_t)v1),
                       "v"((unsigned)(uintptr_t)lds + L_MISC + 28)
                     : "memory", "scc", "vcc", "s40", ATT_CLOBBER);
        o[0] = r0; o[1] = r1; o[2] = r2; o[3] = r3; o[4] = r4; o[5] = r5; o[6] = r6; o[7] = r7; o[8] = r8;
    } else {
        WAITV(0);
        __hip_atomic_fetch_add((LAS unsigned*)(lds + L_MISC + 32), 1u, __ATOMIC_RELAXED, __HIP_MEMORY_SCOPE_WORKGROUP);
        lds_wait(lds, 3, 4);
        const int idx = (int)0xDDDDDDDDu;
        float mx = -INFINITY;
#pragma unroll 1
        for (int it = 0; it < 64; ++it) {
            f32x16 s = MFMA32(ld_b128(kp), qf, f32x16{});
            const bf16x8 va = ld_tr2(v0, v1), vb = ld_tr2(v0 + VT, v1 + VT);
            float bm = s[0];
#pragma unroll
            for (int r = 1; r < 16; ++r) bm = fmaxf(bm, s[r]);
            const float mn = fmaxf(mx, max_x32(bm));
            const float al = __builtin_amdgcn_exp2f(mx - mn);
            mx = mn;
#pragma unroll
            for (int r = 0; r < 16; ++r) { o[r] *= al; s[r] = __builtin_amdgcn_exp2f(s[r] - mn); }
            o = SMF(va, lo8(s), o, idx); o = SMF(vb, hi8(s), o, idx);
            kp += 1024; v0 += 2 * VT; v1 += 2 * VT;
        }
    }
    const float inv = 1.0f / sum_x32(o[8]);
    const u32x4 d = {pk2(o[0] * inv, o[1] * inv), pk2(o[2] * inv, o[3] * inv), pk2(o[4] * inv, o[5] * inv), pk2(o[6] * inv, o[7] * inv)};
    __builtin_amdgcn_raw_buffer_store_b128(d, wsr, WS_O + (unsigned)((8 * qt + w) * 32 + head) * 1024 + lane * 16, 0, 16);
    CSTAMP(13);
    arrive_publish(lds, 2, 8, ws, WS_F3 + (head * 8 + qt) * 128, lane);
}

struct G2Ctx { ldsp wr, rd0, rd1; };
__device__ __forceinline__ void g2_wfrag(const G2Ctx& c, int half, const u32x4& r0, const u32x4& r1, bf16x8& w0, bf16x8& w1) {
    *(LAS u32x4*)(c.wr + half * 2048) = r0; *(LAS u32x4*)(c.wr + half * 2048 + 1024) = r1;
    w0 = ld_tr2(c.rd0 + half * 2048, c.rd0 + half * 2048 + 512); w1 = ld_tr2(c.rd1 + half * 2048, c.rd1 + half * 2048 + 512);
}
#define G2SLEEP 0
template <int PACE>
__device__ __forceinline__ void g2_mma(f32x16 (&acc)[2][2], const bf16x8& w0, const bf16x8& w1, const u32x4& a0, const u32x4& a1) {
    const bf16x8 h0 = __builtin_bit_cast(bf16x8, a0), h1 = __builtin_bit_cast(bf16x8, a1);
    auto gap = [&]() { if (PACE > 0) { __builtin_amdgcn_sched_barrier(0); __builtin_amdgcn_s_sleep(PACE); __builtin_amdgcn_sched_barrier(0); } };
    acc[0][0] = MFMA32(w0, h0, acc[0][0]); gap(); acc[0][1] = MFMA32(w0, h1, acc[0][1]); gap();
    acc[1][0] = MFMA32(w1, h0, acc[1][0]); gap(); acc[1][1] = MFMA32(w1, h1, acc[1][1]); gap();
}

__global__ __launch_bounds__(768, 3) void fused_block_kernel(const __hip_bfloat16* __restrict__ x, const __hip_bfloat16* __restrict__ wqkv, const __hip_bfloat16* __restrict__ wo,
                                                             const __hip_bfloat16* __restrict__ w1, const __hip_bfloat16* __restrict__ b1, const __hip_bfloat16* __restrict__ w2,
                                                             const __hip_bfloat16* __restrict__ b2, __hip_bfloat16* __restrict__ out, char* __restrict__ ws) {
    extern __shared__ __attribute__((aligned(16))) char smem[];
    ldsp lds = (ldsp)smem;
    int lane = threadIdx.x & 63;
    const int w = __builtin_amdgcn_readfirstlane(threadIdx.x >> 6);
    const int bg = blockIdx.x & 7, br = blockIdx.x >> 3;
    const int mt = (bg & 1) * 8 + (br & 7), nt = (bg >> 1) * 4 + (br >> 3);
    const int head = bg * 4 + (br >> 3), qt = br & 7;
    const int rt = bg * 4 + (br >> 3), ct = br & 7;
    const __amdgpu_buffer_rsrc_t wsr = __builtin_amdgcn_make_buffer_rsrc(ws, 0, WS_BYTES, 0x00020000);
    STAMP(0); CSTAMP(10);
    if (w < 8) {
        g1_rowstat(ws, (const char*)x, 128 * mt + 8 * nt + w, lane);
        u32x4 hv[4];
        if (w < 4) g1_compute<0>(lds, ws, wsr, w, lane, mt, nt, hv); else g1_compute<1>(lds, ws, wsr, w - 4, lane, mt, nt, hv);
        STAMP(4);
        BAR();
        {
            lane = lane_id();
            const unsigned ho = WS_H + (unsigned)((4 * mt + (w & 3)) * 112 + 7 * nt + (w & 4)) * 1024 + (lane >> 5) * 512 + (lane & 31) * 16;
#pragma unroll
            for (int t = 0; t < 4; ++t) if (t < 3 || w < 4) __builtin_amdgcn_raw_buffer_store_b128(hv[t], wsr, ho, t * 1024, 16);
        }
        STAMP(5); CSTAMP(12);
        attention(lds, ws, wsr, w, lane_id(), head, qt);
        STAMP(6);
        lane = lane_id();
        const int G = lane >> 4, tq = (lane & 15) >> 2, tp = lane & 3, wrow = lane >> 3, wch = (lane & 7) ^ (((wrow >> 1) & 1) << 2);
        const int lrow = 8 * w + wrow, orow = 64 * rt + lrow, ocol = 64 * ct + (lane & 7) * 8;
        const u32x4 xres = *(const GAS u32x4*)((const char*)x + (orow * DM + ocol) * 2), bres = *(const GAS u32x4*)((const char*)b2 + ocol * 2);
        const int kq = w >> 1, tb = w & 1;
        u32x4 t[8][2];
        {
            const char* wsrc = (const char*)wo + (size_t)(128 * kq + ((wrow & 3) | ((wrow & 4) << 1))) * 1024 + ct * 128 + wch * 16;
#pragma unroll
            for (int d = 0; d < 8; ++d) { t[d][0] = *(const GAS u32x4*)(wsrc + (size_t)d * 16384); t[d][1] = *(const GAS u32x4*)(wsrc + (size_t)d * 16384 + 4096); }
        }
        lds_wait(lds, 2, 8);
        G2Ctx gc;
        { ldsp stage = lds + L_RED2 + (2 * kq + tb) * (32 * RED_PITCH); ldsp rd = stage + (8 * (G >> 1) + tq) * 128 + 32 * (G & 1) + 8 * tp;
          const int sw = ((tq >> 1) & 1) * 64; gc.wr = stage + lane * 16; gc.rd0 = rd + sw; gc.rd1 = rd + (sw ^ 64); }
        bf16x8 wq[8][2];
#pragma unroll
        for (int d = 0; d < 8; ++d) { g2_wfrag(gc, 0, t[d][0], t[d][1], wq[d][0], wq[d][1]); asm volatile("" : "+v"(wq[d][0]), "+v"(wq[d][1])); }
        wait_flags(ws, WS_F3 + ((8 * kq + (lane & 7)) * 8 + (rt >> 2)) * 128, *(const volatile LAS u64*)(lds + L_MISC));
        STAMP(7);
        u32x4 of[8];
#pragma unroll
        for (int d = 0; d < 8; ++d) of[d] = __builtin_amdgcn_raw_buffer_load_b128(wsr, WS_O + (unsigned)((2 * rt + tb) * 32 + 8 * kq) * 1024 + lane * 16, d * 1024, 16);
        f32x16 oa[2] = {};
#pragma unroll
        for (int d = 0; d < 8; ++d) { oa[0] = MFMA32(wq[d][0], __builtin_bit_cast(bf16x8, of[d]), oa[0]); oa[1] = MFMA32(wq[d][1], __builtin_bit_cast(bf16x8, of[d]), oa[1]); }
#pragma unroll
        for (int a = 0; a < 2; ++a)
#pragma unroll
            for (int r4 = 0; r4 < 4; ++r4)
                *(LAS f32x4*)(lds + L_RED2 + kq * (64 * RED_PITCH) + (32 * tb + (lane & 31)) * RED_PITCH + (32 * a + 8 * r4 + 4 * (lane >> 5)) * 4) =
                    f32x4{oa[a][4 * r4], oa[a][4 * r4 + 1], oa[a][4 * r4 + 2], oa[a][4 * r4 + 3]};
        STAMP(8);
        BAR();
        ldsp rp = lds + lrow * RED_PITCH + (lane & 7) * 32;
        f32x4 s0 = *(const LAS f32x4*)rp, s1 = *(const LAS f32x4*)(rp + 16);
#pragma unroll
        for (int k = 1; k < 8; ++k) { s0 += *(const LAS f32x4*)(rp + k * 64 * RED_PITCH); s1 += *(const LAS f32x4*)(rp + k * 64 * RED_PITCH + 16); }
        u32x4 o;
        o[0] = pk2(s0[0] + bf_lo(xres[0]) + bf_lo(bres[0]), s0[1] + bf_hi(xres[0]) + bf_hi(bres[0]));
        o[1] = pk2(s0[2] + bf_lo(xres[1]) + bf_lo(bres[1]), s0[3] + bf_hi(xres[1]) + bf_hi(bres[1]));
        o[2] = pk2(s1[0] + bf_lo(xres[2]) + bf_lo(bres[2]), s1[1] + bf_hi(xres[2]) + bf_hi(bres[2]));
        o[3] = pk2(s1[2] + bf_lo(xres[3]) + bf_lo(bres[3]), s1[3] + bf_hi(xres[3]) + bf_hi(bres[3]));
        *(GAS u32x4*)((char*)out + (orow * DM + ocol) * 2) = o;
    } else {
        const int j = w - 8;
        const int ntk = (512 + 16 * head) / 96, ntv = (1024 + 16 * head) / 96, ntq = (16 * head) / 96;
        if (j == 0 || j == 3) {
            const int r0 = 64 * (j & 1) + (lane >> 3);
            const unsigned o0 = r0 * 1024 + (((lane & 7) ^ ((r0 >> 1) & 7)) << 4), o1 = (r0 + 8) * 1024 + (((lane & 7) ^ (((r0 + 8) >> 1) & 7)) << 4);
            const char* src = (const char*)x + (size_t)mt * 128 * 1024; ldsp dst = lds + (j & 1) * 8192;
            g1_load<8, 0>([&](int s, int slot) { if (s >= 8 && s < 13) return; const char* sp = src + (s < 8 ? s : s - 13) * 128;
#pragma unroll
                for (int i = 0; i < 4; ++i) { glds16(sp + i * 16384 + o0, dst + slot + i * 2048); glds16(sp + i * 16384 + o1, dst + slot + i * 2048 + 1024); } });
        } else if (j == 2) {
            unsigned oq[3], of[7];
#pragma unroll
            for (int i = 0; i < 3; ++i) { const int e = 64 * i + lane, k = e / 12, pos = e - 12 * k; oq[i] = k * 3072 + ((pos ^ (((k >> 3) & 1) << 1)) << 4); }
#pragma unroll
            for (int i = 0; i < 7; ++i) { const int e = 64 * i + lane, rho = e / 14, pos = e - 14 * rho; of[i] = ((rho & ~12) | ((rho & 4) << 1) | ((rho & 8) >> 1)) * 3584 + (pos << 4); }
            const char* sq = (const char*)wqkv + nt * 192; const char* sf = (const char*)w1 + nt * 224; ldsp dst = lds + OFF_B;
            g1_load<12, 12>([&](int s, int slot) {
                if (s < 8) { const char* sp = sq + s * (64 * 3072);
#pragma unroll
                    for (int i = 0; i < 12; ++i) glds16(sp + (i / 3) * (16 * 3072) + oq[i % 3], dst + slot + i * 1024);
                } else { const char* sp = sf + ((s - 5) & 7) * (64 * 3584);
#pragma unroll
                    for (int i = 0; i < 14; ++i) glds16(sp + (i / 7) * (32 * 3584) + of[i % 7], dst + slot + i * 1024);
                } });
        } else {
            const u64 tg = call_tag(ws);
            if (lane < 56) *(LAS unsigned*)(lds + L_BIAS + lane * 4) = *(const GAS unsigned*)((const char*)b1 + 224 * nt + lane * 4);
            if (lane == 0) { *(LAS u64*)(lds + L_MISC) = tg; *(LAS u32x4*)(lds + L_MISC + 16) = u32x4{0u, 0u, 0u, 0u}; *(LAS u32x4*)(lds + L_MISC + 32) = u32x4{0u, 0u, 0u, 0u}; }
            BAR(); SSTAMP(16); BAR(); SSTAMP(17); BAR(); SSTAMP(18);
            const GAS u64* pr = (const GAS u64*)(ws + WS_RS) + 128 * mt + lane;
            u64 g0 = __hip_atomic_load(pr, RLX_AGENT), g1 = __hip_atomic_load(pr + 64, RLX_AGENT);
            BAR(); SSTAMP(19); BAR(); SSTAMP(20); BAR(); SSTAMP(21); BAR(); SSTAMP(22); BAR(); SSTAMP(23);
#pragma unroll 1
            for (unsigned spins = 0; spins < SPIN_CAP; ++spins) {
                if (__all((unsigned)(g0 >> 32) == (unsigned)tg && (unsigned)(g1 >> 32) == (unsigned)tg)) break;
                __builtin_amdgcn_s_sleep(1);
                g0 = __hip_atomic_load(pr, RLX_AGENT); g1 = __hip_atomic_load(pr + 64, RLX_AGENT);
            }
            *(LAS unsigned*)(lds + L_ROWST + lane * 4) = (unsigned)g0; *(LAS unsigned*)(lds + L_ROWST + 256 + lane * 4) = (unsigned)g1;
            BAR(); SSTAMP(24); BAR(); SSTAMP(25); BAR(); SSTAMP(26); BAR(); SSTAMP(27); BAR(); SSTAMP(28);
            if (lane == 0) __hip_atomic_store((GAS u64*)(ws + WS_F1 + (mt * 16 + nt) * 128), tg, RLX_AGENT);
            BAR(); SSTAMP(29);
        }
        const u64 tag = *(const volatile LAS u64*)(lds + L_MISC);
        {
            lane = lane_id();
            const int l16 = lane & 15;
            wait_flags(ws, WS_F1 + ((lane < 32) ? l16 * 16 + ((lane & 16) ? ntv : ntk) : (2 * qt + (lane & 1)) * 16 + ntq) * 128, tag);
            STAMP(3);
            const u32x4 tq0 = __builtin_amdgcn_raw_buffer_load_b128(wsr, WS_Q + head * 65536 + qt * 8192 + j * 2048 + lane * 16, 0, 16);
            const u32x4 tq1 = __builtin_amdgcn_raw_buffer_load_b128(wsr, WS_Q + head * 65536 + qt * 8192 + j * 2048 + lane * 16, 1024, 16);
            const unsigned tk = __builtin_amdgcn_raw_buffer_load_b32(wsr, WS_KMAX + (head * 64 + 16 * j + l16) * 4, 0, 16);
            u32x4 tk4[16], tv4[16];
#pragma unroll
            for (int i = 0; i < 16; ++i) {
                tk4[i] = __builtin_amdgcn_raw_buffer_load_b128(wsr, WS_K + head * 65536 + j * 4096 + lane * 16, (i >> 2) * 16384 + (i & 3) * 1024, 16);
                tv4[i] = __builtin_amdgcn_raw_buffer_load_b128(wsr, WS_V + head * 65536 + j * 4096 + lane * 16, (i >> 2) * 16384 + (i & 3) * 1024, 16);
            }
            lds_wait(lds, 0, 8);
            *(LAS u32x4*)(lds + L_V + (32 * j + (lane >> 1)) * VT + 512 + (lane & 1) * 16) = u32x4{(lane & 1) ? 0u : 0x3f80u, 0u, 0u, 0u};
            *(LAS u32x4*)(lds + L_Q + j * 2048 + lane * 16) = tq0; *(LAS u32x4*)(lds + L_Q + j * 2048 + 1024 + lane * 16) = tq1;
            if (lane < 16) *(LAS unsigned*)(lds + L_KMAX + (16 * j + lane) * 4) = tk;
            ldsp kd = lds + L_K + j * 4096 + lane * 16, vd = lds + L_V + (8 * j + ((lane >> 4) & 1)) * VT + l16 * 32 + (lane >> 5) * 16;
#pragma unroll
            for (int i = 0; i < 16; ++i) {
                *(LAS u32x4*)(kd + (i >> 2) * 16384 + (i & 3) * 1024) = tk4[i]; *(LAS u32x4*)(vd + ((i >> 2) * 32 + (i & 3) * 2) * VT) = tv4[i];
                if (i == 3) { STAMP(4); BAR(); }
            }
            asm volatile("s_waitcnt lgkmcnt(0)" ::: "memory");
            lds_count(lds, 3, lane);
        }
        __builtin_amdgcn_s_setprio(3);
        lane = lane_id();
        const int G = lane >> 4, tq = (lane & 15) >> 2, tp = lane & 3, wrow = lane >> 3, wch = (lane & 7) ^ (((wrow >> 1) & 1) << 2);
        f32x16 acc[2][2] = {};
        {
            G2Ctx gc;
            { ldsp stage = lds + L_G2 + j * 4096; ldsp rd = stage + (8 * (G >> 1) + tq) * 128 + 32 * (G & 1) + 8 * tp;
              const int sw = ((tq >> 1) & 1) * 64; gc.wr = stage + lane * 16; gc.rd0 = rd + sw; gc.rd1 = rd + (sw ^ 64); }
            constexpr int DEPTH = 4;
            const __amdgpu_buffer_rsrc_t w2r = __builtin_amdgcn_make_buffer_rsrc((void*)w2, 0, FF * DM * 2, 0x00020000);
            const unsigned wv = (448 * j + wrow) * 1024 + ct * 128 + wch * 16, av = WS_H + (unsigned)(2 * rt * 112 + 28 * j) * 1024 + lane * 16;
            u32x4 wr[DEPTH][2], ar[DEPTH][2];
#pragma unroll
            for (int d = 0; d < DEPTH; ++d) { wr[d][0] = __builtin_amdgcn_raw_buffer_load_b128(w2r, wv, d * 16384, 0); wr[d][1] = __builtin_amdgcn_raw_buffer_load_b128(w2r, wv + 8192, d * 16384, 0); }
            if (j == 1) { lds_wait(lds, 4, 512); STAMP(7); if (lane == 0) __hip_atomic_store((GAS u64*)(ws + WS_F2 + (mt * 16 + nt) * 128), tag, RLX_AGENT); }
            wait_flags(ws, WS_F2 + ((rt >> 1) * 16 + 4 * j + (lane & 3)) * 128, tag);
            STAMP(5);
#pragma unroll
            for (int d = 0; d < DEPTH; ++d) { ar[d][0] = __builtin_amdgcn_raw_buffer_load_b128(wsr, av, d * 1024, 16); ar[d][1] = __builtin_amdgcn_raw_buffer_load_b128(wsr, av + 112 * 1024, d * 1024, 16); }
            __builtin_amdgcn_s_setprio(G2PRIO);
            bf16x8 wf[2][2];
            g2_wfrag(gc, 0, wr[0][0], wr[0][1], wf[0][0], wf[0][1]);
            wr[0][0] = __builtin_amdgcn_raw_buffer_load_b128(w2r, wv, DEPTH * 16384, 0); wr[0][1] = __builtin_amdgcn_raw_buffer_load_b128(w2r, wv + 8192, DEPTH * 16384, 0);
#pragma unroll 1
            for (int s0 = 0; s0 < 28; s0 += DEPTH) {
#pragma unroll
                for (int d = 0; d < DEPTH; ++d) {
                    const int dn = (d + 1) % DEPTH;
                    g2_wfrag(gc, (d + 1) & 1, wr[dn][0], wr[dn][1], wf[(d + 1) & 1][0], wf[(d + 1) & 1][1]);
                    wr[dn][0] = __builtin_amdgcn_raw_buffer_load_b128(w2r, wv, (s0 + d + 1 + DEPTH) * 16384, 0); wr[dn][1] = __builtin_amdgcn_raw_buffer_load_b128(w2r, wv + 8192, (s0 + d + 1 + DEPTH) * 16384, 0);
                    __builtin_amdgcn_sched_barrier(0);
                    g2_mma<G2SLEEP>(acc, wf[d & 1][0], wf[d & 1][1], ar[d][0], ar[d][1]);
                    __builtin_amdgcn_sched_barrier(0);
                    const int sa = (s0 + d + DEPTH < 28 ? s0 + d + DEPTH : 27) * 1024;
                    ar[d][0] = __builtin_amdgcn_raw_buffer_load_b128(wsr, av, sa, 16); ar[d][1] = __builtin_amdgcn_raw_buffer_load_b128(wsr, av + 112 * 1024, sa, 16);
                    __builtin_amdgcn_sched_barrier(0);
                }
            }
        }
        STAMP(6);
        if (blockIdx.x == 0 && j == 3) {
#pragma unroll 1
            for (int i = 0; i < 4; ++i) wait_flags(ws, WS_F1 + (i * 64 + lane) * 128, tag);
            if (lane == 0) __hip_atomic_store((GAS u64*)(ws + WS_EPOCH), tag, RLX_AGENT);
        }
        lds_wait(lds, 2, 8);
#pragma unroll
        for (int a = 0; a < 2; ++a)
#pragma unroll
            for (int b = 0; b < 2; ++b)
#pragma unroll
                for (int r4 = 0; r4 < 4; ++r4)
                    *(LAS f32x4*)(lds + j * (64 * RED_PITCH) + (32 * b + (lane & 31)) * RED_PITCH + (32 * a + 8 * r4 + 4 * (lane >> 5)) * 4) =
                        f32x4{acc[a][b][4 * r4], acc[a][b][4 * r4 + 1], acc[a][b][4 * r4 + 2], acc[a][b][4 * r4 + 3]};
        BAR();
    }
    STAMP(9); CSTAMP(11);
}

extern "C" void kernel_launch(void* const* d_in, const int* in_sizes, int n_in, void* d_out, int out_size, void* d_ws, size_t ws_size, hipStream_t stream) {
    (void)in_sizes; (void)n_in; (void)out_size; (void)ws_size;
    (void)hipFuncSetAttribute((const void*)fused_block_kernel, hipFuncAttributeMaxDynamicSharedMemorySize, LDS_BYTES);
    fused_block_kernel<<<256, 768, LDS_BYTES, stream>>>((const __hip_bfloat16*)d_in[0], (const __hip_bfloat16*)d_in[1], (const __hip_bfloat16*)d_in[2],
                                                       (const __hip_bfloat16*)d_in[4], (const __hip_bfloat16*)d_in[5], (const __hip_bfloat16*)d_in[6],
                                                       (const __hip_bfloat16*)d_in[7], (__hip_bfloat16*)d_out, (char*)d_ws);
}
```

```cpp
#include <hip/hip_runtime.h>
#include <hip/hip_bf16.h>
#include <stdint.h>

typedef __bf16 bf16x2 __attribute__((ext_vector_type(2)));
typedef __bf16 bf16x8 __attribute__((ext_vector_type(8)));
typedef __bf16 bf16x16 __attribute__((ext_vector_type(16)));
typedef float f32x2 __attribute__((ext_vector_type(2)));
typedef float f32x4 __attribute__((ext_vector_type(4)));
typedef float f32x16 __attribute__((ext_vector_type(16)));
typedef unsigned u32x2 __attribute__((ext_vector_type(2)));
typedef unsigned u32x4 __attribute__((ext_vector_type(4)));
typedef short s16x4 __attribute__((ext_vector_type(4)));
typedef short s16x8 __attribute__((ext_vector_type(8)));
typedef unsigned long long u64;
#define LAS __attribute__((address_space(3)))
#define GAS __attribute__((address_space(1)))
typedef LAS char* ldsp;

constexpr int DM = 512;
constexpr unsigned WS_EPOCH = 0, WS_F1 = 4096, WS_F2 = WS_F1 + 32768, WS_KMAX = WS_F2 + 32768;
constexpr unsigned WS_RS = WS_KMAX + 8192;
constexpr unsigned WS_Q = 1u << 20, WS_K = 3u << 20, WS_V = 5u << 20, WS_H = 8u << 20, WS_O = 16u << 20, WS_WT = 18u << 20, WS_BYTES = 21u << 20;
constexpr unsigned VT = 544;
constexpr int SLOT = 30720, OFF_B = 16384, NSLOT = 5;
constexpr int L_ROWST = NSLOT * SLOT, L_COLS = L_ROWST + 512, L_BIAS = L_COLS + 1792, L_WT = L_BIAS + 256;
constexpr int L_K = 0, L_V = 65536, L_Q = 135168, L_NB = 143360;
__device__ constexpr int wo_stage(int kq) { return (kq < 2 ? L_K : L_V - 32768) + kq * 16384; }
__device__ constexpr int red_slice(int i) { return i < 3 ? 32768 + i * 8704 : 98304 + (i - 3) * 8704; }
constexpr int L_MISC = 163840 - 64, LDS_BYTES = 163840;
constexpr int RED_PITCH = 272;
constexpr float QSCALE = 0.25f * 1.4426950408889634f;
constexpr unsigned CODE_BYTES = 42 * 1024;
constexpr unsigned POISON = 0xFFFFFFFFu;
constexpr unsigned SPIN_CAP = 1u << 18;

#define MFMA32(a, b, c) __builtin_amdgcn_mfma_f32_32x32x16_bf16(a, b, c, 0, 0, 0)
#define SMF(a, b, c, i) __builtin_amdgcn_smfmac_f32_32x32x32_bf16(a, b, c, i, 0, 0)
#define BAR() asm volatile("s_waitcnt lgkmcnt(0)\n\ts_barrier" ::: "memory")
#define GSB() __builtin_amdgcn_sched_barrier(0)
#define WAITV(n) asm volatile("s_waitcnt vmcnt(" #n ")" ::: "memory")
#define RLX_AGENT __ATOMIC_RELAXED, __HIP_MEMORY_SCOPE_AGENT

__device__ __forceinline__ bf16x8 ld_b128(ldsp p) { return *(const LAS bf16x8*)p; }
__device__ __forceinline__ bf16x8 ld_tr2(ldsp p0, ldsp p1) {
    s16x4 lo = __builtin_amdgcn_ds_read_tr16_b64_v4i16((LAS s16x4*)p0);
    s16x4 hi = __builtin_amdgcn_ds_read_tr16_b64_v4i16((LAS s16x4*)p1);
    s16x8 t = __builtin_shufflevector(lo, hi, 0, 1, 2, 3, 4, 5, 6, 7);
    return __builtin_bit_cast(bf16x8, t);
}
__device__ __forceinline__ unsigned pk2(float a, float b) { f32x2 v = {a, b}; return __builtin_bit_cast(unsigned, __builtin_convertvector(v, bf16x2)); }
__device__ __forceinline__ float bf_lo(unsigned u) { return __uint_as_float(u << 16); }
__device__ __forceinline__ float bf_hi(unsigned u) { return __uint_as_float(u & 0xffff0000u); }
__device__ __forceinline__ float rbf(float a) { return (float)(__bf16)a; }
__device__ __forceinline__ float dot2(unsigned a, unsigned b, float c) { return __builtin_amdgcn_fdot2_f32_bf16(__builtin_bit_cast(bf16x2, a), __builtin_bit_cast(bf16x2, b), c, false); }
__device__ __forceinline__ float sum_x16(float v) { auto r = __builtin_amdgcn_permlane16_swap(__float_as_uint(v), __float_as_uint(v), false, false); return __uint_as_float(r[0]) + __uint_as_float(r[1]); }
__device__ __forceinline__ float sum_x32(float v) { auto r = __builtin_amdgcn_permlane32_swap(__float_as_uint(v), __float_as_uint(v), false, false); return __uint_as_float(r[0]) + __uint_as_float(r[1]); }
__device__ __forceinline__ float max_x32(float v) { auto r = __builtin_amdgcn_permlane32_swap(__float_as_uint(v), __float_as_uint(v), false, false); return fmaxf(__uint_as_float(r[0]), __uint_as_float(r[1])); }
template <int N> __device__ __forceinline__ float ror16(float v) { return __uint_as_float(__builtin_amdgcn_update_dpp(0u, __float_as_uint(v), 0x120 + N, 0xf, 0xf, false)); }
template <int N> __device__ __forceinline__ unsigned umax_ror(unsigned v) { const unsigned o = __builtin_amdgcn_update_dpp(0u, v, 0x120 + N, 0xf, 0xf, false); return v > o ? v : o; }
__device__ __forceinline__ unsigned umax4(const u32x4& v) { const unsigned a = v[0] > v[1] ? v[0] : v[1], b = v[2] > v[3] ? v[2] : v[3]; return a > b ? a : b; }
__device__ __forceinline__ unsigned umax_32lanes(unsigned v) {
    v = umax_ror<1>(umax_ror<2>(umax_ror<4>(umax_ror<8>(v))));
    const auto r = __builtin_amdgcn_permlane16_swap(v, v, false, false); return r[0] > r[1] ? r[0] : r[1];
}

__device__ __forceinline__ int lane_id() { int l; asm volatile("v_mbcnt_lo_u32_b32 %0, -1, 0\n\tv_mbcnt_hi_u32_b32 %0, -1, %0" : "=v"(l)); return l; }
__device__ __forceinline__ void lds_count(ldsp lds, int ctr, int lane) { if (lane == 0) __hip_atomic_fetch_add((LAS unsigned*)(lds + L_MISC + 16 + 4 * ctr), 1u, __ATOMIC_RELAXED, __HIP_MEMORY_SCOPE_WORKGROUP); }
template <int SLEEP = 1>
__device__ __forceinline__ void lds_wait(ldsp lds, int ctr, unsigned n) {
#pragma unroll 1
    for (unsigned spins = 0; spins < (SPIN_CAP << 4); ++spins) {
        if (__builtin_amdgcn_readfirstlane(*(const volatile LAS unsigned*)(lds + L_MISC + 16 + 4 * ctr)) >= n) break;
        __builtin_amdgcn_s_sleep(SLEEP);
    }
    asm volatile("" ::: "memory");
}
__device__ __forceinline__ void glds16(const char* g, ldsp l) { __builtin_amdgcn_global_load_lds((const GAS unsigned*)g, (LAS unsigned*)l, 16, 0, 0); }

__device__ __forceinline__ void wait_flags(const char* ws, unsigned off, u64 tag) {
    const GAS u64* p = (const GAS u64*)(ws + off);
#pragma unroll 1
    for (unsigned spins = 0; spins < SPIN_CAP; ++spins) {
        const u64 v = __hip_atomic_load(p, RLX_AGENT);
        if (__all(v == tag)) break;
        __builtin_amdgcn_s_sleep(2);
    }
    asm volatile("" ::: "memory");
}

__device__ __forceinline__ u32x4 widen(u32x2 d0, u32x2 d1) {
    const auto a = __builtin_amdgcn_permlane16_swap(d0[0], d1[0], false, false), b = __builtin_amdgcn_permlane16_swap(d0[1], d1[1], false, false);
    return u32x4{a[0], b[0], a[1], b[1]};
}
__device__ __forceinline__ float sum_row16(float v) { v += ror16<8>(v); v += ror16<4>(v); v += ror16<2>(v); return v + ror16<1>(v); }
__device__ __forceinline__ u64 call_tag(const char* ws) {
    return __hip_atomic_load((const GAS u64*)(ws + WS_EPOCH), RLX_AGENT) * 6364136223846793005ull + 1442695040888963407ull;
}
__device__ __forceinline__ void g1_rowstat(char* ws, const char* x, int row, int lane) {
    const u32x4 xr = *(const GAS u32x4*)(x + (size_t)row * 1024 + lane * 16);
    const u64 tag = call_tag(ws);
    float e[8], s = 0.f;
#pragma unroll
    for (int i = 0; i < 4; ++i) { e[2 * i] = bf_lo(xr[i]); e[2 * i + 1] = bf_hi(xr[i]); s += e[2 * i] + e[2 * i + 1]; }
    const float mu = sum_x32(sum_x16(sum_row16(s))) * (1.0f / DM);
    float v = 0.f;
#pragma unroll
    for (int i = 0; i < 8; ++i) v = __builtin_fmaf(e[i] - mu, e[i] - mu, v);
    v = sum_x32(sum_x16(sum_row16(v))) * (1.0f / DM);
    if (lane == 0) __hip_atomic_store((GAS u64*)(ws + WS_RS) + row, (tag << 32) | pk2(mu, __builtin_amdgcn_sqrtf(rbf(rbf(v) + 1e-5f))), RLX_AGENT);
}

#define G1_QKV0 \
    "v_add_u32 v58, 61440, %15\n" \
    "v_add_u32 v59, 61440, %16\n" \
    "v_add_u32 v62, 61440, %17\n" \
    "v_add_u32 v63, 61440, %18\n" \
    "v_add_u32 v66, 61440, %19\n" \
    "v_add_u32 v60, 122880, %15\n" \
    "v_add_u32 v61, 122880, %16\n" \
    "v_add_u32 v64, 122880, %17\n" \
    "v_add_u32 v65, 122880, %18\n" \
    "v_add_u32 v67, 122880, %19\n" \
    "v_mov_b32 v68, 0x3f803f80\n" \
    "v_mov_b32 v69, 0x3f803f80\n" \
    "v_mov_b32 v70, 0x3f803f80\n" \
    "v_mov_b32 v71, 0x3f803f80\n" \
    "s_barrier\n" \
    "ds_read_b128 v[72:75], %15 offset:0\n" \
    "ds_read_b64_tr_b16 v[80:81], %17 offset:0\n" \
    "ds_read_b64_tr_b16 v[82:83], %17 offset:768\n" \
    "ds_read_b128 v[76:79], %15 offset:2048\n" \
    "ds_read_b64_tr_b16 v[84:85], %18 offset:0\n" \
    "ds_read_b64_tr_b16 v[86:87], %18 offset:768\n" \
    "ds_read_b64_tr_b16 v[88:89], %17 offset:64\n" \
    "ds_read_b64_tr_b16 v[90:91], %17 offset:832\n" \
    "ds_read_b128 v[92:95], %16 offset:0\n" \
    "ds_read_b64_tr_b16 v[100:101], %17 offset:6144\n" \
    "ds_read_b64_tr_b16 v[102:103], %17 offset:6912\n" \
    "ds_read_b128 v[96:99], %16 offset:2048\n" \
    "ds_read_b64_tr_b16 v[104:105], %18 offset:6144\n" \
    "ds_read_b64_tr_b16 v[106:107], %18 offset:6912\n" \
    "ds_read_b64_tr_b16 v[108:109], %17 offset:6208\n" \
    "ds_read_b64_tr_b16 v[110:111], %17 offset:6976\n" \
    "s_waitcnt lgkmcnt(0)\n" \
    "s_barrier\n" \
    "v_mfma_f32_16x16x32_bf16 %0, v[80:83], v[72:75], 0\n" \
    "ds_read_b128 v[120:123], %15 offset:30720\n" \
    "ds_read_b64_tr_b16 v[128:129], %17 offset:30720\n" \
    "v_mfma_f32_16x16x32_bf16 %1, v[80:83], v[76:79], 0\n" \
    "ds_read_b64_tr_b16 v[130:131], %17 offset:31488\n" \
    "ds_read_b128 v[124:127], %15 offset:32768\n" \
    "v_mfma_f32_16x16x32_bf16 %2, v[84:87], v[72:75], 0\n" \
    "ds_read_b64_tr_b16 v[132:133], %18 offset:30720\n" \
    "ds_read_b64_tr_b16 v[134:135], %18 offset:31488\n" \
    "v_mfma_f32_16x16x32_bf16 %3, v[84:87], v[76:79], 0\n" \
    "ds_read_b64_tr_b16 v[136:137], %17 offset:30784\n" \
    "ds_read_b64_tr_b16 v[138:139], %17 offset:31552\n" \
    "v_mfma_f32_16x16x32_bf16 %4, v[88:91], v[72:75], 0\n" \
    "ds_read_b128 v[140:143], %16 offset:30720\n" \
    "v_mfma_f32_16x16x32_bf16 %5, v[88:91], v[76:79], 0\n" \
    "ds_read_b64_tr_b16 v[148:149], %17 offset:36864\n" \
    "v_mfma_f32_16x16x32_bf16 %0, v[100:103], v[92:95], %0\n" \
    "ds_read_b64_tr_b16 v[150:151], %17 offset:37632\n" \
    "v_mfma_f32_16x16x32_bf16 %1, v[100:103], v[96:99], %1\n" \
    "ds_read_b128 v[144:147], %16 offset:32768\n" \
    "v_mfma_f32_16x16x32_bf16 %2, v[104:107], v[92:95], %2\n" \
    "ds_read_b64_tr_b16 v[152:153], %18 offset:36864\n" \
    "v_mfma_f32_16x16x32_bf16 %3, v[104:107], v[96:99], %3\n" \
    "ds_read_b64_tr_b16 v[154:155], %18 offset:37632\n" \
    "v_mfma_f32_16x16x32_bf16 %4, v[108:111], v[92:95], %4\n" \
    "ds_read_b64_tr_b16 v[156:157], %17 offset:36928\n" \
    "v_mfma_f32_16x16x32_bf16 %5, v[108:111], v[96:99], %5\n" \
    "ds_read_b64_tr_b16 v[158:159], %17 offset:37696\n" \
    "s_cmp_lg_u32 %21, 0\n" \
    "s_cbranch_scc1 G1C00_%=\n" \
    "v_mfma_f32_16x16x32_bf16 %6, v[80:83], v[68:71], %6\n" \
    "v_mfma_f32_16x16x32_bf16 %6, v[100:103], v[68:71], %6\n" \
    "s_branch G1C0E_%=\n" \
    "G1C00_%=:\n" \
    "s_cmp_lg_u32 %21, 1\n" \
    "s_cbranch_scc1 G1C01_%=\n" \
    "v_mfma_f32_16x16x32_bf16 %6, v[84:87], v[68:71], %6\n" \
    "v_mfma_f32_16x16x32_bf16 %6, v[104:107], v[68:71], %6\n" \
    "s_branch G1C0E_%=\n" \
    "G1C01_%=:\n" \
    "s_cmp_lg_u32 %21, 2\n" \
    "s_cbranch_scc1 G1C02_%=\n" \
    "v_mfma_f32_16x16x32_bf16 %6, v[88:91], v[68:71], %6\n" \
    "v_mfma_f32_16x16x32_bf16 %6, v[108:111], v[68:71], %6\n" \
    "G1C02_%=:\n" \
    "G1C0E_%=:\n" \
    "s_waitcnt lgkmcnt(0)\n" \
    "s_barrier\n" \
    "v_mfma_f32_16x16x32_bf16 %0, v[128:131], v[120:123], %0\n" \
    "ds_read_b128 v[72:75], v58 offset:0\n" \
    "ds_read_b64_tr_b16 v[80:81], v62 offset:0\n" \
    "v_mfma_f32_16x16x32_bf16 %1, v[128:131], v[124:127], %1\n" \
    "ds_read_b64_tr_b16 v[82:83], v62 offset:768\n" \
    "ds_read_b128 v[76:79], v58 offset:2048\n" \
    "v_mfma_f32_16x16x32_bf16 %2, v[132:135], v[120:123], %2\n" \
    "ds_read_b64_tr_b16 v[84:85], v63 offset:0\n" \
    "ds_read_b64_tr_b16 v[86:87], v63 offset:768\n" \
    "v_mfma_f32_16x16x32_bf16 %3, v[132:135], v[124:127], %3\n" \
    "ds_read_b64_tr_b16 v[88:89], v62 offset:64\n" \
    "ds_read_b64_tr_b16 v[90:91], v62 offset:832\n" \
    "v_mfma_f32_16x16x32_bf16 %4, v[136:139], v[120:123], %4\n" \
    "ds_read_b128 v[92:95], v59 offset:0\n" \
    "v_mfma_f32_16x16x32_bf16 %5, v[136:139], v[124:127], %5\n" \
    "ds_read_b64_tr_b16 v[100:101], v62 offset:6144\n" \
    "v_mfma_f32_16x16x32_bf16 %0, v[148:151], v[140:143], %0\n" \
    "ds_read_b64_tr_b16 v[102:103], v62 offset:6912\n" \
    "v_mfma_f32_16x16x32_bf16 %1, v[148:151], v[144:147], %1\n" \
    "ds_read_b128 v[96:99], v59 offset:2048\n" \
    "v_mfma_f32_16x16x32_bf16 %2, v[152:155], v[140:143], %2\n" \
    "ds_read_b64_tr_b16 v[104:105], v63 offset:6144\n" \
    "v_mfma_f32_16x16x32_bf16 %3, v[152:155], v[144:147], %3\n" \
    "ds_read_b64_tr_b16 v[106:107], v63 offset:6912\n" \
    "v_mfma_f32_16x16x32_bf16 %4, v[156:159], v[140:143], %4\n" \
    "ds_read_b64_tr_b16 v[108:109], v62 offset:6208\n" \
    "v_mfma_f32_16x16x32_bf16 %5, v[156:159], v[144:147], %5\n" \
    "ds_read_b64_tr_b16 v[110:111], v62 offset:6976\n" \
    "s_cmp_lg_u32 %21, 0\n" \
    "s_cbranch_scc1 G1C10_%=\n" \
    "v_mfma_f32_16x16x32_bf16 %6, v[128:131], v[68:71], %6\n" \
    "v_mfma_f32_16x16x32_bf16 %6, v[148:151], v[68:71], %6\n" \
    "s_branch G1C1E_%=\n" \
    "G1C10_%=:\n" \
    "s_cmp_lg_u32 %21, 1\n" \
    "s_cbranch_scc1 G1C11_%=\n" \
    "v_mfma_f32_16x16x32_bf16 %6, v[132:135], v[68:71], %6\n" \
    "v_mfma_f32_16x16x32_bf16 %6, v[152:155], v[68:71], %6\n" \
    "s_branch G1C1E_%=\n" \
    "G1C11_%=:\n" \
    "s_cmp_lg_u32 %21, 2\n" \
    "s_cbranch_scc1 G1C12_%=\n" \
    "v_mfma_f32_16x16x32_bf16 %6, v[136:139], v[68:71], %6\n" \
    "v_mfma_f32_16x16x32_bf16 %6, v[156:159], v[68:71], %6\n" \
    "G1C12_%=:\n" \
    "G1C1E_%=:\n" \
    "s_waitcnt lgkmcnt(0)\n" \
    "s_barrier\n" \
    "v_mfma_f32_16x16x32_bf16 %0, v[80:83], v[72:75], %0\n" \
    "ds_read_b128 v[120:123], v58 offset:30720\n" \
    "ds_read_b64_tr_b16 v[128:129], v62 offset:30720\n" \
    "v_mfma_f32_16x16x32_bf16 %1, v[80:83], v[76:79], %1\n" \
    "ds_read_b64_tr_b16 v[130:131], v62 offset:31488\n" \
    "ds_read_b128 v[124:127], v58 offset:32768\n" \
    "v_mfma_f32_16x16x32_bf16 %2, v[84:87], v[72:75], %2\n" \
    "ds_read_b64_tr_b16 v[132:133], v63 offset:30720\n" \
    "ds_read_b64_tr_b16 v[134:135], v63 offset:31488\n" \
    "v_mfma_f32_16x16x32_bf16 %3, v[84:87], v[76:79], %3\n" \
    "ds_read_b64_tr_b16 v[136:137], v62 offset:30784\n" \
    "ds_read_b64_tr_b16 v[138:139], v62 offset:31552\n" \
    "v_mfma_f32_16x16x32_bf16 %4, v[88:91], v[72:75], %4\n" \
    "ds_read_b128 v[140:143], v59 offset:30720\n" \
    "v_mfma_f32_16x16x32_bf16 %5, v[88:91], v[76:79], %5\n" \
    "ds_read_b64_tr_b16 v[148:149], v62 offset:36864\n" \
    "v_mfma_f32_16x16x32_bf16 %0, v[100:103], v[92:95], %0\n" \
    "ds_read_b64_tr_b16 v[150:151], v62 offset:37632\n" \
    "v_mfma_f32_16x16x32_bf16 %1, v[100:103], v[96:99], %1\n" \
    "ds_read_b128 v[144:147], v59 offset:32768\n" \
    "v_mfma_f32_16x16x32_bf16 %2, v[104:107], v[92:95], %2\n" \
    "ds_read_b64_tr_b16 v[152:153], v63 offset:36864\n" \
    "v_mfma_f32_16x16x32_bf16 %3, v[104:107], v[96:99], %3\n" \
    "ds_read_b64_tr_b16 v[154:155], v63 offset:37632\n" \
    "v_mfma_f32_16x16x32_bf16 %4, v[108:111], v[92:95], %4\n" \
    "ds_read_b64_tr_b16 v[156:157], v62 offset:36928\n" \
    "v_mfma_f32_16x16x32_bf16 %5, v[108:111], v[96:99], %5\n" \
    "ds_read_b64_tr_b16 v[158:159], v62 offset:37696\n" \
    "s_cmp_lg_u32 %21, 0\n" \
    "s_cbranch_scc1 G1C20_%=\n" \
    "v_mfma_f32_16x16x32_bf16 %6, v[80:83], v[68:71], %6\n" \
    "v_mfma_f32_16x16x32_bf16 %6, v[100:103], v[68:71], %6\n" \
    "s_branch G1C2E_%=\n" \
    "G1C20_%=:\n" \
    "s_cmp_lg_u32 %21, 1\n" \
    "s_cbranch_scc1 G1C21_%=\n" \
    "v_mfma_f32_16x16x32_bf16 %6, v[84:87], v[68:71], %6\n" \
    "v_mfma_f32_16x16x32_bf16 %6, v[104:107], v[68:71], %6\n" \
    "s_branch G1C2E_%=\n" \
    "G1C21_%=:\n" \
    "s_cmp_lg_u32 %21, 2\n" \
    "s_cbranch_scc1 G1C22_%=\n" \
    "v_mfma_f32_16x16x32_bf16 %6, v[88:91], v[68:71], %6\n" \
    "v_mfma_f32_16x16x32_bf16 %6, v[108:111], v[68:71], %6\n" \
    "G1C22_%=:\n" \
    "G1C2E_%=:\n" \
    "s_waitcnt lgkmcnt(0)\n" \
    "s_barrier\n" \
    "v_mfma_f32_16x16x32_bf16 %0, v[128:131], v[120:123], %0\n" \
    "ds_read_b128 v[72:75], v60 offset:0\n" \
    "ds_read_b64_tr_b16 v[80:81], v64 offset:0\n" \
    "v_mfma_f32_16x16x32_bf16 %1, v[128:131], v[124:127], %1\n" \
    "ds_read_b64_tr_b16 v[82:83], v64 offset:768\n" \
    "ds_read_b128 v[76:79], v60 offset:2048\n" \
    "v_mfma_f32_16x16x32_bf16 %2, v[132:135], v[120:123], %2\n" \
    "ds_read_b64_tr_b16 v[84:85], v65 offset:0\n" \
    "ds_read_b64_tr_b16 v[86:87], v65 offset:768\n" \
    "v_mfma_f32_16x16x32_bf16 %3, v[132:135], v[124:127], %3\n" \
    "ds_read_b64_tr_b16 v[88:89], v64 offset:64\n" \
    "ds_read_b64_tr_b16 v[90:91], v64 offset:832\n" \
    "v_mfma_f32_16x16x32_bf16 %4, v[136:139], v[120:123], %4\n" \
    "ds_read_b128 v[92:95], v61 offset:0\n" \
    "v_mfma_f32_16x16x32_bf16 %5, v[136:139], v[124:127], %5\n" \
    "ds_read_b64_tr_b16 v[100:101], v64 offset:6144\n" \
    "v_mfma_f32_16x16x32_bf16 %0, v[148:151], v[140:143], %0\n" \
    "ds_read_b64_tr_b16 v[102:103], v64 offset:6912\n" \
    "v_mfma_f32_16x16x32_bf16 %1, v[148:151], v[144:147], %1\n" \
    "ds_read_b128 v[96:99], v61 offset:2048\n" \
    "v_mfma_f32_16x16x32_bf16 %2, v[152:155], v[140:143], %2\n" \
    "ds_read_b64_tr_b16 v[104:105], v65 offset:6144\n" \
    "v_mfma_f32_16x16x32_bf16 %3, v[152:155], v[144:147], %3\n" \
    "ds_read_b64_tr_b16 v[106:107], v65 offset:6912\n" \
    "v_mfma_f32_16x16x32_bf16 %4, v[156:159], v[140:143], %4\n" \
    "ds_read_b64_tr_b16 v[108:109], v64 offset:6208\n" \
    "v_mfma_f32_16x16x32_bf16 %5, v[156:159], v[144:147], %5\n" \
    "ds_read_b64_tr_b16 v[110:111], v64 offset:6976\n" \
    "s_cmp_lg_u32 %21, 0\n" \
    "s_cbranch_scc1 G1C30_%=\n" \
    "v_mfma_f32_16x16x32_bf16 %6, v[128:131], v[68:71], %6\n" \
    "v_mfma_f32_16x16x32_bf16 %6, v[148:151], v[68:71], %6\n" \
    "s_branch G1C3E_%=\n" \
    "G1C30_%=:\n" \
    "s_cmp_lg_u32 %21, 1\n" \
    "s_cbranch_scc1 G1C31_%=\n" \
    "v_mfma_f32_16x16x32_bf16 %6, v[132:135], v[68:71], %6\n" \
    "v_mfma_f32_16x16x32_bf16 %6, v[152:155], v[68:71], %6\n" \
    "s_branch G1C3E_%=\n" \
    "G1C31_%=:\n" \
    "s_cmp_lg_u32 %21, 2\n" \
    "s_cbranch_scc1 G1C32_%=\n" \
    "v_mfma_f32_16x16x32_bf16 %6, v[136:139], v[68:71], %6\n" \
    "v_mfma_f32_16x16x32_bf16 %6, v[156:159], v[68:71], %6\n" \
    "G1C32_%=:\n" \
    "G1C3E_%=:\n" \
    "s_waitcnt lgkmcnt(0)\n" \
    "s_barrier\n" \
    "v_mfma_f32_16x16x32_bf16 %0, v[80:83], v[72:75], %0\n" \
    "ds_read_b128 v[120:123], %15 offset:0\n" \
    "ds_read_b64_tr_b16 v[128:129], %17 offset:0\n" \
    "v_mfma_f32_16x16x32_bf16 %1, v[80:83], v[76:79], %1\n" \
    "ds_read_b64_tr_b16 v[130:131], %17 offset:768\n" \
    "ds_read_b128 v[124:127], %15 offset:2048\n" \
    "v_mfma_f32_16x16x32_bf16 %2, v[84:87], v[72:75], %2\n" \
    "ds_read_b64_tr_b16 v[132:133], %18 offset:0\n" \
    "ds_read_b64_tr_b16 v[134:135], %18 offset:768\n" \
    "v_mfma_f32_16x16x32_bf16 %3, v[84:87], v[76:79], %3\n" \
    "ds_read_b64_tr_b16 v[136:137], %17 offset:64\n" \
    "ds_read_b64_tr_b16 v[138:139], %17 offset:832\n" \
    "v_mfma_f32_16x16x32_bf16 %4, v[88:91], v[72:75], %4\n" \
    "ds_read_b128 v[140:143], %16 offset:0\n" \
    "v_mfma_f32_16x16x32_bf16 %5, v[88:91], v[76:79], %5\n" \
    "ds_read_b64_tr_b16 v[148:149], %17 offset:6144\n" \
    "v_mfma_f32_16x16x32_bf16 %0, v[100:103], v[92:95], %0\n" \
    "ds_read_b64_tr_b16 v[150:151], %17 offset:6912\n" \
    "v_mfma_f32_16x16x32_bf16 %1, v[100:103], v[96:99], %1\n" \
    "ds_read_b128 v[144:147], %16 offset:2048\n" \
    "v_mfma_f32_16x16x32_bf16 %2, v[104:107], v[92:95], %2\n" \
    "ds_read_b64_tr_b16 v[152:153], %18 offset:6144\n" \
    "v_mfma_f32_16x16x32_bf16 %3, v[104:107], v[96:99], %3\n" \
    "ds_read_b64_tr_b16 v[154:155], %18 offset:6912\n" \
    "v_mfma_f32_16x16x32_bf16 %4, v[108:111], v[92:95], %4\n" \
    "ds_read_b64_tr_b16 v[156:157], %17 offset:6208\n" \
    "v_mfma_f32_16x16x32_bf16 %5, v[108:111], v[96:99], %5\n" \
    "ds_read_b64_tr_b16 v[158:159], %17 offset:6976\n" \
    "s_cmp_lg_u32 %21, 0\n" \
    "s_cbranch_scc1 G1C40_%=\n" \
    "v_mfma_f32_16x16x32_bf16 %6, v[80:83], v[68:71], %6\n" \
    "v_mfma_f32_16x16x32_bf16 %6, v[100:103], v[68:71], %6\n" \
    "s_branch G1C4E_%=\n" \
    "G1C40_%=:\n" \
    "s_cmp_lg_u32 %21, 1\n" \
    "s_cbranch_scc1 G1C41_%=\n" \
    "v_mfma_f32_16x16x32_bf16 %6, v[84:87], v[68:71], %6\n" \
    "v_mfma_f32_16x16x32_bf16 %6, v[104:107], v[68:71], %6\n" \
    "s_branch G1C4E_%=\n" \
    "G1C41_%=:\n" \
    "s_cmp_lg_u32 %21, 2\n" \
    "s_cbranch_scc1 G1C42_%=\n" \
    "v_mfma_f32_16x16x32_bf16 %6, v[88:91], v[68:71], %6\n" \
    "v_mfma_f32_16x16x32_bf16 %6, v[108:111], v[68:71], %6\n" \
    "G1C42_%=:\n" \
    "G1C4E_%=:\n" \
    "s_waitcnt lgkmcnt(0)\n" \
    "s_barrier\n" \
    "v_mfma_f32_16x16x32_bf16 %0, v[128:131], v[120:123], %0\n" \
    "ds_read_b128 v[72:75], %15 offset:30720\n" \
    "ds_read_b64_tr_b16 v[80:81], %17 offset:30720\n" \
    "v_mfma_f32_16x16x32_bf16 %1, v[128:131], v[124:127], %1\n" \
    "ds_read_b64_tr_b16 v[82:83], %17 offset:31488\n" \
    "ds_read_b128 v[76:79], %15 offset:32768\n" \
    "v_mfma_f32_16x16x32_bf16 %2, v[132:135], v[120:123], %2\n" \
    "ds_read_b64_tr_b16 v[84:85], %18 offset:30720\n" \
    "ds_read_b64_tr_b16 v[86:87], %18 offset:31488\n" \
    "v_mfma_f32_16x16x32_bf16 %3, v[132:135], v[124:127], %3\n" \
    "ds_read_b64_tr_b16 v[88:89], %17 offset:30784\n" \
    "ds_read_b64_tr_b16 v[90:91], %17 offset:31552\n" \
    "v_mfma_f32_16x16x32_bf16 %4, v[136:139], v[120:123], %4\n" \
    "ds_read_b128 v[92:95], %16 offset:30720\n" \
    "v_mfma_f32_16x16x32_bf16 %5, v[136:139], v[124:127], %5\n" \
    "ds_read_b64_tr_b16 v[100:101], %17 offset:36864\n" \
    "v_mfma_f32_16x16x32_bf16 %0, v[148:151], v[140:143], %0\n" \
    "ds_read_b64_tr_b16 v[102:103], %17 offset:37632\n" \
    "v_mfma_f32_16x16x32_bf16 %1, v[148:151], v[144:147], %1\n" \
    "ds_read_b128 v[96:99], %16 offset:32768\n" \
    "v_mfma_f32_16x16x32_bf16 %2, v[152:155], v[140:143], %2\n" \
    "ds_read_b64_tr_b16 v[104:105], %18 offset:36864\n" \
    "v_mfma_f32_16x16x32_bf16 %3, v[152:155], v[144:147], %3\n" \
    "ds_read_b64_tr_b16 v[106:107], %18 offset:37632\n" \
    "v_mfma_f32_16x16x32_bf16 %4, v[156:159], v[140:143], %4\n" \
    "ds_read_b64_tr_b16 v[108:109], %17 offset:36928\n" \
    "v_mfma_f32_16x16x32_bf16 %5, v[156:159], v[144:147], %5\n" \
    "ds_read_b64_tr_b16 v[110:111], %17 offset:37696\n" \
    "s_cmp_lg_u32 %21, 0\n" \
    "s_cbranch_scc1 G1C50_%=\n" \
    "v_mfma_f32_16x16x32_bf16 %6, v[128:131], v[68:71], %6\n" \
    "v_mfma_f32_16x16x32_bf16 %6, v[148:151], v[68:71], %6\n" \
    "s_branch G1C5E_%=\n" \
    "G1C50_%=:\n" \
    "s_cmp_lg_u32 %21, 1\n" \
    "s_cbranch_scc1 G1C51_%=\n" \
    "v_mfma_f32_16x16x32_bf16 %6, v[132:135], v[68:71], %6\n" \
    "v_mfma_f32_16x16x32_bf16 %6, v[152:155], v[68:71], %6\n" \
    "s_branch G1C5E_%=\n" \
    "G1C51_%=:\n" \
    "s_cmp_lg_u32 %21, 2\n" \
    "s_cbranch_scc1 G1C52_%=\n" \
    "v_mfma_f32_16x16x32_bf16 %6, v[136:139], v[68:71], %6\n" \
    "v_mfma_f32_16x16x32_bf16 %6, v[156:159], v[68:71], %6\n" \
    "G1C52_%=:\n" \
    "G1C5E_%=:\n" \
    "s_waitcnt lgkmcnt(0)\n" \
    "s_barrier\n" \
    "v_mfma_f32_16x16x32_bf16 %0, v[80:83], v[72:75], %0\n" \
    "ds_read_b128 v[120:123], v58 offset:0\n" \
    "ds_read_b64_tr_b16 v[128:129], v62 offset:0\n" \
    "v_mfma_f32_16x16x32_bf16 %1, v[80:83], v[76:79], %1\n" \
    "ds_read_b64_tr_b16 v[130:131], v62 offset:768\n" \
    "ds_read_b128 v[124:127], v58 offset:2048\n" \
    "v_mfma_f32_16x16x32_bf16 %2, v[84:87], v[72:75], %2\n" \
    "ds_read_b64_tr_b16 v[132:133], v63 offset:0\n" \
    "ds_read_b64_tr_b16 v[134:135], v63 offset:768\n" \
    "v_mfma_f32_16x16x32_bf16 %3, v[84:87], v[76:79], %3\n" \
    "ds_read_b64_tr_b16 v[136:137], v62 offset:64\n" \
    "ds_read_b64_tr_b16 v[138:139], v62 offset:832\n" \
    "v_mfma_f32_16x16x32_bf16 %4, v[88:91], v[72:75], %4\n" \
    "ds_read_b128 v[140:143], v59 offset:0\n" \
    "v_mfma_f32_16x16x32_bf16 %5, v[88:91], v[76:79], %5\n" \
    "ds_read_b64_tr_b16 v[148:149], v62 offset:6144\n" \
    "v_mfma_f32_16x16x32_bf16 %0, v[100:103], v[92:95], %0\n" \
    "ds_read_b64_tr_b16 v[150:151], v62 offset:6912\n" \
    "v_mfma_f32_16x16x32_bf16 %1, v[100:103], v[96:99], %1\n" \
    "ds_read_b128 v[144:147], v59 offset:2048\n" \
    "v_mfma_f32_16x16x32_bf16 %2, v[104:107], v[92:95], %2\n" \
    "ds_read_b64_tr_b16 v[152:153], v63 offset:6144\n" \
    "v_mfma_f32_16x16x32_bf16 %3, v[104:107], v[96:99], %3\n" \
    "ds_read_b64_tr_b16 v[154:155], v63 offset:6912\n" \
    "v_mfma_f32_16x16x32_bf16 %4, v[108:111], v[92:95], %4\n" \
    "ds_read_b64_tr_b16 v[156:157], v62 offset:6208\n" \
    "v_mfma_f32_16x16x32_bf16 %5, v[108:111], v[96:99], %5\n" \
    "ds_read_b64_tr_b16 v[158:159], v62 offset:6976\n" \
    "s_cmp_lg_u32 %21, 0\n" \
    "s_cbranch_scc1 G1C60_%=\n" \
    "v_mfma_f32_16x16x32_bf16 %6, v[80:83], v[68:71], %6\n" \
    "v_mfma_f32_16x16x32_bf16 %6, v[100:103], v[68:71], %6\n" \
    "s_branch G1C6E_%=\n" \
    "G1C60_%=:\n" \
    "s_cmp_lg_u32 %21, 1\n" \
    "s_cbranch_scc1 G1C61_%=\n" \
    "v_mfma_f32_16x16x32_bf16 %6, v[84:87], v[68:71], %6\n" \
    "v_mfma_f32_16x16x32_bf16 %6, v[104:107], v[68:71], %6\n" \
    "s_branch G1C6E_%=\n" \
    "G1C61_%=:\n" \
    "s_cmp_lg_u32 %21, 2\n" \
    "s_cbranch_scc1 G1C62_%=\n" \
    "v_mfma_f32_16x16x32_bf16 %6, v[88:91], v[68:71], %6\n" \
    "v_mfma_f32_16x16x32_bf16 %6, v[108:111], v[68:71], %6\n" \
    "G1C62_%=:\n" \
    "G1C6E_%=:\n" \
    "s_waitcnt lgkmcnt(0)\n" \
    "s_cmp_lg_u32 %21, 0\n" \
    "s_cbranch_scc1 G1C70_%=\n" \
    "v_mfma_f32_16x16x32_bf16 %6, v[128:131], v[68:71], %6\n" \
    "v_mfma_f32_16x16x32_bf16 %6, v[148:151], v[68:71], %6\n" \
    "s_branch G1C7E_%=\n" \
    "G1C70_%=:\n" \
    "s_cmp_lg_u32 %21, 1\n" \
    "s_cbranch_scc1 G1C71_%=\n" \
    "v_mfma_f32_16x16x32_bf16 %6, v[132:135], v[68:71], %6\n" \
    "v_mfma_f32_16x16x32_bf16 %6, v[152:155], v[68:71], %6\n" \
    "s_branch G1C7E_%=\n" \
    "G1C71_%=:\n" \
    "s_cmp_lg_u32 %21, 2\n" \
    "s_cbranch_scc1 G1C72_%=\n" \
    "v_mfma_f32_16x16x32_bf16 %6, v[136:139], v[68:71], %6\n" \
    "v_mfma_f32_16x16x32_bf16 %6, v[156:159], v[68:71], %6\n" \
    "G1C72_%=:\n" \
    "G1C7E_%=:\n" \
    "s_nop 7\n" \
    "s_mov_b32 exec_lo, 0x10001\n" \
    "s_mov_b32 exec_hi, 0x10001\n" \
    "ds_write_b128 %20, %6\n" \
    "s_mov_b64 exec, -1\n" \
    "s_waitcnt lgkmcnt(0)\n" \
    "s_barrier\n" \
    "v_mfma_f32_16x16x32_bf16 %0, v[128:131], v[120:123], %0\n" \
    "ds_read_b128 v[72:75], v58 offset:30720\n" \
    "ds_read_b64_tr_b16 v[80:81], v66 offset:30720\n" \
    "ds_read_b64_tr_b16 v[82:83], v66 offset:32512\n" \
    "v_mfma_f32_16x16x32_bf16 %1, v[128:131], v[124:127], %1\n" \
    "ds_read_b128 v[76:79], v58 offset:32768\n" \
    "ds_read_b64_tr_b16 v[84:85], v66 offset:30752\n" \
    "v_mfma_f32_16x16x32_bf16 %2, v[132:135], v[120:123], %2\n" \
    "ds_read_b64_tr_b16 v[86:87], v66 offset:32544\n" \
    "ds_read_b64_tr_b16 v[88:89], v66 offset:30784\n" \
    "v_mfma_f32_16x16x32_bf16 %3, v[132:135], v[124:127], %3\n" \
    "ds_read_b64_tr_b16 v[90:91], v66 offset:32576\n" \
    "ds_read_b64_tr_b16 v[92:93], v66 offset:30816\n" \
    "v_mfma_f32_16x16x32_bf16 %4, v[136:139], v[120:123], %4\n" \
    "ds_read_b64_tr_b16 v[94:95], v66 offset:32608\n" \
    "ds_read_b128 v[96:99], v59 offset:30720\n" \
    "v_mfma_f32_16x16x32_bf16 %5, v[136:139], v[124:127], %5\n" \
    "ds_read_b64_tr_b16 v[104:105], v66 offset:37888\n" \
    "ds_read_b64_tr_b16 v[106:107], v66 offset:39680\n" \
    "v_mfma_f32_16x16x32_bf16 %0, v[148:151], v[140:143], %0\n" \
    "ds_read_b128 v[100:103], v59 offset:32768\n" \
    "ds_read_b64_tr_b16 v[108:109], v66 offset:37920\n" \
    "v_mfma_f32_16x16x32_bf16 %1, v[148:151], v[144:147], %1\n" \
    "ds_read_b64_tr_b16 v[110:111], v66 offset:39712\n" \
    "ds_read_b64_tr_b16 v[112:113], v66 offset:37952\n" \
    "v_mfma_f32_16x16x32_bf16 %2, v[152:155], v[140:143], %2\n" \
    "ds_read_b64_tr_b16 v[114:115], v66 offset:39744\n" \
    "ds_read_b64_tr_b16 v[116:117], v66 offset:37984\n" \
    "v_mfma_f32_16x16x32_bf16 %3, v[152:155], v[144:147], %3\n" \
    "ds_read_b64_tr_b16 v[118:119], v66 offset:39776\n" \
    "ds_read_b32 %10, %22\n" \
    "v_mfma_f32_16x16x32_bf16 %4, v[156:159], v[140:143], %4\n" \
    "ds_read_b32 %11, %22 offset:64\n" \
    "ds_read_b128 %12, %23 offset:0\n" \
    "v_mfma_f32_16x16x32_bf16 %5, v[156:159], v[144:147], %5\n" \
    "ds_read_b128 %13, %23 offset:64\n" \
    "ds_read_b128 %14, %23 offset:128\n" \
    "s_waitcnt lgkmcnt(0)\n" \
    "s_barrier\n" \
    "s_nop 4\n" \
    ""
#define G1_QKV1 \
    "v_add_u32 v58, 61440, %15\n" \
    "v_add_u32 v59, 61440, %16\n" \
    "v_add_u32 v62, 61440, %17\n" \
    "v_add_u32 v63, 61440, %18\n" \
    "v_add_u32 v66, 61440, %19\n" \
    "v_add_u32 v60, 122880, %15\n" \
    "v_add_u32 v61, 122880, %16\n" \
    "v_add_u32 v64, 122880, %17\n" \
    "v_add_u32 v65, 122880, %18\n" \
    "v_add_u32 v67, 122880, %19\n" \
    "v_mov_b32 v68, 0x3f803f80\n" \
    "v_mov_b32 v69, 0x3f803f80\n" \
    "v_mov_b32 v70, 0x3f803f80\n" \
    "v_mov_b32 v71, 0x3f803f80\n" \
    "s_barrier\n" \
    "ds_read_b128 v[72:75], %15 offset:0\n" \
    "ds_read_b64_tr_b16 v[80:81], %17 offset:0\n" \
    "ds_read_b64_tr_b16 v[82:83], %17 offset:768\n" \
    "ds_read_b128 v[76:79], %15 offset:2048\n" \
    "ds_read_b64_tr_b16 v[84:85], %18 offset:0\n" \
    "ds_read_b64_tr_b16 v[86:87], %18 offset:768\n" \
    "ds_read_b64_tr_b16 v[88:89], %17 offset:64\n" \
    "ds_read_b64_tr_b16 v[90:91], %17 offset:832\n" \
    "ds_read_b128 v[92:95], %16 offset:0\n" \
    "ds_read_b64_tr_b16 v[100:101], %17 offset:6144\n" \
    "ds_read_b64_tr_b16 v[102:103], %17 offset:6912\n" \
    "ds_read_b128 v[96:99], %16 offset:2048\n" \
    "ds_read_b64_tr_b16 v[104:105], %18 offset:6144\n" \
    "ds_read_b64_tr_b16 v[106:107], %18 offset:6912\n" \
    "ds_read_b64_tr_b16 v[108:109], %17 offset:6208\n" \
    "ds_read_b64_tr_b16 v[110:111], %17 offset:6976\n" \
    "s_waitcnt lgkmcnt(0)\n" \
    "s_barrier\n" \
    "ds_read_b128 v[120:123], %15 offset:30720\n" \
    "ds_read_b64_tr_b16 v[128:129], %17 offset:30720\n" \
    "v_mfma_f32_16x16x32_bf16 %0, v[80:83], v[72:75], 0\n" \
    "ds_read_b64_tr_b16 v[130:131], %17 offset:31488\n" \
    "ds_read_b128 v[124:127], %15 offset:32768\n" \
    "v_mfma_f32_16x16x32_bf16 %1, v[80:83], v[76:79], 0\n" \
    "ds_read_b64_tr_b16 v[132:133], %18 offset:30720\n" \
    "ds_read_b64_tr_b16 v[134:135], %18 offset:31488\n" \
    "v_mfma_f32_16x16x32_bf16 %2, v[84:87], v[72:75], 0\n" \
    "ds_read_b64_tr_b16 v[136:137], %17 offset:30784\n" \
    "ds_read_b64_tr_b16 v[138:139], %17 offset:31552\n" \
    "v_mfma_f32_16x16x32_bf16 %3, v[84:87], v[76:79], 0\n" \
    "ds_read_b128 v[140:143], %16 offset:30720\n" \
    "v_mfma_f32_16x16x32_bf16 %4, v[88:91], v[72:75], 0\n" \
    "ds_read_b64_tr_b16 v[148:149], %17 offset:36864\n" \
    "v_mfma_f32_16x16x32_bf16 %5, v[88:91], v[76:79], 0\n" \
    "ds_read_b64_tr_b16 v[150:151], %17 offset:37632\n" \
    "v_mfma_f32_16x16x32_bf16 %0, v[100:103], v[92:95], %0\n" \
    "ds_read_b128 v[144:147], %16 offset:32768\n" \
    "v_mfma_f32_16x16x32_bf16 %1, v[100:103], v[96:99], %1\n" \
    "ds_read_b64_tr_b16 v[152:153], %18 offset:36864\n" \
    "v_mfma_f32_16x16x32_bf16 %2, v[104:107], v[92:95], %2\n" \
    "ds_read_b64_tr_b16 v[154:155], %18 offset:37632\n" \
    "v_mfma_f32_16x16x32_bf16 %3, v[104:107], v[96:99], %3\n" \
    "ds_read_b64_tr_b16 v[156:157], %17 offset:36928\n" \
    "v_mfma_f32_16x16x32_bf16 %4, v[108:111], v[92:95], %4\n" \
    "ds_read_b64_tr_b16 v[158:159], %17 offset:37696\n" \
    "v_mfma_f32_16x16x32_bf16 %5, v[108:111], v[96:99], %5\n" \
    "s_cmp_lg_u32 %21, 0\n" \
    "s_cbranch_scc1 G1C00_%=\n" \
    "v_mfma_f32_16x16x32_bf16 %6, v[80:83], v[68:71], %6\n" \
    "v_mfma_f32_16x16x32_bf16 %6, v[100:103], v[68:71], %6\n" \
    "s_branch G1C0E_%=\n" \
    "G1C00_%=:\n" \
    "s_cmp_lg_u32 %21, 1\n" \
    "s_cbranch_scc1 G1C01_%=\n" \
    "v_mfma_f32_16x16x32_bf16 %6, v[84:87], v[68:71], %6\n" \
    "v_mfma_f32_16x16x32_bf16 %6, v[104:107], v[68:71], %6\n" \
    "s_branch G1C0E_%=\n" \
    "G1C01_%=:\n" \
    "s_cmp_lg_u32 %21, 2\n" \
    "s_cbranch_scc1 G1C02_%=\n" \
    "v_mfma_f32_16x16x32_bf16 %6, v[88:91], v[68:71], %6\n" \
    "v_mfma_f32_16x16x32_bf16 %6, v[108:111], v[68:71], %6\n" \
    "G1C02_%=:\n" \
    "G1C0E_%=:\n" \
    "s_waitcnt lgkmcnt(0)\n" \
    "s_barrier\n" \
    "ds_read_b128 v[72:75], v58 offset:0\n" \
    "ds_read_b64_tr_b16 v[80:81], v62 offset:0\n" \
    "v_mfma_f32_16x16x32_bf16 %0, v[128:131], v[120:123], %0\n" \
    "ds_read_b64_tr_b16 v[82:83], v62 offset:768\n" \
    "ds_read_b128 v[76:79], v58 offset:2048\n" \
    "v_mfma_f32_16x16x32_bf16 %1, v[128:131], v[124:127], %1\n" \
    "ds_read_b64_tr_b16 v[84:85], v63 offset:0\n" \
    "ds_read_b64_tr_b16 v[86:87], v63 offset:768\n" \
    "v_mfma_f32_16x16x32_bf16 %2, v[132:135], v[120:123], %2\n" \
    "ds_read_b64_tr_b16 v[88:89], v62 offset:64\n" \
    "ds_read_b64_tr_b16 v[90:91], v62 offset:832\n" \
    "v_mfma_f32_16x16x32_bf16 %3, v[132:135], v[124:127], %3\n" \
    "ds_read_b128 v[92:95], v59 offset:0\n" \
    "v_mfma_f32_16x16x32_bf16 %4, v[136:139], v[120:123], %4\n" \
    "ds_read_b64_tr_b16 v[100:101], v62 offset:6144\n" \
    "v_mfma_f32_16x16x32_bf16 %5, v[136:139], v[124:127], %5\n" \
    "ds_read_b64_tr_b16 v[102:103], v62 offset:6912\n" \
    "v_mfma_f32_16x16x32_bf16 %0, v[148:151], v[140:143], %0\n" \
    "ds_read_b128 v[96:99], v59 offset:2048\n" \
    "v_mfma_f32_16x16x32_bf16 %1, v[148:151], v[144:147], %1\n" \
    "ds_read_b64_tr_b16 v[104:105], v63 offset:6144\n" \
    "v_mfma_f32_16x16x32_bf16 %2, v[152:155], v[140:143], %2\n" \
    "ds_read_b64_tr_b16 v[106:107], v63 offset:6912\n" \
    "v_mfma_f32_16x16x32_bf16 %3, v[152:155], v[144:147], %3\n" \
    "ds_read_b64_tr_b16 v[108:109], v62 offset:6208\n" \
    "v_mfma_f32_16x16x32_bf16 %4, v[156:159], v[140:143], %4\n" \
    "ds_read_b64_tr_b16 v[110:111], v62 offset:6976\n" \
    "v_mfma_f32_16x16x32_bf16 %5, v[156:159], v[144:147], %5\n" \
    "s_cmp_lg_u32 %21, 0\n" \
    "s_cbranch_scc1 G1C10_%=\n" \
    "v_mfma_f32_16x16x32_bf16 %6, v[128:131], v[68:71], %6\n" \
    "v_mfma_f32_16x16x32_bf16 %6, v[148:151], v[68:71], %6\n" \
    "s_branch G1C1E_%=\n" \
    "G1C10_%=:\n" \
    "s_cmp_lg_u32 %21, 1\n" \
    "s_cbranch_scc1 G1C11_%=\n" \
    "v_mfma_f32_16x16x32_bf16 %6, v[132:135], v[68:71], %6\n" \
    "v_mfma_f32_16x16x32_bf16 %6, v[152:155], v[68:71], %6\n" \
    "s_branch G1C1E_%=\n" \
    "G1C11_%=:\n" \
    "s_cmp_lg_u32 %21, 2\n" \
    "s_cbranch_scc1 G1C12_%=\n" \
    "v_mfma_f32_16x16x32_bf16 %6, v[136:139], v[68:71], %6\n" \
    "v_mfma_f32_16x16x32_bf16 %6, v[156:159], v[68:71], %6\n" \
    "G1C12_%=:\n" \
    "G1C1E_%=:\n" \
    "s_waitcnt lgkmcnt(0)\n" \
    "s_barrier\n" \
    "ds_read_b128 v[120:123], v58 offset:30720\n" \
    "ds_read_b64_tr_b16 v[128:129], v62 offset:30720\n" \
    "v_mfma_f32_16x16x32_bf16 %0, v[80:83], v[72:75], %0\n" \
    "ds_read_b64_tr_b16 v[130:131], v62 offset:31488\n" \
    "ds_read_b128 v[124:127], v58 offset:32768\n" \
    "v_mfma_f32_16x16x32_bf16 %1, v[80:83], v[76:79], %1\n" \
    "ds_read_b64_tr_b16 v[132:133], v63 offset:30720\n" \
    "ds_read_b64_tr_b16 v[134:135], v63 offset:31488\n" \
    "v_mfma_f32_16x16x32_bf16 %2, v[84:87], v[72:75], %2\n" \
    "ds_read_b64_tr_b16 v[136:137], v62 offset:30784\n" \
    "ds_read_b64_tr_b16 v[138:139], v62 offset:31552\n" \
    "v_mfma_f32_16x16x32_bf16 %3, v[84:87], v[76:79], %3\n" \
    "ds_read_b128 v[140:143], v59 offset:30720\n" \
    "v_mfma_f32_16x16x32_bf16 %4, v[88:91], v[72:75], %4\n" \
    "ds_read_b64_tr_b16 v[148:149], v62 offset:36864\n" \
    "v_mfma_f32_16x16x32_bf16 %5, v[88:91], v[76:79], %5\n" \
    "ds_read_b64_tr_b16 v[150:151], v62 offset:37632\n" \
    "v_mfma_f32_16x16x32_bf16 %0, v[100:103], v[92:95], %0\n" \
    "ds_read_b128 v[144:147], v59 offset:32768\n" \
    "v_mfma_f32_16x16x32_bf16 %1, v[100:103], v[96:99], %1\n" \
    "ds_read_b64_tr_b16 v[152:153], v63 offset:36864\n" \
    "v_mfma_f32_16x16x32_bf16 %2, v[104:107], v[92:95], %2\n" \
    "ds_read_b64_tr_b16 v[154:155], v63 offset:37632\n" \
    "v_mfma_f32_16x16x32_bf16 %3, v[104:107], v[96:99], %3\n" \
    "ds_read_b64_tr_b16 v[156:157], v62 offset:36928\n" \
    "v_mfma_f32_16x16x32_bf16 %4, v[108:111], v[92:95], %4\n" \
    "ds_read_b64_tr_b16 v[158:159], v62 offset:37696\n" \
    "v_mfma_f32_16x16x32_bf16 %5, v[108:111], v[96:99], %5\n" \
    "s_cmp_lg_u32 %21, 0\n" \
    "s_cbranch_scc1 G1C20_%=\n" \
    "v_mfma_f32_16x16x32_bf16 %6, v[80:83], v[68:71], %6\n" \
    "v_mfma_f32_16x16x32_bf16 %6, v[100:103], v[68:71], %6\n" \
    "s_branch G1C2E_%=\n" \
    "G1C20_%=:\n" \
    "s_cmp_lg_u32 %21, 1\n" \
    "s_cbranch_scc1 G1C21_%=\n" \
    "v_mfma_f32_16x16x32_bf16 %6, v[84:87], v[68:71], %6\n" \
    "v_mfma_f32_16x16x32_bf16 %6, v[104:107], v[68:71], %6\n" \
    "s_branch G1C2E_%=\n" \
    "G1C21_%=:\n" \
    "s_cmp_lg_u32 %21, 2\n" \
    "s_cbranch_scc1 G1C22_%=\n" \
    "v_mfma_f32_16x16x32_bf16 %6, v[88:91], v[68:71], %6\n" \
    "v_mfma_f32_16x16x32_bf16 %6, v[108:111], v[68:71], %6\n" \
    "G1C22_%=:\n" \
    "G1C2E_%=:\n" \
    "s_waitcnt lgkmcnt(0)\n" \
    "s_barrier\n" \
    "ds_read_b128 v[72:75], v60 offset:0\n" \
    "ds_read_b64_tr_b16 v[80:81], v64 offset:0\n" \
    "v_mfma_f32_16x16x32_bf16 %0, v[128:131], v[120:123], %0\n" \
    "ds_read_b64_tr_b16 v[82:83], v64 offset:768\n" \
    "ds_read_b128 v[76:79], v60 offset:2048\n" \
    "v_mfma_f32_16x16x32_bf16 %1, v[128:131], v[124:127], %1\n" \
    "ds_read_b64_tr_b16 v[84:85], v65 offset:0\n" \
    "ds_read_b64_tr_b16 v[86:87], v65 offset:768\n" \
    "v_mfma_f32_16x16x32_bf16 %2, v[132:135], v[120:123], %2\n" \
    "ds_read_b64_tr_b16 v[88:89], v64 offset:64\n" \
    "ds_read_b64_tr_b16 v[90:91], v64 offset:832\n" \
    "v_mfma_f32_16x16x32_bf16 %3, v[132:135], v[124:127], %3\n" \
    "ds_read_b128 v[92:95], v61 offset:0\n" \
    "v_mfma_f32_16x16x32_bf16 %4, v[136:139], v[120:123], %4\n" \
    "ds_read_b64_tr_b16 v[100:101], v64 offset:6144\n" \
    "v_mfma_f32_16x16x32_bf16 %5, v[136:139], v[124:127], %5\n" \
    "ds_read_b64_tr_b16 v[102:103], v64 offset:6912\n" \
    "v_mfma_f32_16x16x32_bf16 %0, v[148:151], v[140:143], %0\n" \
    "ds_read_b128 v[96:99], v61 offset:2048\n" \
    "v_mfma_f32_16x16x32_bf16 %1, v[148:151], v[144:147], %1\n" \
    "ds_read_b64_tr_b16 v[104:105], v65 offset:6144\n" \
    "v_mfma_f32_16x16x32_bf16 %2, v[152:155], v[140:143], %2\n" \
    "ds_read_b64_tr_b16 v[106:107], v65 offset:6912\n" \
    "v_mfma_f32_16x16x32_bf16 %3, v[152:155], v[144:147], %3\n" \
    "ds_read_b64_tr_b16 v[108:109], v64 offset:6208\n" \
    "v_mfma_f32_16x16x32_bf16 %4, v[156:159], v[140:143], %4\n" \
    "ds_read_b64_tr_b16 v[110:111], v64 offset:6976\n" \
    "v_mfma_f32_16x16x32_bf16 %5, v[156:159], v[144:147], %5\n" \
    "s_cmp_lg_u32 %21, 0\n" \
    "s_cbranch_scc1 G1C30_%=\n" \
    "v_mfma_f32_16x16x32_bf16 %6, v[128:131], v[68:71], %6\n" \
    "v_mfma_f32_16x16x32_bf16 %6, v[148:151], v[68:71], %6\n" \
    "s_branch G1C3E_%=\n" \
    "G1C30_%=:\n" \
    "s_cmp_lg_u32 %21, 1\n" \
    "s_cbranch_scc1 G1C31_%=\n" \
    "v_mfma_f32_16x16x32_bf16 %6, v[132:135], v[68:71], %6\n" \
    "v_mfma_f32_16x16x32_bf16 %6, v[152:155], v[68:71], %6\n" \
    "s_branch G1C3E_%=\n" \
    "G1C31_%=:\n" \
    "s_cmp_lg_u32 %21, 2\n" \
    "s_cbranch_scc1 G1C32_%=\n" \
    "v_mfma_f32_16x16x32_bf16 %6, v[136:139], v[68:71], %6\n" \
    "v_mfma_f32_16x16x32_bf16 %6, v[156:159], v[68:71], %6\n" \
    "G1C32_%=:\n" \
    "G1C3E_%=:\n" \
    "s_waitcnt lgkmcnt(0)\n" \
    "s_barrier\n" \
    "ds_read_b128 v[120:123], %15 offset:0\n" \
    "ds_read_b64_tr_b16 v[128:129], %17 offset:0\n" \
    "v_mfma_f32_16x16x32_bf16 %0, v[80:83], v[72:75], %0\n" \
    "ds_read_b64_tr_b16 v[130:131], %17 offset:768\n" \
    "ds_read_b128 v[124:127], %15 offset:2048\n" \
    "v_mfma_f32_16x16x32_bf16 %1, v[80:83], v[76:79], %1\n" \
    "ds_read_b64_tr_b16 v[132:133], %18 offset:0\n" \
    "ds_read_b64_tr_b16 v[134:135], %18 offset:768\n" \
    "v_mfma_f32_16x16x32_bf16 %2, v[84:87], v[72:75], %2\n" \
    "ds_read_b64_tr_b16 v[136:137], %17 offset:64\n" \
    "ds_read_b64_tr_b16 v[138:139], %17 offset:832\n" \
    "v_mfma_f32_16x16x32_bf16 %3, v[84:87], v[76:79], %3\n" \
    "ds_read_b128 v[140:143], %16 offset:0\n" \
    "v_mfma_f32_16x16x32_bf16 %4, v[88:91], v[72:75], %4\n" \
    "ds_read_b64_tr_b16 v[148:149], %17 offset:6144\n" \
    "v_mfma_f32_16x16x32_bf16 %5, v[88:91], v[76:79], %5\n" \
    "ds_read_b64_tr_b16 v[150:151], %17 offset:6912\n" \
    "v_mfma_f32_16x16x32_bf16 %0, v[100:103], v[92:95], %0\n" \
    "ds_read_b128 v[144:147], %16 offset:2048\n" \
    "v_mfma_f32_16x16x32_bf16 %1, v[100:103], v[96:99], %1\n" \
    "ds_read_b64_tr_b16 v[152:153], %18 offset:6144\n" \
    "v_mfma_f32_16x16x32_bf16 %2, v[104:107], v[92:95], %2\n" \
    "ds_read_b64_tr_b16 v[154:155], %18 offset:6912\n" \
    "v_mfma_f32_16x16x32_bf16 %3, v[104:107], v[96:99], %3\n" \
    "ds_read_b64_tr_b16 v[156:157], %17 offset:6208\n" \
    "v_mfma_f32_16x16x32_bf16 %4, v[108:111], v[92:95], %4\n" \
    "ds_read_b64_tr_b16 v[158:159], %17 offset:6976\n" \
    "v_mfma_f32_16x16x32_bf16 %5, v[108:111], v[96:99], %5\n" \
    "s_cmp_lg_u32 %21, 0\n" \
    "s_cbranch_scc1 G1C40_%=\n" \
    "v_mfma_f32_16x16x32_bf16 %6, v[80:83], v[68:71], %6\n" \
    "v_mfma_f32_16x16x32_bf16 %6, v[100:103], v[68:71], %6\n" \
    "s_branch G1C4E_%=\n" \
    "G1C40_%=:\n" \
    "s_cmp_lg_u32 %21, 1\n" \
    "s_cbranch_scc1 G1C41_%=\n" \
    "v_mfma_f32_16x16x32_bf16 %6, v[84:87], v[68:71], %6\n" \
    "v_mfma_f32_16x16x32_bf16 %6, v[104:107], v[68:71], %6\n" \
    "s_branch G1C4E_%=\n" \
    "G1C41_%=:\n" \
    "s_cmp_lg_u32 %21, 2\n" \
    "s_cbranch_scc1 G1C42_%=\n" \
    "v_mfma_f32_16x16x32_bf16 %6, v[88:91], v[68:71], %6\n" \
    "v_mfma_f32_16x16x32_bf16 %6, v[108:111], v[68:71], %6\n" \
    "G1C42_%=:\n" \
    "G1C4E_%=:\n" \
    "s_waitcnt lgkmcnt(0)\n" \
    "s_barrier\n" \
    "ds_read_b128 v[72:75], %15 offset:30720\n" \
    "ds_read_b64_tr_b16 v[80:81], %17 offset:30720\n" \
    "v_mfma_f32_16x16x32_bf16 %0, v[128:131], v[120:123], %0\n" \
    "ds_read_b64_tr_b16 v[82:83], %17 offset:31488\n" \
    "ds_read_b128 v[76:79], %15 offset:32768\n" \
    "v_mfma_f32_16x16x32_bf16 %1, v[128:131], v[124:127], %1\n" \
    "ds_read_b64_tr_b16 v[84:85], %18 offset:30720\n" \
    "ds_read_b64_tr_b16 v[86:87], %18 offset:31488\n" \
    "v_mfma_f32_16x16x32_bf16 %2, v[132:135], v[120:123], %2\n" \
    "ds_read_b64_tr_b16 v[88:89], %17 offset:30784\n" \
    "ds_read_b64_tr_b16 v[90:91], %17 offset:31552\n" \
    "v_mfma_f32_16x16x32_bf16 %3, v[132:135], v[124:127], %3\n" \
    "ds_read_b128 v[92:95], %16 offset:30720\n" \
    "v_mfma_f32_16x16x32_bf16 %4, v[136:139], v[120:123], %4\n" \
    "ds_read_b64_tr_b16 v[100:101], %17 offset:36864\n" \
    "v_mfma_f32_16x16x32_bf16 %5, v[136:139], v[124:127], %5\n" \
    "ds_read_b64_tr_b16 v[102:103], %17 offset:37632\n" \
    "v_mfma_f32_16x16x32_bf16 %0, v[148:151], v[140:143], %0\n" \
    "ds_read_b128 v[96:99], %16 offset:32768\n" \
    "v_mfma_f32_16x16x32_bf16 %1, v[148:151], v[144:147], %1\n" \
    "ds_read_b64_tr_b16 v[104:105], %18 offset:36864\n" \
    "v_mfma_f32_16x16x32_bf16 %2, v[152:155], v[140:143], %2\n" \
    "ds_read_b64_tr_b16 v[106:107], %18 offset:37632\n" \
    "v_mfma_f32_16x16x32_bf16 %3, v[152:155], v[144:147], %3\n" \
    "ds_read_b64_tr_b16 v[108:109], %17 offset:36928\n" \
    "v_mfma_f32_16x16x32_bf16 %4, v[156:159], v[140:143], %4\n" \
    "ds_read_b64_tr_b16 v[110:111], %17 offset:37696\n" \
    "v_mfma_f32_16x16x32_bf16 %5, v[156:159], v[144:147], %5\n" \
    "s_cmp_lg_u32 %21, 0\n" \
    "s_cbranch_scc1 G1C50_%=\n" \
    "v_mfma_f32_16x16x32_bf16 %6, v[128:131], v[68:71], %6\n" \
    "v_mfma_f32_16x16x32_bf16 %6, v[148:151], v[68:71], %6\n" \
    "s_branch G1C5E_%=\n" \
    "G1C50_%=:\n" \
    "s_cmp_lg_u32 %21, 1\n" \
    "s_cbranch_scc1 G1C51_%=\n" \
    "v_mfma_f32_16x16x32_bf16 %6, v[132:135], v[68:71], %6\n" \
    "v_mfma_f32_16x16x32_bf16 %6, v[152:155], v[68:71], %6\n" \
    "s_branch G1C5E_%=\n" \
    "G1C51_%=:\n" \
    "s_cmp_lg_u32 %21, 2\n" \
    "s_cbranch_scc1 G1C52_%=\n" \
    "v_mfma_f32_16x16x32_bf16 %6, v[136:139], v[68:71], %6\n" \
    "v_mfma_f32_16x16x32_bf16 %6, v[156:159], v[68:71], %6\n" \
    "G1C52_%=:\n" \
    "G1C5E_%=:\n" \
    "s_waitcnt lgkmcnt(0)\n" \
    "s_barrier\n" \
    "ds_read_b128 v[120:123], v58 offset:0\n" \
    "ds_read_b64_tr_b16 v[128:129], v62 offset:0\n" \
    "v_mfma_f32_16x16x32_bf16 %0, v[80:83], v[72:75], %0\n" \
    "ds_read_b64_tr_b16 v[130:131], v62 offset:768\n" \
    "ds_read_b128 v[124:127], v58 offset:2048\n" \
    "v_mfma_f32_16x16x32_bf16 %1, v[80:83], v[76:79], %1\n" \
    "ds_read_b64_tr_b16 v[132:133], v63 offset:0\n" \
    "ds_read_b64_tr_b16 v[134:135], v63 offset:768\n" \
    "v_mfma_f32_16x16x32_bf16 %2, v[84:87], v[72:75], %2\n" \
    "ds_read_b64_tr_b16 v[136:137], v62 offset:64\n" \
    "ds_read_b64_tr_b16 v[138:139], v62 offset:832\n" \
    "v_mfma_f32_16x16x32_bf16 %3, v[84:87], v[76:79], %3\n" \
    "ds_read_b128 v[140:143], v59 offset:0\n" \
    "v_mfma_f32_16x16x32_bf16 %4, v[88:91], v[72:75], %4\n" \
    "ds_read_b64_tr_b16 v[148:149], v62 offset:6144\n" \
    "v_mfma_f32_16x16x32_bf16 %5, v[88:91], v[76:79], %5\n" \
    "ds_read_b64_tr_b16 v[150:151], v62 offset:6912\n" \
    "v_mfma_f32_16x16x32_bf16 %0, v[100:103], v[92:95], %0\n" \
    "ds_read_b128 v[144:147], v59 offset:2048\n" \
    "v_mfma_f32_16x16x32_bf16 %1, v[100:103], v[96:99], %1\n" \
    "ds_read_b64_tr_b16 v[152:153], v63 offset:6144\n" \
    "v_mfma_f32_16x16x32_bf16 %2, v[104:107], v[92:95], %2\n" \
    "ds_read_b64_tr_b16 v[154:155], v63 offset:6912\n" \
    "v_mfma_f32_16x16x32_bf16 %3, v[104:107], v[96:99], %3\n" \
    "ds_read_b64_tr_b16 v[156:157], v62 offset:6208\n" \
    "v_mfma_f32_16x16x32_bf16 %4, v[108:111], v[92:95], %4\n" \
    "ds_read_b64_tr_b16 v[158:159], v62 offset:6976\n" \
    "v_mfma_f32_16x16x32_bf16 %5, v[108:111], v[96:99], %5\n" \
    "s_cmp_lg_u32 %21, 0\n" \
    "s_cbranch_scc1 G1C60_%=\n" \
    "v_mfma_f32_16x16x32_bf16 %6, v[80:83], v[68:71], %6\n" \
    "v_mfma_f32_16x16x32_bf16 %6, v[100:103], v[68:71], %6\n" \
    "s_branch G1C6E_%=\n" \
    "G1C60_%=:\n" \
    "s_cmp_lg_u32 %21, 1\n" \
    "s_cbranch_scc1 G1C61_%=\n" \
    "v_mfma_f32_16x16x32_bf16 %6, v[84:87], v[68:71], %6\n" \
    "v_mfma_f32_16x16x32_bf16 %6, v[104:107], v[68:71], %6\n" \
    "s_branch G1C6E_%=\n" \
    "G1C61_%=:\n" \
    "s_cmp_lg_u32 %21, 2\n" \
    "s_cbranch_scc1 G1C62_%=\n" \
    "v_mfma_f32_16x16x32_bf16 %6, v[88:91], v[68:71], %6\n" \
    "v_mfma_f32_16x16x32_bf16 %6, v[108:111], v[68:71], %6\n" \
    "G1C62_%=:\n" \
    "G1C6E_%=:\n" \
    "s_waitcnt lgkmcnt(0)\n" \
    "s_cmp_lg_u32 %21, 0\n" \
    "s_cbranch_scc1 G1C70_%=\n" \
    "v_mfma_f32_16x16x32_bf16 %6, v[128:131], v[68:71], %6\n" \
    "v_mfma_f32_16x16x32_bf16 %6, v[148:151], v[68:71], %6\n" \
    "s_branch G1C7E_%=\n" \
    "G1C70_%=:\n" \
    "s_cmp_lg_u32 %21, 1\n" \
    "s_cbranch_scc1 G1C71_%=\n" \
    "v_mfma_f32_16x16x32_bf16 %6, v[132:135], v[68:71], %6\n" \
    "v_mfma_f32_16x16x32_bf16 %6, v[152:155], v[68:71], %6\n" \
    "s_branch G1C7E_%=\n" \
    "G1C71_%=:\n" \
    "s_cmp_lg_u32 %21, 2\n" \
    "s_cbranch_scc1 G1C72_%=\n" \
    "v_mfma_f32_16x16x32_bf16 %6, v[136:139], v[68:71], %6\n" \
    "v_mfma_f32_16x16x32_bf16 %6, v[156:159], v[68:71], %6\n" \
    "G1C72_%=:\n" \
    "G1C7E_%=:\n" \
    "s_nop 7\n" \
    "s_mov_b32 exec_lo, 0x10001\n" \
    "s_mov_b32 exec_hi, 0x10001\n" \
    "ds_write_b128 %20, %6\n" \
    "s_mov_b64 exec, -1\n" \
    "s_waitcnt lgkmcnt(0)\n" \
    "s_barrier\n" \
    "ds_read_b128 v[72:75], v58 offset:30720\n" \
    "ds_read_b64_tr_b16 v[80:81], v66 offset:30720\n" \
    "v_mfma_f32_16x16x32_bf16 %0, v[128:131], v[120:123], %0\n" \
    "ds_read_b64_tr_b16 v[82:83], v66 offset:32512\n" \
    "ds_read_b128 v[76:79], v58 offset:32768\n" \
    "v_mfma_f32_16x16x32_bf16 %1, v[128:131], v[124:127], %1\n" \
    "ds_read_b64_tr_b16 v[84:85], v66 offset:30752\n" \
    "ds_read_b64_tr_b16 v[86:87], v66 offset:32544\n" \
    "v_mfma_f32_16x16x32_bf16 %2, v[132:135], v[120:123], %2\n" \
    "ds_read_b64_tr_b16 v[88:89], v66 offset:30784\n" \
    "ds_read_b64_tr_b16 v[90:91], v66 offset:32576\n" \
    "v_mfma_f32_16x16x32_bf16 %3, v[132:135], v[124:127], %3\n" \
    "ds_read_b128 v[92:95], v59 offset:30720\n" \
    "ds_read_b64_tr_b16 v[100:101], v66 offset:37888\n" \
    "v_mfma_f32_16x16x32_bf16 %4, v[136:139], v[120:123], %4\n" \
    "ds_read_b64_tr_b16 v[102:103], v66 offset:39680\n" \
    "ds_read_b128 v[96:99], v59 offset:32768\n" \
    "v_mfma_f32_16x16x32_bf16 %5, v[136:139], v[124:127], %5\n" \
    "ds_read_b64_tr_b16 v[104:105], v66 offset:37920\n" \
    "ds_read_b64_tr_b16 v[106:107], v66 offset:39712\n" \
    "v_mfma_f32_16x16x32_bf16 %0, v[148:151], v[140:143], %0\n" \
    "ds_read_b64_tr_b16 v[108:109], v66 offset:37952\n" \
    "ds_read_b64_tr_b16 v[110:111], v66 offset:39744\n" \
    "v_mfma_f32_16x16x32_bf16 %1, v[148:151], v[144:147], %1\n" \
    "ds_read_b32 %10, %22\n" \
    "ds_read_b32 %11, %22 offset:64\n" \
    "v_mfma_f32_16x16x32_bf16 %2, v[152:155], v[140:143], %2\n" \
    "ds_read_b128 %12, %23 offset:192\n" \
    "v_mfma_f32_16x16x32_bf16 %3, v[152:155], v[144:147], %3\n" \
    "ds_read_b128 %13, %23 offset:256\n" \
    "v_mfma_f32_16x16x32_bf16 %4, v[156:159], v[140:143], %4\n" \
    "ds_read_b128 %14, %23 offset:320\n" \
    "v_mfma_f32_16x16x32_bf16 %5, v[156:159], v[144:147], %5\n" \
    "s_waitcnt lgkmcnt(0)\n" \
    "s_barrier\n" \
    "s_nop 4\n" \
    ""
#define G1_FFN0 \
    "v_add_u32 v58, 61440, %12\n" \
    "v_add_u32 v59, 61440, %13\n" \
    "v_add_u32 v66, 61440, %14\n" \
    "v_add_u32 v60, 122880, %12\n" \
    "v_add_u32 v61, 122880, %13\n" \
    "v_add_u32 v67, 122880, %14\n" \
    "v_mov_b32 v68, 0x3f803f80\n" \
    "v_mov_b32 v69, 0x3f803f80\n" \
    "v_mov_b32 v70, 0x3f803f80\n" \
    "v_mov_b32 v71, 0x3f803f80\n" \
    "v_mfma_f32_16x16x32_bf16 %0, v[80:83], v[72:75], 0\n" \
    "ds_read_b128 v[120:123], v60 offset:0\n" \
    "ds_read_b64_tr_b16 v[128:129], v67 offset:0\n" \
    "v_mfma_f32_16x16x32_bf16 %1, v[80:83], v[76:79], 0\n" \
    "ds_read_b64_tr_b16 v[130:131], v67 offset:1792\n" \
    "ds_read_b128 v[124:127], v60 offset:2048\n" \
    "v_mfma_f32_16x16x32_bf16 %2, v[84:87], v[72:75], 0\n" \
    "ds_read_b64_tr_b16 v[132:133], v67 offset:32\n" \
    "ds_read_b64_tr_b16 v[134:135], v67 offset:1824\n" \
    "v_mfma_f32_16x16x32_bf16 %3, v[84:87], v[76:79], 0\n" \
    "ds_read_b64_tr_b16 v[136:137], v67 offset:64\n" \
    "ds_read_b64_tr_b16 v[138:139], v67 offset:1856\n" \
    "v_mfma_f32_16x16x32_bf16 %4, v[88:91], v[72:75], 0\n" \
    "ds_read_b64_tr_b16 v[140:141], v67 offset:96\n" \
    "v_mfma_f32_16x16x32_bf16 %5, v[88:91], v[76:79], 0\n" \
    "ds_read_b64_tr_b16 v[142:143], v67 offset:1888\n" \
    "v_mfma_f32_16x16x32_bf16 %6, v[92:95], v[72:75], 0\n" \
    "ds_read_b128 v[144:147], v61 offset:0\n" \
    "v_mfma_f32_16x16x32_bf16 %7, v[92:95], v[76:79], 0\n" \
    "ds_read_b64_tr_b16 v[152:153], v67 offset:7168\n" \
    "v_mfma_f32_16x16x32_bf16 %0, v[104:107], v[96:99], %0\n" \
    "ds_read_b64_tr_b16 v[154:155], v67 offset:8960\n" \
    "v_mfma_f32_16x16x32_bf16 %1, v[104:107], v[100:103], %1\n" \
    "ds_read_b128 v[148:151], v61 offset:2048\n" \
    "v_mfma_f32_16x16x32_bf16 %2, v[108:111], v[96:99], %2\n" \
    "ds_read_b64_tr_b16 v[156:157], v67 offset:7200\n" \
    "v_mfma_f32_16x16x32_bf16 %3, v[108:111], v[100:103], %3\n" \
    "ds_read_b64_tr_b16 v[158:159], v67 offset:8992\n" \
    "v_mfma_f32_16x16x32_bf16 %4, v[112:115], v[96:99], %4\n" \
    "ds_read_b64_tr_b16 v[160:161], v67 offset:7232\n" \
    "v_mfma_f32_16x16x32_bf16 %5, v[112:115], v[100:103], %5\n" \
    "ds_read_b64_tr_b16 v[162:163], v67 offset:9024\n" \
    "v_mfma_f32_16x16x32_bf16 %6, v[116:119], v[96:99], %6\n" \
    "ds_read_b64_tr_b16 v[164:165], v67 offset:7264\n" \
    "v_mfma_f32_16x16x32_bf16 %7, v[116:119], v[100:103], %7\n" \
    "ds_read_b64_tr_b16 v[166:167], v67 offset:9056\n" \
    "s_cmp_lg_u32 %16, 0\n" \
    "s_cbranch_scc1 G1C00_%=\n" \
    "v_mfma_f32_16x16x32_bf16 %8, v[80:83], v[68:71], %8\n" \
    "v_mfma_f32_16x16x32_bf16 %8, v[104:107], v[68:71], %8\n" \
    "s_branch G1C0E_%=\n" \
    "G1C00_%=:\n" \
    "s_cmp_lg_u32 %16, 1\n" \
    "s_cbranch_scc1 G1C01_%=\n" \
    "v_mfma_f32_16x16x32_bf16 %8, v[84:87], v[68:71], %8\n" \
    "v_mfma_f32_16x16x32_bf16 %8, v[108:111], v[68:71], %8\n" \
    "s_branch G1C0E_%=\n" \
    "G1C01_%=:\n" \
    "s_cmp_lg_u32 %16, 2\n" \
    "s_cbranch_scc1 G1C02_%=\n" \
    "v_mfma_f32_16x16x32_bf16 %8, v[88:91], v[68:71], %8\n" \
    "v_mfma_f32_16x16x32_bf16 %8, v[112:115], v[68:71], %8\n" \
    "s_branch G1C0E_%=\n" \
    "G1C02_%=:\n" \
    "s_cmp_lg_u32 %16, 3\n" \
    "s_cbranch_scc1 G1C03_%=\n" \
    "v_mfma_f32_16x16x32_bf16 %8, v[92:95], v[68:71], %8\n" \
    "v_mfma_f32_16x16x32_bf16 %8, v[116:119], v[68:71], %8\n" \
    "G1C03_%=:\n" \
    "G1C0E_%=:\n" \
    "s_waitcnt lgkmcnt(0)\n" \
    "s_barrier\n" \
    "v_mfma_f32_16x16x32_bf16 %0, v[128:131], v[120:123], %0\n" \
    "ds_read_b128 v[72:75], %12 offset:0\n" \
    "ds_read_b64_tr_b16 v[80:81], %14 offset:0\n" \
    "v_mfma_f32_16x16x32_bf16 %1, v[128:131], v[124:127], %1\n" \
    "ds_read_b64_tr_b16 v[82:83], %14 offset:1792\n" \
    "ds_read_b128 v[76:79], %12 offset:2048\n" \
    "v_mfma_f32_16x16x32_bf16 %2, v[132:135], v[120:123], %2\n" \
    "ds_read_b64_tr_b16 v[84:85], %14 offset:32\n" \
    "ds_read_b64_tr_b16 v[86:87], %14 offset:1824\n" \
    "v_mfma_f32_16x16x32_bf16 %3, v[132:135], v[124:127], %3\n" \
    "ds_read_b64_tr_b16 v[88:89], %14 offset:64\n" \
    "ds_read_b64_tr_b16 v[90:91], %14 offset:1856\n" \
    "v_mfma_f32_16x16x32_bf16 %4, v[136:139], v[120:123], %4\n" \
    "ds_read_b64_tr_b16 v[92:93], %14 offset:96\n" \
    "v_mfma_f32_16x16x32_bf16 %5, v[136:139], v[124:127], %5\n" \
    "ds_read_b64_tr_b16 v[94:95], %14 offset:1888\n" \
    "v_mfma_f32_16x16x32_bf16 %6, v[140:143], v[120:123], %6\n" \
    "ds_read_b128 v[96:99], %13 offset:0\n" \
    "v_mfma_f32_16x16x32_bf16 %7, v[140:143], v[124:127], %7\n" \
    "ds_read_b64_tr_b16 v[104:105], %14 offset:7168\n" \
    "v_mfma_f32_16x16x32_bf16 %0, v[152:155], v[144:147], %0\n" \
    "ds_read_b64_tr_b16 v[106:107], %14 offset:8960\n" \
    "v_mfma_f32_16x16x32_bf16 %1, v[152:155], v[148:151], %1\n" \
    "ds_read_b128 v[100:103], %13 offset:2048\n" \
    "v_mfma_f32_16x16x32_bf16 %2, v[156:159], v[144:147], %2\n" \
    "ds_read_b64_tr_b16 v[108:109], %14 offset:7200\n" \
    "v_mfma_f32_16x16x32_bf16 %3, v[156:159], v[148:151], %3\n" \
    "ds_read_b64_tr_b16 v[110:111], %14 offset:8992\n" \
    "v_mfma_f32_16x16x32_bf16 %4, v[160:163], v[144:147], %4\n" \
    "ds_read_b64_tr_b16 v[112:113], %14 offset:7232\n" \
    "v_mfma_f32_16x16x32_bf16 %5, v[160:163], v[148:151], %5\n" \
    "ds_read_b64_tr_b16 v[114:115], %14 offset:9024\n" \
    "v_mfma_f32_16x16x32_bf16 %6, v[164:167], v[144:147], %6\n" \
    "ds_read_b64_tr_b16 v[116:117], %14 offset:7264\n" \
    "v_mfma_f32_16x16x32_bf16 %7, v[164:167], v[148:151], %7\n" \
    "ds_read_b64_tr_b16 v[118:119], %14 offset:9056\n" \
    "s_cmp_lg_u32 %16, 0\n" \
    "s_cbranch_scc1 G1C10_%=\n" \
    "v_mfma_f32_16x16x32_bf16 %8, v[128:131], v[68:71], %8\n" \
    "v_mfma_f32_16x16x32_bf16 %8, v[152:155], v[68:71], %8\n" \
    "s_branch G1C1E_%=\n" \
    "G1C10_%=:\n" \
    "s_cmp_lg_u32 %16, 1\n" \
    "s_cbranch_scc1 G1C11_%=\n" \
    "v_mfma_f32_16x16x32_bf16 %8, v[132:135], v[68:71], %8\n" \
    "v_mfma_f32_16x16x32_bf16 %8, v[156:159], v[68:71], %8\n" \
    "s_branch G1C1E_%=\n" \
    "G1C11_%=:\n" \
    "s_cmp_lg_u32 %16, 2\n" \
    "s_cbranch_scc1 G1C12_%=\n" \
    "v_mfma_f32_16x16x32_bf16 %8, v[136:139], v[68:71], %8\n" \
    "v_mfma_f32_16x16x32_bf16 %8, v[160:163], v[68:71], %8\n" \
    "s_branch G1C1E_%=\n" \
    "G1C12_%=:\n" \
    "s_cmp_lg_u32 %16, 3\n" \
    "s_cbranch_scc1 G1C13_%=\n" \
    "v_mfma_f32_16x16x32_bf16 %8, v[140:143], v[68:71], %8\n" \
    "v_mfma_f32_16x16x32_bf16 %8, v[164:167], v[68:71], %8\n" \
    "G1C13_%=:\n" \
    "G1C1E_%=:\n" \
    "s_waitcnt lgkmcnt(0)\n" \
    "s_barrier\n" \
    "v_mfma_f32_16x16x32_bf16 %0, v[80:83], v[72:75], %0\n" \
    "ds_read_b128 v[120:123], %12 offset:30720\n" \
    "ds_read_b64_tr_b16 v[128:129], %14 offset:30720\n" \
    "v_mfma_f32_16x16x32_bf16 %1, v[80:83], v[76:79], %1\n" \
    "ds_read_b64_tr_b16 v[130:131], %14 offset:32512\n" \
    "ds_read_b128 v[124:127], %12 offset:32768\n" \
    "v_mfma_f32_16x16x32_bf16 %2, v[84:87], v[72:75], %2\n" \
    "ds_read_b64_tr_b16 v[132:133], %14 offset:30752\n" \
    "ds_read_b64_tr_b16 v[134:135], %14 offset:32544\n" \
    "v_mfma_f32_16x16x32_bf16 %3, v[84:87], v[76:79], %3\n" \
    "ds_read_b64_tr_b16 v[136:137], %14 offset:30784\n" \
    "ds_read_b64_tr_b16 v[138:139], %14 offset:32576\n" \
    "v_mfma_f32_16x16x32_bf16 %4, v[88:91], v[72:75], %4\n" \
    "ds_read_b64_tr_b16 v[140:141], %14 offset:30816\n" \
    "v_mfma_f32_16x16x32_bf16 %5, v[88:91], v[76:79], %5\n" \
    "ds_read_b64_tr_b16 v[142:143], %14 offset:32608\n" \
    "v_mfma_f32_16x16x32_bf16 %6, v[92:95], v[72:75], %6\n" \
    "ds_read_b128 v[144:147], %13 offset:30720\n" \
    "v_mfma_f32_16x16x32_bf16 %7, v[92:95], v[76:79], %7\n" \
    "ds_read_b64_tr_b16 v[152:153], %14 offset:37888\n" \
    "v_mfma_f32_16x16x32_bf16 %0, v[104:107], v[96:99], %0\n" \
    "ds_read_b64_tr_b16 v[154:155], %14 offset:39680\n" \
    "v_mfma_f32_16x16x32_bf16 %1, v[104:107], v[100:103], %1\n" \
    "ds_read_b128 v[148:151], %13 offset:32768\n" \
    "v_mfma_f32_16x16x32_bf16 %2, v[108:111], v[96:99], %2\n" \
    "ds_read_b64_tr_b16 v[156:157], %14 offset:37920\n" \
    "v_mfma_f32_16x16x32_bf16 %3, v[108:111], v[100:103], %3\n" \
    "ds_read_b64_tr_b16 v[158:159], %14 offset:39712\n" \
    "v_mfma_f32_16x16x32_bf16 %4, v[112:115], v[96:99], %4\n" \
    "ds_read_b64_tr_b16 v[160:161], %14 offset:37952\n" \
    "v_mfma_f32_16x16x32_bf16 %5, v[112:115], v[100:103], %5\n" \
    "ds_read_b64_tr_b16 v[162:163], %14 offset:39744\n" \
    "v_mfma_f32_16x16x32_bf16 %6, v[116:119], v[96:99], %6\n" \
    "ds_read_b64_tr_b16 v[164:165], %14 offset:37984\n" \
    "v_mfma_f32_16x16x32_bf16 %7, v[116:119], v[100:103], %7\n" \
    "ds_read_b64_tr_b16 v[166:167], %14 offset:39776\n" \
    "s_cmp_lg_u32 %16, 0\n" \
    "s_cbranch_scc1 G1C20_%=\n" \
    "v_mfma_f32_16x16x32_bf16 %8, v[80:83], v[68:71], %8\n" \
    "v_mfma_f32_16x16x32_bf16 %8, v[104:107], v[68:71], %8\n" \
    "s_branch G1C2E_%=\n" \
    "G1C20_%=:\n" \
    "s_cmp_lg_u32 %16, 1\n" \
    "s_cbranch_scc1 G1C21_%=\n" \
    "v_mfma_f32_16x16x32_bf16 %8, v[84:87], v[68:71], %8\n" \
    "v_mfma_f32_16x16x32_bf16 %8, v[108:111], v[68:71], %8\n" \
    "s_branch G1C2E_%=\n" \
    "G1C21_%=:\n" \
    "s_cmp_lg_u32 %16, 2\n" \
    "s_cbranch_scc1 G1C22_%=\n" \
    "v_mfma_f32_16x16x32_bf16 %8, v[88:91], v[68:71], %8\n" \
    "v_mfma_f32_16x16x32_bf16 %8, v[112:115], v[68:71], %8\n" \
    "s_branch G1C2E_%=\n" \
    "G1C22_%=:\n" \
    "s_cmp_lg_u32 %16, 3\n" \
    "s_cbranch_scc1 G1C23_%=\n" \
    "v_mfma_f32_16x16x32_bf16 %8, v[92:95], v[68:71], %8\n" \
    "v_mfma_f32_16x16x32_bf16 %8, v[116:119], v[68:71], %8\n" \
    "G1C23_%=:\n" \
    "G1C2E_%=:\n" \
    "s_waitcnt lgkmcnt(0)\n" \
    "s_barrier\n" \
    "v_mfma_f32_16x16x32_bf16 %0, v[128:131], v[120:123], %0\n" \
    "ds_read_b128 v[72:75], v58 offset:0\n" \
    "ds_read_b64_tr_b16 v[80:81], v66 offset:0\n" \
    "v_mfma_f32_16x16x32_bf16 %1, v[128:131], v[124:127], %1\n" \
    "ds_read_b64_tr_b16 v[82:83], v66 offset:1792\n" \
    "ds_read_b128 v[76:79], v58 offset:2048\n" \
    "v_mfma_f32_16x16x32_bf16 %2, v[132:135], v[120:123], %2\n" \
    "ds_read_b64_tr_b16 v[84:85], v66 offset:32\n" \
    "ds_read_b64_tr_b16 v[86:87], v66 offset:1824\n" \
    "v_mfma_f32_16x16x32_bf16 %3, v[132:135], v[124:127], %3\n" \
    "ds_read_b64_tr_b16 v[88:89], v66 offset:64\n" \
    "ds_read_b64_tr_b16 v[90:91], v66 offset:1856\n" \
    "v_mfma_f32_16x16x32_bf16 %4, v[136:139], v[120:123], %4\n" \
    "ds_read_b64_tr_b16 v[92:93], v66 offset:96\n" \
    "v_mfma_f32_16x16x32_bf16 %5, v[136:139], v[124:127], %5\n" \
    "ds_read_b64_tr_b16 v[94:95], v66 offset:1888\n" \
    "v_mfma_f32_16x16x32_bf16 %6, v[140:143], v[120:123], %6\n" \
    "ds_read_b128 v[96:99], v59 offset:0\n" \
    "v_mfma_f32_16x16x32_bf16 %7, v[140:143], v[124:127], %7\n" \
    "ds_read_b64_tr_b16 v[104:105], v66 offset:7168\n" \
    "v_mfma_f32_16x16x32_bf16 %0, v[152:155], v[144:147], %0\n" \
    "ds_read_b64_tr_b16 v[106:107], v66 offset:8960\n" \
    "v_mfma_f32_16x16x32_bf16 %1, v[152:155], v[148:151], %1\n" \
    "ds_read_b128 v[100:103], v59 offset:2048\n" \
    "v_mfma_f32_16x16x32_bf16 %2, v[156:159], v[144:147], %2\n" \
    "ds_read_b64_tr_b16 v[108:109], v66 offset:7200\n" \
    "v_mfma_f32_16x16x32_bf16 %3, v[156:159], v[148:151], %3\n" \
    "ds_read_b64_tr_b16 v[110:111], v66 offset:8992\n" \
    "v_mfma_f32_16x16x32_bf16 %4, v[160:163], v[144:147], %4\n" \
    "ds_read_b64_tr_b16 v[112:113], v66 offset:7232\n" \
    "v_mfma_f32_16x16x32_bf16 %5, v[160:163], v[148:151], %5\n" \
    "ds_read_b64_tr_b16 v[114:115], v66 offset:9024\n" \
    "v_mfma_f32_16x16x32_bf16 %6, v[164:167], v[144:147], %6\n" \
    "ds_read_b64_tr_b16 v[116:117], v66 offset:7264\n" \
    "v_mfma_f32_16x16x32_bf16 %7, v[164:167], v[148:151], %7\n" \
    "ds_read_b64_tr_b16 v[118:119], v66 offset:9056\n" \
    "s_cmp_lg_u32 %16, 0\n" \
    "s_cbranch_scc1 G1C30_%=\n" \
    "v_mfma_f32_16x16x32_bf16 %8, v[128:131], v[68:71], %8\n" \
    "v_mfma_f32_16x16x32_bf16 %8, v[152:155], v[68:71], %8\n" \
    "s_branch G1C3E_%=\n" \
    "G1C30_%=:\n" \
    "s_cmp_lg_u32 %16, 1\n" \
    "s_cbranch_scc1 G1C31_%=\n" \
    "v_mfma_f32_16x16x32_bf16 %8, v[132:135], v[68:71], %8\n" \
    "v_mfma_f32_16x16x32_bf16 %8, v[156:159], v[68:71], %8\n" \
    "s_branch G1C3E_%=\n" \
    "G1C31_%=:\n" \
    "s_cmp_lg_u32 %16, 2\n" \
    "s_cbranch_scc1 G1C32_%=\n" \
    "v_mfma_f32_16x16x32_bf16 %8, v[136:139], v[68:71], %8\n" \
    "v_mfma_f32_16x16x32_bf16 %8, v[160:163], v[68:71], %8\n" \
    "s_branch G1C3E_%=\n" \
    "G1C32_%=:\n" \
    "s_cmp_lg_u32 %16, 3\n" \
    "s_cbranch_scc1 G1C33_%=\n" \
    "v_mfma_f32_16x16x32_bf16 %8, v[140:143], v[68:71], %8\n" \
    "v_mfma_f32_16x16x32_bf16 %8, v[164:167], v[68:71], %8\n" \
    "G1C33_%=:\n" \
    "G1C3E_%=:\n" \
    "s_waitcnt vmcnt(0) lgkmcnt(0)\n" \
    "s_barrier\n" \
    "v_mfma_f32_16x16x32_bf16 %0, v[80:83], v[72:75], %0\n" \
    "ds_read_b128 v[120:123], v58 offset:30720\n" \
    "ds_read_b64_tr_b16 v[128:129], v66 offset:30720\n" \
    "v_mfma_f32_16x16x32_bf16 %1, v[80:83], v[76:79], %1\n" \
    "ds_read_b64_tr_b16 v[130:131], v66 offset:32512\n" \
    "ds_read_b128 v[124:127], v58 offset:32768\n" \
    "v_mfma_f32_16x16x32_bf16 %2, v[84:87], v[72:75], %2\n" \
    "ds_read_b64_tr_b16 v[132:133], v66 offset:30752\n" \
    "ds_read_b64_tr_b16 v[134:135], v66 offset:32544\n" \
    "v_mfma_f32_16x16x32_bf16 %3, v[84:87], v[76:79], %3\n" \
    "ds_read_b64_tr_b16 v[136:137], v66 offset:30784\n" \
    "ds_read_b64_tr_b16 v[138:139], v66 offset:32576\n" \
    "v_mfma_f32_16x16x32_bf16 %4, v[88:91], v[72:75], %4\n" \
    "ds_read_b64_tr_b16 v[140:141], v66 offset:30816\n" \
    "v_mfma_f32_16x16x32_bf16 %5, v[88:91], v[76:79], %5\n" \
    "ds_read_b64_tr_b16 v[142:143], v66 offset:32608\n" \
    "v_mfma_f32_16x16x32_bf16 %6, v[92:95], v[72:75], %6\n" \
    "ds_read_b128 v[144:147], v59 offset:30720\n" \
    "v_mfma_f32_16x16x32_bf16 %7, v[92:95], v[76:79], %7\n" \
    "ds_read_b64_tr_b16 v[152:153], v66 offset:37888\n" \
    "v_mfma_f32_16x16x32_bf16 %0, v[104:107], v[96:99], %0\n" \
    "ds_read_b64_tr_b16 v[154:155], v66 offset:39680\n" \
    "v_mfma_f32_16x16x32_bf16 %1, v[104:107], v[100:103], %1\n" \
    "ds_read_b128 v[148:151], v59 offset:32768\n" \
    "v_mfma_f32_16x16x32_bf16 %2, v[108:111], v[96:99], %2\n" \
    "ds_read_b64_tr_b16 v[156:157], v66 offset:37920\n" \
    "v_mfma_f32_16x16x32_bf16 %3, v[108:111], v[100:103], %3\n" \
    "ds_read_b64_tr_b16 v[158:159], v66 offset:39712\n" \
    "v_mfma_f32_16x16x32_bf16 %4, v[112:115], v[96:99], %4\n" \
    "ds_read_b64_tr_b16 v[160:161], v66 offset:37952\n" \
    "v_mfma_f32_16x16x32_bf16 %5, v[112:115], v[100:103], %5\n" \
    "ds_read_b64_tr_b16 v[162:163], v66 offset:39744\n" \
    "v_mfma_f32_16x16x32_bf16 %6, v[116:119], v[96:99], %6\n" \
    "ds_read_b64_tr_b16 v[164:165], v66 offset:37984\n" \
    "v_mfma_f32_16x16x32_bf16 %7, v[116:119], v[100:103], %7\n" \
    "ds_read_b64_tr_b16 v[166:167], v66 offset:39776\n" \
    "s_cmp_lg_u32 %16, 0\n" \
    "s_cbranch_scc1 G1C40_%=\n" \
    "v_mfma_f32_16x16x32_bf16 %8, v[80:83], v[68:71], %8\n" \
    "v_mfma_f32_16x16x32_bf16 %8, v[104:107], v[68:71], %8\n" \
    "s_branch G1C4E_%=\n" \
    "G1C40_%=:\n" \
    "s_cmp_lg_u32 %16, 1\n" \
    "s_cbranch_scc1 G1C41_%=\n" \
    "v_mfma_f32_16x16x32_bf16 %8, v[84:87], v[68:71], %8\n" \
    "v_mfma_f32_16x16x32_bf16 %8, v[108:111], v[68:71], %8\n" \
    "s_branch G1C4E_%=\n" \
    "G1C41_%=:\n" \
    "s_cmp_lg_u32 %16, 2\n" \
    "s_cbranch_scc1 G1C42_%=\n" \
    "v_mfma_f32_16x16x32_bf16 %8, v[88:91], v[68:71], %8\n" \
    "v_mfma_f32_16x16x32_bf16 %8, v[112:115], v[68:71], %8\n" \
    "s_branch G1C4E_%=\n" \
    "G1C42_%=:\n" \
    "s_cmp_lg_u32 %16, 3\n" \
    "s_cbranch_scc1 G1C43_%=\n" \
    "v_mfma_f32_16x16x32_bf16 %8, v[92:95], v[68:71], %8\n" \
    "v_mfma_f32_16x16x32_bf16 %8, v[116:119], v[68:71], %8\n" \
    "G1C43_%=:\n" \
    "G1C4E_%=:\n" \
    "s_waitcnt lgkmcnt(0)\n" \
    "s_barrier\n" \
    "v_mfma_f32_16x16x32_bf16 %0, v[128:131], v[120:123], %0\n" \
    "ds_read_b128 v[72:75], v60 offset:0\n" \
    "ds_read_b64_tr_b16 v[80:81], v67 offset:0\n" \
    "v_mfma_f32_16x16x32_bf16 %1, v[128:131], v[124:127], %1\n" \
    "ds_read_b64_tr_b16 v[82:83], v67 offset:1792\n" \
    "ds_read_b128 v[76:79], v60 offset:2048\n" \
    "v_mfma_f32_16x16x32_bf16 %2, v[132:135], v[120:123], %2\n" \
    "ds_read_b64_tr_b16 v[84:85], v67 offset:32\n" \
    "ds_read_b64_tr_b16 v[86:87], v67 offset:1824\n" \
    "v_mfma_f32_16x16x32_bf16 %3, v[132:135], v[124:127], %3\n" \
    "ds_read_b64_tr_b16 v[88:89], v67 offset:64\n" \
    "ds_read_b64_tr_b16 v[90:91], v67 offset:1856\n" \
    "v_mfma_f32_16x16x32_bf16 %4, v[136:139], v[120:123], %4\n" \
    "ds_read_b64_tr_b16 v[92:93], v67 offset:96\n" \
    "v_mfma_f32_16x16x32_bf16 %5, v[136:139], v[124:127], %5\n" \
    "ds_read_b64_tr_b16 v[94:95], v67 offset:1888\n" \
    "v_mfma_f32_16x16x32_bf16 %6, v[140:143], v[120:123], %6\n" \
    "ds_read_b128 v[96:99], v61 offset:0\n" \
    "v_mfma_f32_16x16x32_bf16 %7, v[140:143], v[124:127], %7\n" \
    "ds_read_b64_tr_b16 v[104:105], v67 offset:7168\n" \
    "v_mfma_f32_16x16x32_bf16 %0, v[152:155], v[144:147], %0\n" \
    "ds_read_b64_tr_b16 v[106:107], v67 offset:8960\n" \
    "v_mfma_f32_16x16x32_bf16 %1, v[152:155], v[148:151], %1\n" \
    "ds_read_b128 v[100:103], v61 offset:2048\n" \
    "v_mfma_f32_16x16x32_bf16 %2, v[156:159], v[144:147], %2\n" \
    "ds_read_b64_tr_b16 v[108:109], v67 offset:7200\n" \
    "v_mfma_f32_16x16x32_bf16 %3, v[156:159], v[148:151], %3\n" \
    "ds_read_b64_tr_b16 v[110:111], v67 offset:8992\n" \
    "v_mfma_f32_16x16x32_bf16 %4, v[160:163], v[144:147], %4\n" \
    "ds_read_b64_tr_b16 v[112:113], v67 offset:7232\n" \
    "v_mfma_f32_16x16x32_bf16 %5, v[160:163], v[148:151], %5\n" \
    "ds_read_b64_tr_b16 v[114:115], v67 offset:9024\n" \
    "v_mfma_f32_16x16x32_bf16 %6, v[164:167], v[144:147], %6\n" \
    "ds_read_b64_tr_b16 v[116:117], v67 offset:7264\n" \
    "v_mfma_f32_16x16x32_bf16 %7, v[164:167], v[148:151], %7\n" \
    "ds_read_b64_tr_b16 v[118:119], v67 offset:9056\n" \
    "s_cmp_lg_u32 %16, 0\n" \
    "s_cbranch_scc1 G1C50_%=\n" \
    "v_mfma_f32_16x16x32_bf16 %8, v[128:131], v[68:71], %8\n" \
    "v_mfma_f32_16x16x32_bf16 %8, v[152:155], v[68:71], %8\n" \
    "s_branch G1C5E_%=\n" \
    "G1C50_%=:\n" \
    "s_cmp_lg_u32 %16, 1\n" \
    "s_cbranch_scc1 G1C51_%=\n" \
    "v_mfma_f32_16x16x32_bf16 %8, v[132:135], v[68:71], %8\n" \
    "v_mfma_f32_16x16x32_bf16 %8, v[156:159], v[68:71], %8\n" \
    "s_branch G1C5E_%=\n" \
    "G1C51_%=:\n" \
    "s_cmp_lg_u32 %16, 2\n" \
    "s_cbranch_scc1 G1C52_%=\n" \
    "v_mfma_f32_16x16x32_bf16 %8, v[136:139], v[68:71], %8\n" \
    "v_mfma_f32_16x16x32_bf16 %8, v[160:163], v[68:71], %8\n" \
    "s_branch G1C5E_%=\n" \
    "G1C52_%=:\n" \
    "s_cmp_lg_u32 %16, 3\n" \
    "s_cbranch_scc1 G1C53_%=\n" \
    "v_mfma_f32_16x16x32_bf16 %8, v[140:143], v[68:71], %8\n" \
    "v_mfma_f32_16x16x32_bf16 %8, v[164:167], v[68:71], %8\n" \
    "G1C53_%=:\n" \
    "G1C5E_%=:\n" \
    "s_waitcnt lgkmcnt(0)\n" \
    "s_barrier\n" \
    "v_mfma_f32_16x16x32_bf16 %0, v[80:83], v[72:75], %0\n" \
    "ds_read_b128 v[120:123], %12 offset:0\n" \
    "ds_read_b64_tr_b16 v[128:129], %14 offset:0\n" \
    "v_mfma_f32_16x16x32_bf16 %1, v[80:83], v[76:79], %1\n" \
    "ds_read_b64_tr_b16 v[130:131], %14 offset:1792\n" \
    "ds_read_b128 v[124:127], %12 offset:2048\n" \
    "v_mfma_f32_16x16x32_bf16 %2, v[84:87], v[72:75], %2\n" \
    "ds_read_b64_tr_b16 v[132:133], %14 offset:32\n" \
    "ds_read_b64_tr_b16 v[134:135], %14 offset:1824\n" \
    "v_mfma_f32_16x16x32_bf16 %3, v[84:87], v[76:79], %3\n" \
    "ds_read_b64_tr_b16 v[136:137], %14 offset:64\n" \
    "ds_read_b64_tr_b16 v[138:139], %14 offset:1856\n" \
    "v_mfma_f32_16x16x32_bf16 %4, v[88:91], v[72:75], %4\n" \
    "ds_read_b64_tr_b16 v[140:141], %14 offset:96\n" \
    "v_mfma_f32_16x16x32_bf16 %5, v[88:91], v[76:79], %5\n" \
    "ds_read_b64_tr_b16 v[142:143], %14 offset:1888\n" \
    "v_mfma_f32_16x16x32_bf16 %6, v[92:95], v[72:75], %6\n" \
    "ds_read_b128 v[144:147], %13 offset:0\n" \
    "v_mfma_f32_16x16x32_bf16 %7, v[92:95], v[76:79], %7\n" \
    "ds_read_b64_tr_b16 v[152:153], %14 offset:7168\n" \
    "v_mfma_f32_16x16x32_bf16 %0, v[104:107], v[96:99], %0\n" \
    "ds_read_b64_tr_b16 v[154:155], %14 offset:8960\n" \
    "v_mfma_f32_16x16x32_bf16 %1, v[104:107], v[100:103], %1\n" \
    "ds_read_b128 v[148:151], %13 offset:2048\n" \
    "v_mfma_f32_16x16x32_bf16 %2, v[108:111], v[96:99], %2\n" \
    "ds_read_b64_tr_b16 v[156:157], %14 offset:7200\n" \
    "v_mfma_f32_16x16x32_bf16 %3, v[108:111], v[100:103], %3\n" \
    "ds_read_b64_tr_b16 v[158:159], %14 offset:8992\n" \
    "v_mfma_f32_16x16x32_bf16 %4, v[112:115], v[96:99], %4\n" \
    "ds_read_b64_tr_b16 v[160:161], %14 offset:7232\n" \
    "v_mfma_f32_16x16x32_bf16 %5, v[112:115], v[100:103], %5\n" \
    "ds_read_b64_tr_b16 v[162:163], %14 offset:9024\n" \
    "v_mfma_f32_16x16x32_bf16 %6, v[116:119], v[96:99], %6\n" \
    "ds_read_b64_tr_b16 v[164:165], %14 offset:7264\n" \
    "v_mfma_f32_16x16x32_bf16 %7, v[116:119], v[100:103], %7\n" \
    "ds_read_b64_tr_b16 v[166:167], %14 offset:9056\n" \
    "s_cmp_lg_u32 %16, 0\n" \
    "s_cbranch_scc1 G1C60_%=\n" \
    "v_mfma_f32_16x16x32_bf16 %8, v[80:83], v[68:71], %8\n" \
    "v_mfma_f32_16x16x32_bf16 %8, v[104:107], v[68:71], %8\n" \
    "s_branch G1C6E_%=\n" \
    "G1C60_%=:\n" \
    "s_cmp_lg_u32 %16, 1\n" \
    "s_cbranch_scc1 G1C61_%=\n" \
    "v_mfma_f32_16x16x32_bf16 %8, v[84:87], v[68:71], %8\n" \
    "v_mfma_f32_16x16x32_bf16 %8, v[108:111], v[68:71], %8\n" \
    "s_branch G1C6E_%=\n" \
    "G1C61_%=:\n" \
    "s_cmp_lg_u32 %16, 2\n" \
    "s_cbranch_scc1 G1C62_%=\n" \
    "v_mfma_f32_16x16x32_bf16 %8, v[88:91], v[68:71], %8\n" \
    "v_mfma_f32_16x16x32_bf16 %8, v[112:115], v[68:71], %8\n" \
    "s_branch G1C6E_%=\n" \
    "G1C62_%=:\n" \
    "s_cmp_lg_u32 %16, 3\n" \
    "s_cbranch_scc1 G1C63_%=\n" \
    "v_mfma_f32_16x16x32_bf16 %8, v[92:95], v[68:71], %8\n" \
    "v_mfma_f32_16x16x32_bf16 %8, v[116:119], v[68:71], %8\n" \
    "G1C63_%=:\n" \
    "G1C6E_%=:\n" \
    "s_waitcnt lgkmcnt(0)\n" \
    "s_cmp_lg_u32 %16, 0\n" \
    "s_cbranch_scc1 G1C70_%=\n" \
    "v_mfma_f32_16x16x32_bf16 %8, v[128:131], v[68:71], %8\n" \
    "v_mfma_f32_16x16x32_bf16 %8, v[152:155], v[68:71], %8\n" \
    "s_branch G1C7E_%=\n" \
    "G1C70_%=:\n" \
    "s_cmp_lg_u32 %16, 1\n" \
    "s_cbranch_scc1 G1C71_%=\n" \
    "v_mfma_f32_16x16x32_bf16 %8, v[132:135], v[68:71], %8\n" \
    "v_mfma_f32_16x16x32_bf16 %8, v[156:159], v[68:71], %8\n" \
    "s_branch G1C7E_%=\n" \
    "G1C71_%=:\n" \
    "s_cmp_lg_u32 %16, 2\n" \
    "s_cbranch_scc1 G1C72_%=\n" \
    "v_mfma_f32_16x16x32_bf16 %8, v[136:139], v[68:71], %8\n" \
    "v_mfma_f32_16x16x32_bf16 %8, v[160:163], v[68:71], %8\n" \
    "s_branch G1C7E_%=\n" \
    "G1C72_%=:\n" \
    "s_cmp_lg_u32 %16, 3\n" \
    "s_cbranch_scc1 G1C73_%=\n" \
    "v_mfma_f32_16x16x32_bf16 %8, v[140:143], v[68:71], %8\n" \
    "v_mfma_f32_16x16x32_bf16 %8, v[164:167], v[68:71], %8\n" \
    "G1C73_%=:\n" \
    "G1C7E_%=:\n" \
    "s_nop 7\n" \
    "s_mov_b32 exec_lo, 0x10001\n" \
    "s_mov_b32 exec_hi, 0x10001\n" \
    "ds_write_b128 %15, %8\n" \
    "s_mov_b64 exec, -1\n" \
    "v_mov_b32 v72, 1\n" \
    "s_waitcnt lgkmcnt(0)\n" \
    "ds_add_u32 %17, v72\n" \
    "v_mfma_f32_16x16x32_bf16 %0, v[128:131], v[120:123], %0\n" \
    "v_mfma_f32_16x16x32_bf16 %1, v[128:131], v[124:127], %1\n" \
    "v_mfma_f32_16x16x32_bf16 %2, v[132:135], v[120:123], %2\n" \
    "v_mfma_f32_16x16x32_bf16 %3, v[132:135], v[124:127], %3\n" \
    "v_mfma_f32_16x16x32_bf16 %4, v[136:139], v[120:123], %4\n" \
    "v_mfma_f32_16x16x32_bf16 %5, v[136:139], v[124:127], %5\n" \
    "v_mfma_f32_16x16x32_bf16 %6, v[140:143], v[120:123], %6\n" \
    "v_mfma_f32_16x16x32_bf16 %7, v[140:143], v[124:127], %7\n" \
    "v_mfma_f32_16x16x32_bf16 %0, v[152:155], v[144:147], %0\n" \
    "v_mfma_f32_16x16x32_bf16 %1, v[152:155], v[148:151], %1\n" \
    "v_mfma_f32_16x16x32_bf16 %2, v[156:159], v[144:147], %2\n" \
    "v_mfma_f32_16x16x32_bf16 %3, v[156:159], v[148:151], %3\n" \
    "v_mfma_f32_16x16x32_bf16 %4, v[160:163], v[144:147], %4\n" \
    "v_mfma_f32_16x16x32_bf16 %5, v[160:163], v[148:151], %5\n" \
    "v_mfma_f32_16x16x32_bf16 %6, v[164:167], v[144:147], %6\n" \
    "v_mfma_f32_16x16x32_bf16 %7, v[164:167], v[148:151], %7\n" \
    "s_waitcnt lgkmcnt(0)\n" \
    "s_nop 7\n" \
    ""
#define G1_FFN1A \
    "v_add_u32 v58, 61440, %13\n" \
    "v_add_u32 v59, 61440, %14\n" \
    "v_add_u32 v66, 61440, %15\n" \
    "v_add_u32 v60, 122880, %13\n" \
    "v_add_u32 v61, 122880, %14\n" \
    "v_add_u32 v67, 122880, %15\n" \
    "v_mov_b32 v68, 0x3f803f80\n" \
    "v_mov_b32 v69, 0x3f803f80\n" \
    "v_mov_b32 v70, 0x3f803f80\n" \
    "v_mov_b32 v71, 0x3f803f80\n" \
    "ds_read_b128 v[120:123], v60 offset:0\n" \
    "ds_read_b64_tr_b16 v[128:129], v67 offset:0\n" \
    "v_mfma_f32_16x16x32_bf16 %0, v[80:83], v[72:75], 0\n" \
    "ds_read_b64_tr_b16 v[130:131], v67 offset:1792\n" \
    "ds_read_b128 v[124:127], v60 offset:2048\n" \
    "v_mfma_f32_16x16x32_bf16 %1, v[80:83], v[76:79], 0\n" \
    "ds_read_b64_tr_b16 v[132:133], v67 offset:32\n" \
    "ds_read_b64_tr_b16 v[134:135], v67 offset:1824\n" \
    "v_mfma_f32_16x16x32_bf16 %2, v[84:87], v[72:75], 0\n" \
    "ds_read_b64_tr_b16 v[136:137], v67 offset:64\n" \
    "ds_read_b64_tr_b16 v[138:139], v67 offset:1856\n" \
    "v_mfma_f32_16x16x32_bf16 %3, v[84:87], v[76:79], 0\n" \
    "ds_read_b128 v[140:143], v61 offset:0\n" \
    "v_mfma_f32_16x16x32_bf16 %4, v[88:91], v[72:75], 0\n" \
    "ds_read_b64_tr_b16 v[148:149], v67 offset:7168\n" \
    "v_mfma_f32_16x16x32_bf16 %5, v[88:91], v[76:79], 0\n" \
    "ds_read_b64_tr_b16 v[150:151], v67 offset:8960\n" \
    "v_mfma_f32_16x16x32_bf16 %0, v[100:103], v[92:95], %0\n" \
    "ds_read_b128 v[144:147], v61 offset:2048\n" \
    "v_mfma_f32_16x16x32_bf16 %1, v[100:103], v[96:99], %1\n" \
    "ds_read_b64_tr_b16 v[152:153], v67 offset:7200\n" \
    "v_mfma_f32_16x16x32_bf16 %2, v[104:107], v[92:95], %2\n" \
    "ds_read_b64_tr_b16 v[154:155], v67 offset:8992\n" \
    "v_mfma_f32_16x16x32_bf16 %3, v[104:107], v[96:99], %3\n" \
    "ds_read_b64_tr_b16 v[156:157], v67 offset:7232\n" \
    "v_mfma_f32_16x16x32_bf16 %4, v[108:111], v[92:95], %4\n" \
    "ds_read_b64_tr_b16 v[158:159], v67 offset:9024\n" \
    "v_mfma_f32_16x16x32_bf16 %5, v[108:111], v[96:99], %5\n" \
    "s_cmp_lg_u32 %17, 0\n" \
    "s_cbranch_scc1 G1C00_%=\n" \
    "v_mfma_f32_16x16x32_bf16 %6, v[80:83], v[68:71], %6\n" \
    "v_mfma_f32_16x16x32_bf16 %6, v[100:103], v[68:71], %6\n" \
    "s_branch G1C0E_%=\n" \
    "G1C00_%=:\n" \
    "s_cmp_lg_u32 %17, 1\n" \
    "s_cbranch_scc1 G1C01_%=\n" \
    "v_mfma_f32_16x16x32_bf16 %6, v[84:87], v[68:71], %6\n" \
    "v_mfma_f32_16x16x32_bf16 %6, v[104:107], v[68:71], %6\n" \
    "s_branch G1C0E_%=\n" \
    "G1C01_%=:\n" \
    "s_cmp_lg_u32 %17, 2\n" \
    "s_cbranch_scc1 G1C02_%=\n" \
    "v_mfma_f32_16x16x32_bf16 %6, v[88:91], v[68:71], %6\n" \
    "v_mfma_f32_16x16x32_bf16 %6, v[108:111], v[68:71], %6\n" \
    "G1C02_%=:\n" \
    "G1C0E_%=:\n" \
    "s_waitcnt lgkmcnt(0)\n" \
    "s_nop 7\n" \
    ""
#define G1_FFN1B \
    "v_add_u32 v58, 61440, %10\n" \
    "v_add_u32 v59, 61440, %11\n" \
    "v_add_u32 v66, 61440, %12\n" \
    "v_add_u32 v60, 122880, %10\n" \
    "v_add_u32 v61, 122880, %11\n" \
    "v_add_u32 v67, 122880, %12\n" \
    "v_mov_b32 v68, 0x3f803f80\n" \
    "v_mov_b32 v69, 0x3f803f80\n" \
    "v_mov_b32 v70, 0x3f803f80\n" \
    "v_mov_b32 v71, 0x3f803f80\n" \
    "s_waitcnt lgkmcnt(0)\n" \
    "s_barrier\n" \
    "ds_read_b128 v[72:75], %10 offset:0\n" \
    "ds_read_b64_tr_b16 v[80:81], %12 offset:0\n" \
    "v_mfma_f32_16x16x32_bf16 %0, v[128:131], v[120:123], %0\n" \
    "ds_read_b64_tr_b16 v[82:83], %12 offset:1792\n" \
    "ds_read_b128 v[76:79], %10 offset:2048\n" \
    "v_mfma_f32_16x16x32_bf16 %1, v[128:131], v[124:127], %1\n" \
    "ds_read_b64_tr_b16 v[84:85], %12 offset:32\n" \
    "ds_read_b64_tr_b16 v[86:87], %12 offset:1824\n" \
    "v_mfma_f32_16x16x32_bf16 %2, v[132:135], v[120:123], %2\n" \
    "ds_read_b64_tr_b16 v[88:89], %12 offset:64\n" \
    "ds_read_b64_tr_b16 v[90:91], %12 offset:1856\n" \
    "v_mfma_f32_16x16x32_bf16 %3, v[132:135], v[124:127], %3\n" \
    "ds_read_b128 v[92:95], %11 offset:0\n" \
    "v_mfma_f32_16x16x32_bf16 %4, v[136:139], v[120:123], %4\n" \
    "ds_read_b64_tr_b16 v[100:101], %12 offset:7168\n" \
    "v_mfma_f32_16x16x32_bf16 %5, v[136:139], v[124:127], %5\n" \
    "ds_read_b64_tr_b16 v[102:103], %12 offset:8960\n" \
    "v_mfma_f32_16x16x32_bf16 %0, v[148:151], v[140:143], %0\n" \
    "ds_read_b128 v[96:99], %11 offset:2048\n" \
    "v_mfma_f32_16x16x32_bf16 %1, v[148:151], v[144:147], %1\n" \
    "ds_read_b64_tr_b16 v[104:105], %12 offset:7200\n" \
    "v_mfma_f32_16x16x32_bf16 %2, v[152:155], v[140:143], %2\n" \
    "ds_read_b64_tr_b16 v[106:107], %12 offset:8992\n" \
    "v_mfma_f32_16x16x32_bf16 %3, v[152:155], v[144:147], %3\n" \
    "ds_read_b64_tr_b16 v[108:109], %12 offset:7232\n" \
    "v_mfma_f32_16x16x32_bf16 %4, v[156:159], v[140:143], %4\n" \
    "ds_read_b64_tr_b16 v[110:111], %12 offset:9024\n" \
    "v_mfma_f32_16x16x32_bf16 %5, v[156:159], v[144:147], %5\n" \
    "s_cmp_lg_u32 %14, 0\n" \
    "s_cbranch_scc1 G1C10_%=\n" \
    "v_mfma_f32_16x16x32_bf16 %6, v[128:131], v[68:71], %6\n" \
    "v_mfma_f32_16x16x32_bf16 %6, v[148:151], v[68:71], %6\n" \
    "s_branch G1C1E_%=\n" \
    "G1C10_%=:\n" \
    "s_cmp_lg_u32 %14, 1\n" \
    "s_cbranch_scc1 G1C11_%=\n" \
    "v_mfma_f32_16x16x32_bf16 %6, v[132:135], v[68:71], %6\n" \
    "v_mfma_f32_16x16x32_bf16 %6, v[152:155], v[68:71], %6\n" \
    "s_branch G1C1E_%=\n" \
    "G1C11_%=:\n" \
    "s_cmp_lg_u32 %14, 2\n" \
    "s_cbranch_scc1 G1C12_%=\n" \
    "v_mfma_f32_16x16x32_bf16 %6, v[136:139], v[68:71], %6\n" \
    "v_mfma_f32_16x16x32_bf16 %6, v[156:159], v[68:71], %6\n" \
    "G1C12_%=:\n" \
    "G1C1E_%=:\n" \
    "s_waitcnt lgkmcnt(0)\n" \
    "s_barrier\n" \
    "ds_read_b128 v[120:123], %10 offset:30720\n" \
    "ds_read_b64_tr_b16 v[128:129], %12 offset:30720\n" \
    "v_mfma_f32_16x16x32_bf16 %0, v[80:83], v[72:75], %0\n" \
    "ds_read_b64_tr_b16 v[130:131], %12 offset:32512\n" \
    "ds_read_b128 v[124:127], %10 offset:32768\n" \
    "v_mfma_f32_16x16x32_bf16 %1, v[80:83], v[76:79], %1\n" \
    "ds_read_b64_tr_b16 v[132:133], %12 offset:30752\n" \
    "ds_read_b64_tr_b16 v[134:135], %12 offset:32544\n" \
    "v_mfma_f32_16x16x32_bf16 %2, v[84:87], v[72:75], %2\n" \
    "ds_read_b64_tr_b16 v[136:137], %12 offset:30784\n" \
    "ds_read_b64_tr_b16 v[138:139], %12 offset:32576\n" \
    "v_mfma_f32_16x16x32_bf16 %3, v[84:87], v[76:79], %3\n" \
    "ds_read_b128 v[140:143], %11 offset:30720\n" \
    "v_mfma_f32_16x16x32_bf16 %4, v[88:91], v[72:75], %4\n" \
    "ds_read_b64_tr_b16 v[148:149], %12 offset:37888\n" \
    "v_mfma_f32_16x16x32_bf16 %5, v[88:91], v[76:79], %5\n" \
    "ds_read_b64_tr_b16 v[150:151], %12 offset:39680\n" \
    "v_mfma_f32_16x16x32_bf16 %0, v[100:103], v[92:95], %0\n" \
    "ds_read_b128 v[144:147], %11 offset:32768\n" \
    "v_mfma_f32_16x16x32_bf16 %1, v[100:103], v[96:99], %1\n" \
    "ds_read_b64_tr_b16 v[152:153], %12 offset:37920\n" \
    "v_mfma_f32_16x16x32_bf16 %2, v[104:107], v[92:95], %2\n" \
    "ds_read_b64_tr_b16 v[154:155], %12 offset:39712\n" \
    "v_mfma_f32_16x16x32_bf16 %3, v[104:107], v[96:99], %3\n" \
    "ds_read_b64_tr_b16 v[156:157], %12 offset:37952\n" \
    "v_mfma_f32_16x16x32_bf16 %4, v[108:111], v[92:95], %4\n" \
    "ds_read_b64_tr_b16 v[158:159], %12 offset:39744\n" \
    "v_mfma_f32_16x16x32_bf16 %5, v[108:111], v[96:99], %5\n" \
    "s_cmp_lg_u32 %14, 0\n" \
    "s_cbranch_scc1 G1C20_%=\n" \
    "v_mfma_f32_16x16x32_bf16 %6, v[80:83], v[68:71], %6\n" \
    "v_mfma_f32_16x16x32_bf16 %6, v[100:103], v[68:71], %6\n" \
    "s_branch G1C2E_%=\n" \
    "G1C20_%=:\n" \
    "s_cmp_lg_u32 %14, 1\n" \
    "s_cbranch_scc1 G1C21_%=\n" \
    "v_mfma_f32_16x16x32_bf16 %6, v[84:87], v[68:71], %6\n" \
    "v_mfma_f32_16x16x32_bf16 %6, v[104:107], v[68:71], %6\n" \
    "s_branch G1C2E_%=\n" \
    "G1C21_%=:\n" \
    "s_cmp_lg_u32 %14, 2\n" \
    "s_cbranch_scc1 G1C22_%=\n" \
    "v_mfma_f32_16x16x32_bf16 %6, v[88:91], v[68:71], %6\n" \
    "v_mfma_f32_16x16x32_bf16 %6, v[108:111], v[68:71], %6\n" \
    "G1C22_%=:\n" \
    "G1C2E_%=:\n" \
    "s_waitcnt lgkmcnt(0)\n" \
    "s_barrier\n" \
    "ds_read_b128 v[72:75], v58 offset:0\n" \
    "ds_read_b64_tr_b16 v[80:81], v66 offset:0\n" \
    "v_mfma_f32_16x16x32_bf16 %0, v[128:131], v[120:123], %0\n" \
    "ds_read_b64_tr_b16 v[82:83], v66 offset:1792\n" \
    "ds_read_b128 v[76:79], v58 offset:2048\n" \
    "v_mfma_f32_16x16x32_bf16 %1, v[128:131], v[124:127], %1\n" \
    "ds_read_b64_tr_b16 v[84:85], v66 offset:32\n" \
    "ds_read_b64_tr_b16 v[86:87], v66 offset:1824\n" \
    "v_mfma_f32_16x16x32_bf16 %2, v[132:135], v[120:123], %2\n" \
    "ds_read_b64_tr_b16 v[88:89], v66 offset:64\n" \
    "ds_read_b64_tr_b16 v[90:91], v66 offset:1856\n" \
    "v_mfma_f32_16x16x32_bf16 %3, v[132:135], v[124:127], %3\n" \
    "ds_read_b128 v[92:95], v59 offset:0\n" \
    "v_mfma_f32_16x16x32_bf16 %4, v[136:139], v[120:123], %4\n" \
    "ds_read_b64_tr_b16 v[100:101], v66 offset:7168\n" \
    "v_mfma_f32_16x16x32_bf16 %5, v[136:139], v[124:127], %5\n" \
    "ds_read_b64_tr_b16 v[102:103], v66 offset:8960\n" \
    "v_mfma_f32_16x16x32_bf16 %0, v[148:151], v[140:143], %0\n" \
    "ds_read_b128 v[96:99], v59 offset:2048\n" \
    "v_mfma_f32_16x16x32_bf16 %1, v[148:151], v[144:147], %1\n" \
    "ds_read_b64_tr_b16 v[104:105], v66 offset:7200\n" \
    "v_mfma_f32_16x16x32_bf16 %2, v[152:155], v[140:143], %2\n" \
    "ds_read_b64_tr_b16 v[106:107], v66 offset:8992\n" \
    "v_mfma_f32_16x16x32_bf16 %3, v[152:155], v[144:147], %3\n" \
    "ds_read_b64_tr_b16 v[108:109], v66 offset:7232\n" \
    "v_mfma_f32_16x16x32_bf16 %4, v[156:159], v[140:143], %4\n" \
    "ds_read_b64_tr_b16 v[110:111], v66 offset:9024\n" \
    "v_mfma_f32_16x16x32_bf16 %5, v[156:159], v[144:147], %5\n" \
    "s_cmp_lg_u32 %14, 0\n" \
    "s_cbranch_scc1 G1C30_%=\n" \
    "v_mfma_f32_16x16x32_bf16 %6, v[128:131], v[68:71], %6\n" \
    "v_mfma_f32_16x16x32_bf16 %6, v[148:151], v[68:71], %6\n" \
    "s_branch G1C3E_%=\n" \
    "G1C30_%=:\n" \
    "s_cmp_lg_u32 %14, 1\n" \
    "s_cbranch_scc1 G1C31_%=\n" \
    "v_mfma_f32_16x16x32_bf16 %6, v[132:135], v[68:71], %6\n" \
    "v_mfma_f32_16x16x32_bf16 %6, v[152:155], v[68:71], %6\n" \
    "s_branch G1C3E_%=\n" \
    "G1C31_%=:\n" \
    "s_cmp_lg_u32 %14, 2\n" \
    "s_cbranch_scc1 G1C32_%=\n" \
    "v_mfma_f32_16x16x32_bf16 %6, v[136:139], v[68:71], %6\n" \
    "v_mfma_f32_16x16x32_bf16 %6, v[156:159], v[68:71], %6\n" \
    "G1C32_%=:\n" \
    "G1C3E_%=:\n" \
    "s_waitcnt vmcnt(0) lgkmcnt(0)\n" \
    "s_barrier\n" \
    "ds_read_b128 v[120:123], v58 offset:30720\n" \
    "ds_read_b64_tr_b16 v[128:129], v66 offset:30720\n" \
    "v_mfma_f32_16x16x32_bf16 %0, v[80:83], v[72:75], %0\n" \
    "ds_read_b64_tr_b16 v[130:131], v66 offset:32512\n" \
    "ds_read_b128 v[124:127], v58 offset:32768\n" \
    "v_mfma_f32_16x16x32_bf16 %1, v[80:83], v[76:79], %1\n" \
    "ds_read_b64_tr_b16 v[132:133], v66 offset:30752\n" \
    "ds_read_b64_tr_b16 v[134:135], v66 offset:32544\n" \
    "v_mfma_f32_16x16x32_bf16 %2, v[84:87], v[72:75], %2\n" \
    "ds_read_b64_tr_b16 v[136:137], v66 offset:30784\n" \
    "ds_read_b64_tr_b16 v[138:139], v66 offset:32576\n" \
    "v_mfma_f32_16x16x32_bf16 %3, v[84:87], v[76:79], %3\n" \
    "ds_read_b128 v[140:143], v59 offset:30720\n" \
    "v_mfma_f32_16x16x32_bf16 %4, v[88:91], v[72:75], %4\n" \
    "ds_read_b64_tr_b16 v[148:149], v66 offset:37888\n" \
    "v_mfma_f32_16x16x32_bf16 %5, v[88:91], v[76:79], %5\n" \
    "ds_read_b64_tr_b16 v[150:151], v66 offset:39680\n" \
    "v_mfma_f32_16x16x32_bf16 %0, v[100:103], v[92:95], %0\n" \
    "ds_read_b128 v[144:147], v59 offset:32768\n" \
    "v_mfma_f32_16x16x32_bf16 %1, v[100:103], v[96:99], %1\n" \
    "ds_read_b64_tr_b16 v[152:153], v66 offset:37920\n" \
    "v_mfma_f32_16x16x32_bf16 %2, v[104:107], v[92:95], %2\n" \
    "ds_read_b64_tr_b16 v[154:155], v66 offset:39712\n" \
    "v_mfma_f32_16x16x32_bf16 %3, v[104:107], v[96:99], %3\n" \
    "ds_read_b64_tr_b16 v[156:157], v66 offset:37952\n" \
    "v_mfma_f32_16x16x32_bf16 %4, v[108:111], v[92:95], %4\n" \
    "ds_read_b64_tr_b16 v[158:159], v66 offset:39744\n" \
    "v_mfma_f32_16x16x32_bf16 %5, v[108:111], v[96:99], %5\n" \
    "s_cmp_lg_u32 %14, 0\n" \
    "s_cbranch_scc1 G1C40_%=\n" \
    "v_mfma_f32_16x16x32_bf16 %6, v[80:83], v[68:71], %6\n" \
    "v_mfma_f32_16x16x32_bf16 %6, v[100:103], v[68:71], %6\n" \
    "s_branch G1C4E_%=\n" \
    "G1C40_%=:\n" \
    "s_cmp_lg_u32 %14, 1\n" \
    "s_cbranch_scc1 G1C41_%=\n" \
    "v_mfma_f32_16x16x32_bf16 %6, v[84:87], v[68:71], %6\n" \
    "v_mfma_f32_16x16x32_bf16 %6, v[104:107], v[68:71], %6\n" \
    "s_branch G1C4E_%=\n" \
    "G1C41_%=:\n" \
    "s_cmp_lg_u32 %14, 2\n" \
    "s_cbranch_scc1 G1C42_%=\n" \
    "v_mfma_f32_16x16x32_bf16 %6, v[88:91], v[68:71], %6\n" \
    "v_mfma_f32_16x16x32_bf16 %6, v[108:111], v[68:71], %6\n" \
    "G1C42_%=:\n" \
    "G1C4E_%=:\n" \
    "s_waitcnt lgkmcnt(0)\n" \
    "s_barrier\n" \
    "ds_read_b128 v[72:75], v60 offset:0\n" \
    "ds_read_b64_tr_b16 v[80:81], v67 offset:0\n" \
    "v_mfma_f32_16x16x32_bf16 %0, v[128:131], v[120:123], %0\n" \
    "ds_read_b64_tr_b16 v[82:83], v67 offset:1792\n" \
    "ds_read_b128 v[76:79], v60 offset:2048\n" \
    "v_mfma_f32_16x16x32_bf16 %1, v[128:131], v[124:127], %1\n" \
    "ds_read_b64_tr_b16 v[84:85], v67 offset:32\n" \
    "ds_read_b64_tr_b16 v[86:87], v67 offset:1824\n" \
    "v_mfma_f32_16x16x32_bf16 %2, v[132:135], v[120:123], %2\n" \
    "ds_read_b64_tr_b16 v[88:89], v67 offset:64\n" \
    "ds_read_b64_tr_b16 v[90:91], v67 offset:1856\n" \
    "v_mfma_f32_16x16x32_bf16 %3, v[132:135], v[124:127], %3\n" \
    "ds_read_b128 v[92:95], v61 offset:0\n" \
    "v_mfma_f32_16x16x32_bf16 %4, v[136:139], v[120:123], %4\n" \
    "ds_read_b64_tr_b16 v[100:101], v67 offset:7168\n" \
    "v_mfma_f32_16x16x32_bf16 %5, v[136:139], v[124:127], %5\n" \
    "ds_read_b64_tr_b16 v[102:103], v67 offset:8960\n" \
    "v_mfma_f32_16x16x32_bf16 %0, v[148:151], v[140:143], %0\n" \
    "ds_read_b128 v[96:99], v61 offset:2048\n" \
    "v_mfma_f32_16x16x32_bf16 %1, v[148:151], v[144:147], %1\n" \
    "ds_read_b64_tr_b16 v[104:105], v67 offset:7200\n" \
    "v_mfma_f32_16x16x32_bf16 %2, v[152:155], v[140:143], %2\n" \
    "ds_read_b64_tr_b16 v[106:107], v67 offset:8992\n" \
    "v_mfma_f32_16x16x32_bf16 %3, v[152:155], v[144:147], %3\n" \
    "ds_read_b64_tr_b16 v[108:109], v67 offset:7232\n" \
    "v_mfma_f32_16x16x32_bf16 %4, v[156:159], v[140:143], %4\n" \
    "ds_read_b64_tr_b16 v[110:111], v67 offset:9024\n" \
    "v_mfma_f32_16x16x32_bf16 %5, v[156:159], v[144:147], %5\n" \
    "s_cmp_lg_u32 %14, 0\n" \
    "s_cbranch_scc1 G1C50_%=\n" \
    "v_mfma_f32_16x16x32_bf16 %6, v[128:131], v[68:71], %6\n" \
    "v_mfma_f32_16x16x32_bf16 %6, v[148:151], v[68:71], %6\n" \
    "s_branch G1C5E_%=\n" \
    "G1C50_%=:\n" \
    "s_cmp_lg_u32 %14, 1\n" \
    "s_cbranch_scc1 G1C51_%=\n" \
    "v_mfma_f32_16x16x32_bf16 %6, v[132:135], v[68:71], %6\n" \
    "v_mfma_f32_16x16x32_bf16 %6, v[152:155], v[68:71], %6\n" \
    "s_branch G1C5E_%=\n" \
    "G1C51_%=:\n" \
    "s_cmp_lg_u32 %14, 2\n" \
    "s_cbranch_scc1 G1C52_%=\n" \
    "v_mfma_f32_16x16x32_bf16 %6, v[136:139], v[68:71], %6\n" \
    "v_mfma_f32_16x16x32_bf16 %6, v[156:159], v[68:71], %6\n" \
    "G1C52_%=:\n" \
    "G1C5E_%=:\n" \
    "s_waitcnt lgkmcnt(0)\n" \
    "s_barrier\n" \
    "ds_read_b128 v[120:123], %10 offset:0\n" \
    "ds_read_b64_tr_b16 v[128:129], %12 offset:0\n" \
    "v_mfma_f32_16x16x32_bf16 %0, v[80:83], v[72:75], %0\n" \
    "ds_read_b64_tr_b16 v[130:131], %12 offset:1792\n" \
    "ds_read_b128 v[124:127], %10 offset:2048\n" \
    "v_mfma_f32_16x16x32_bf16 %1, v[80:83], v[76:79], %1\n" \
    "ds_read_b64_tr_b16 v[132:133], %12 offset:32\n" \
    "ds_read_b64_tr_b16 v[134:135], %12 offset:1824\n" \
    "v_mfma_f32_16x16x32_bf16 %2, v[84:87], v[72:75], %2\n" \
    "ds_read_b64_tr_b16 v[136:137], %12 offset:64\n" \
    "ds_read_b64_tr_b16 v[138:139], %12 offset:1856\n" \
    "v_mfma_f32_16x16x32_bf16 %3, v[84:87], v[76:79], %3\n" \
    "ds_read_b128 v[140:143], %11 offset:0\n" \
    "v_mfma_f32_16x16x32_bf16 %4, v[88:91], v[72:75], %4\n" \
    "ds_read_b64_tr_b16 v[148:149], %12 offset:7168\n" \
    "v_mfma_f32_16x16x32_bf16 %5, v[88:91], v[76:79], %5\n" \
    "ds_read_b64_tr_b16 v[150:151], %12 offset:8960\n" \
    "v_mfma_f32_16x16x32_bf16 %0, v[100:103], v[92:95], %0\n" \
    "ds_read_b128 v[144:147], %11 offset:2048\n" \
    "v_mfma_f32_16x16x32_bf16 %1, v[100:103], v[96:99], %1\n" \
    "ds_read_b64_tr_b16 v[152:153], %12 offset:7200\n" \
    "v_mfma_f32_16x16x32_bf16 %2, v[104:107], v[92:95], %2\n" \
    "ds_read_b64_tr_b16 v[154:155], %12 offset:8992\n" \
    "v_mfma_f32_16x16x32_bf16 %3, v[104:107], v[96:99], %3\n" \
    "ds_read_b64_tr_b16 v[156:157], %12 offset:7232\n" \
    "v_mfma_f32_16x16x32_bf16 %4, v[108:111], v[92:95], %4\n" \
    "ds_read_b64_tr_b16 v[158:159], %12 offset:9024\n" \
    "v_mfma_f32_16x16x32_bf16 %5, v[108:111], v[96:99], %5\n" \
    "s_cmp_lg_u32 %14, 0\n" \
    "s_cbranch_scc1 G1C60_%=\n" \
    "v_mfma_f32_16x16x32_bf16 %6, v[80:83], v[68:71], %6\n" \
    "v_mfma_f32_16x16x32_bf16 %6, v[100:103], v[68:71], %6\n" \
    "s_branch G1C6E_%=\n" \
    "G1C60_%=:\n" \
    "s_cmp_lg_u32 %14, 1\n" \
    "s_cbranch_scc1 G1C61_%=\n" \
    "v_mfma_f32_16x16x32_bf16 %6, v[84:87], v[68:71], %6\n" \
    "v_mfma_f32_16x16x32_bf16 %6, v[104:107], v[68:71], %6\n" \
    "s_branch G1C6E_%=\n" \
    "G1C61_%=:\n" \
    "s_cmp_lg_u32 %14, 2\n" \
    "s_cbranch_scc1 G1C62_%=\n" \
    "v_mfma_f32_16x16x32_bf16 %6, v[88:91], v[68:71], %6\n" \
    "v_mfma_f32_16x16x32_bf16 %6, v[108:111], v[68:71], %6\n" \
    "G1C62_%=:\n" \
    "G1C6E_%=:\n" \
    "s_waitcnt lgkmcnt(0)\n" \
    "s_cmp_lg_u32 %14, 0\n" \
    "s_cbranch_scc1 G1C70_%=\n" \
    "v_mfma_f32_16x16x32_bf16 %6, v[128:131], v[68:71], %6\n" \
    "v_mfma_f32_16x16x32_bf16 %6, v[148:151], v[68:71], %6\n" \
    "s_branch G1C7E_%=\n" \
    "G1C70_%=:\n" \
    "s_cmp_lg_u32 %14, 1\n" \
    "s_cbranch_scc1 G1C71_%=\n" \
    "v_mfma_f32_16x16x32_bf16 %6, v[132:135], v[68:71], %6\n" \
    "v_mfma_f32_16x16x32_bf16 %6, v[152:155], v[68:71], %6\n" \
    "s_branch G1C7E_%=\n" \
    "G1C71_%=:\n" \
    "s_cmp_lg_u32 %14, 2\n" \
    "s_cbranch_scc1 G1C72_%=\n" \
    "v_mfma_f32_16x16x32_bf16 %6, v[136:139], v[68:71], %6\n" \
    "v_mfma_f32_16x16x32_bf16 %6, v[156:159], v[68:71], %6\n" \
    "G1C72_%=:\n" \
    "G1C7E_%=:\n" \
    "s_nop 7\n" \
    "s_mov_b32 exec_lo, 0x10001\n" \
    "s_mov_b32 exec_hi, 0x10001\n" \
    "ds_write_b128 %13, %6\n" \
    "s_mov_b64 exec, -1\n" \
    "v_mov_b32 v72, 1\n" \
    "s_waitcnt lgkmcnt(0)\n" \
    "ds_add_u32 %15, v72\n" \
    "v_mfma_f32_16x16x32_bf16 %0, v[128:131], v[120:123], %0\n" \
    "v_mfma_f32_16x16x32_bf16 %1, v[128:131], v[124:127], %1\n" \
    "v_mfma_f32_16x16x32_bf16 %2, v[132:135], v[120:123], %2\n" \
    "v_mfma_f32_16x16x32_bf16 %3, v[132:135], v[124:127], %3\n" \
    "v_mfma_f32_16x16x32_bf16 %4, v[136:139], v[120:123], %4\n" \
    "v_mfma_f32_16x16x32_bf16 %5, v[136:139], v[124:127], %5\n" \
    "v_mfma_f32_16x16x32_bf16 %0, v[148:151], v[140:143], %0\n" \
    "v_mfma_f32_16x16x32_bf16 %1, v[148:151], v[144:147], %1\n" \
    "v_mfma_f32_16x16x32_bf16 %2, v[152:155], v[140:143], %2\n" \
    "v_mfma_f32_16x16x32_bf16 %3, v[152:155], v[144:147], %3\n" \
    "v_mfma_f32_16x16x32_bf16 %4, v[156:159], v[140:143], %4\n" \
    "v_mfma_f32_16x16x32_bf16 %5, v[156:159], v[144:147], %5\n" \
    "s_waitcnt lgkmcnt(0)\n" \
    "s_nop 7\n" \
    ""
#define G1_CLOB_LO "v58", "v59", "v60", "v61", "v62", "v63", "v64", "v65", "v66", "v67", "v68", "v69", "v70", "v71"
#define G1_CLOB_X "v72", "v73", "v74", "v75", "v76", "v77", "v78", "v79", "v80", "v81", "v82", "v83", "v84", "v85", "v86", "v87", "v88", "v89", "v90", "v91", "v92", "v93", "v94", "v95", "v96", "v97", "v98", "v99", "v100", "v101", "v102", "v103", "v104", "v105", "v106", "v107", "v108", "v109", "v110", "v111", "v112", "v113", "v114", "v115", "v116", "v117", "v118", "v119"
#define G1_CLOB_Y "v120", "v121", "v122", "v123", "v124", "v125", "v126", "v127", "v128", "v129", "v130", "v131", "v132", "v133", "v134", "v135", "v136", "v137", "v138", "v139", "v140", "v141", "v142", "v143", "v144", "v145", "v146", "v147", "v148", "v149", "v150", "v151", "v152", "v153", "v154", "v155", "v156", "v157", "v158", "v159", "v160", "v161", "v162", "v163", "v164", "v165", "v166", "v167"
typedef unsigned u32x16 __attribute__((ext_vector_type(16)));
__device__ __forceinline__ void g1_stats(ldsp lds, int wm, int m, float (&mean)[2], float (&rstd)[2]) {
#pragma unroll
    for (int i = 0; i < 2; ++i) { const unsigned a = *(const LAS unsigned*)(lds + L_ROWST + (32 * wm + 16 * i + m) * 4); mean[i] = bf_lo(a); rstd[i] = __builtin_amdgcn_rcpf(bf_hi(a)); }
}
template <int WN>
__device__ __forceinline__ void g1_qkv_out(const __amdgpu_buffer_rsrc_t wsr, int wm, int mt, int nt, const f32x4 (&acc)[3][2], const unsigned (&rw)[2], const f32x4 (&cq)[3]) {
    const int lane = lane_id();
    const float mean[2] = {bf_lo(rw[0]), bf_lo(rw[1])}, rstd[2] = {__builtin_amdgcn_rcpf(bf_hi(rw[0])), __builtin_amdgcn_rcpf(bf_hi(rw[1]))};
    const int blk32 = 4 * mt + wm, ht0 = 6 * nt + 3 * WN;
    u32x4 e[3];
#pragma unroll
    for (int t = 0; t < 3; ++t) {
        const f32x4 c = cq[t];
        const float sc = (ht0 + t < 32) ? QSCALE : 1.0f;
        u32x2 d[2];
#pragma unroll
        for (int i = 0; i < 2; ++i) {
            const float rs = rstd[i] * sc; float v[4];
#pragma unroll
            for (int r = 0; r < 4; ++r) v[r] = __builtin_fmaf(-mean[i], c[r], acc[t][i][r]) * rs;
            d[i] = u32x2{pk2(v[0], v[1]), pk2(v[2], v[3])};
        }
        e[t] = widen(d[0], d[1]);
        __builtin_amdgcn_raw_buffer_store_b128(e[t], wsr, WS_Q + blk32 * 1024 + (lane >> 5) * 512 + (lane & 31) * 16 + (ht0 + t) * 65536, 0, 16);
    }
    if (ht0 + 2 >= 32 && ht0 < 64) {
        unsigned kn[3];
#pragma unroll
        for (int t = 0; t < 3; ++t) kn[t] = umax_32lanes(__float_as_uint(sum_x32(dot2(e[t][3], e[t][3], dot2(e[t][2], e[t][2], dot2(e[t][1], e[t][1], dot2(e[t][0], e[t][0], 0.f)))))));
        const int ht = ht0 + lane;
        if (lane < 3 && ht >= 32 && ht < 64) __builtin_amdgcn_raw_buffer_store_b32(lane == 0 ? kn[0] : lane == 1 ? kn[1] : kn[2], wsr, WS_KMAX + ((ht - 32) * 64 + blk32) * 4, 0, 16);
    }
}
struct G1Adr { unsigned a0, a1, bf, cs; };
template <int WN>
__device__ __forceinline__ G1Adr g1_adr(ldsp lds, int wm, int own, int cols0, int ntile) {
    const int lane = lane_id(), m = lane & 15, g = lane >> 4, q = (lane & 15) >> 2, p = lane & 3;
    const int arow = 32 * wm + m, swz = (arow >> 1) & 7;
    const unsigned l0 = (unsigned)(uintptr_t)lds;
    G1Adr r;
    r.a0 = l0 + arow * 128 + ((g ^ swz) << 4); r.a1 = l0 + arow * 128 + (((4 + g) ^ swz) << 4);
    r.bf = l0 + OFF_B + (16 * (g >> 1) + 4 * (g & 1) + q) * 224 + 8 * p + 4 * WN * 32;
    r.cs = l0 + L_COLS + ((own < ntile) ? (cols0 + own) * 64 : 7 * 64) + g * 16;
    return r;
}
#define G1_ACC3(c, a) c(a[0][0]), c(a[0][1]), c(a[1][0]), c(a[1][1]), c(a[2][0]), c(a[2][1])
#define G1_OUT(x) "=&v"(x)
#define G1_IO(x) "+v"(x)
template <int WN>
__device__ __forceinline__ void g1_compute(ldsp lds, char* ws, const char* x, int wm, int mt, int nt) {
    constexpr int NF = 4 - WN, T0 = 4 * WN;
    const __amdgpu_buffer_rsrc_t wsr = __builtin_amdgcn_make_buffer_rsrc(ws, 0, WS_BYTES, 0x00020000);
    const int own = (wm + 2 * WN) & 3;
    const unsigned ctr0 = (unsigned)(uintptr_t)lds + L_MISC + 16;
    f32x4 qa[3][2], cq[3], acc[NF][2];
    unsigned rw[2];
    u32x16 f0, f1, f2;
    g1_rowstat(ws, x, 128 * mt + 8 * nt + 4 * WN + wm, lane_id());
    {
        const G1Adr ad = g1_adr<WN>(lds, wm, own, 3 * WN, 3);
        const int lane = lane_id(), g = lane >> 4;
        const unsigned r = (unsigned)(uintptr_t)lds + OFF_B + (8 * g + ((lane & 15) >> 2)) * 192 + 8 * (lane & 3), b0 = r + (((3 * WN) ^ (g & 1)) << 5), b1 = r + (((3 * WN + 1) ^ (g & 1)) << 5);
        const unsigned rsa = (unsigned)(uintptr_t)lds + L_ROWST + (32 * wm + (lane & 15)) * 4, cla = (unsigned)(uintptr_t)lds + L_COLS + g * 16;
        f32x4 cs = {};
        if constexpr (WN == 0)
            asm volatile(G1_QKV0 : G1_ACC3(G1_OUT, qa), "+v"(cs), "=&{v[72:87]}"(f0), "=&{v[88:103]}"(f1), "=&{v[104:119]}"(f2), "=&v"(rw[0]), "=&v"(rw[1]), "=&v"(cq[0]), "=&v"(cq[1]), "=&v"(cq[2])
                         : "v"(ad.a0), "v"(ad.a1), "v"(b0), "v"(b1), "v"(ad.bf), "v"(ad.cs), "s"(own), "v"(rsa), "v"(cla) : "memory", "scc", G1_CLOB_LO, G1_CLOB_Y);
        else
            asm volatile(G1_QKV1 : G1_ACC3(G1_OUT, qa), "+v"(cs), "=&{v[72:87]}"(f0), "=&{v[88:103]}"(f1), "=&{v[104:119]}"(f2), "=&v"(rw[0]), "=&v"(rw[1]), "=&v"(cq[0]), "=&v"(cq[1]), "=&v"(cq[2])
                         : "v"(ad.a0), "v"(ad.a1), "v"(b0), "v"(b1), "v"(ad.bf), "v"(ad.cs), "s"(own), "v"(rsa), "v"(cla) : "memory", "scc", G1_CLOB_LO, G1_CLOB_Y);
    }
    f32x4 cs = {};
    if constexpr (WN == 0) {
        g1_qkv_out<0>(wsr, wm, mt, nt, qa, rw, cq);
        const G1Adr ad = g1_adr<0>(lds, wm, own, 8, 4);
        asm volatile(G1_FFN0 : G1_ACC3(G1_OUT, acc), "=&v"(acc[NF - 1][0]), "=&v"(acc[NF - 1][1]), "+v"(cs), "+{v[72:87]}"(f0), "+{v[88:103]}"(f1), "+{v[104:119]}"(f2)
                     : "v"(ad.a0), "v"(ad.a1), "v"(ad.bf), "v"(ad.cs), "s"(own), "v"(ctr0) : "memory", "scc", G1_CLOB_LO, G1_CLOB_Y);
    } else {
        u32x16 g0, g1, g2;
        {
            const G1Adr ad = g1_adr<1>(lds, wm, own, 12, 3);
            asm volatile(G1_FFN1A : G1_ACC3(G1_OUT, acc), "+v"(cs), "+{v[72:87]}"(f0), "+{v[88:103]}"(f1), "+{v[104:119]}"(f2), "=&{v[120:135]}"(g0), "=&{v[136:151]}"(g1), "=&{v[152:167]}"(g2)
                         : "v"(ad.a0), "v"(ad.a1), "v"(ad.bf), "v"(ad.cs), "s"(own), "v"(ctr0) : "memory", "scc", G1_CLOB_LO);
        }
        g1_qkv_out<1>(wsr, wm, mt, nt, qa, rw, cq);
        const G1Adr ad = g1_adr<1>(lds, wm, own, 12, 3);
        asm volatile(G1_FFN1B : G1_ACC3(G1_IO, acc), "+v"(cs), "+{v[120:135]}"(g0), "+{v[136:151]}"(g1), "+{v[152:167]}"(g2)
                     : "v"(ad.a0), "v"(ad.a1), "v"(ad.bf), "v"(ad.cs), "s"(own), "v"(ctr0) : "memory", "scc", G1_CLOB_LO, G1_CLOB_X);
    }
    lds_wait(lds, 0, 512);
    __builtin_amdgcn_s_setprio(2);
    const int lane = lane_id(), m = lane & 15, g = lane >> 4;
    float mean[2], rstd[2];
    g1_stats(lds, wm, m, mean, rstd);
    f32x4 c[NF]; u32x2 bias[NF]; u32x4 hv[NF];
#pragma unroll
    for (int t = 0; t < NF; ++t) { c[t] = *(const LAS f32x4*)(lds + L_COLS + (8 + T0 + t) * 64 + g * 16); bias[t] = *(const LAS u32x2*)(lds + L_BIAS + (16 * (T0 + t) + 4 * g) * 2); }
#pragma unroll
    for (int t = 0; t < NF; ++t) asm volatile("" : "+v"(c[t]), "+v"(bias[t]));
#pragma unroll
    for (int t = 0; t < NF; ++t) {
        constexpr float C1 = 2.0f * 0.7978845608028654f * 1.4426950408889634f, C2 = C1 * 0.044715f;
        const f32x2 b01 = {bf_lo(bias[t][0]), bf_hi(bias[t][0])}, b23 = {bf_lo(bias[t][1]), bf_hi(bias[t][1])};
        f32x2 v[4], z[4];
#pragma unroll
        for (int k = 0; k < 4; ++k) { const int i = k >> 1, h = k & 1;
            const f32x2 a2 = {acc[t][i][2 * h], acc[t][i][2 * h + 1]}, c2 = {c[t][2 * h], c[t][2 * h + 1]};
            v[k] = __builtin_elementwise_fma(__builtin_elementwise_fma(f32x2{-mean[i], -mean[i]}, c2, a2), f32x2{rstd[i], rstd[i]}, h ? b23 : b01); }
        GSB();
#pragma unroll
        for (int k = 0; k < 4; ++k) z[k] = v[k] * v[k];
#pragma unroll
        for (int k = 0; k < 4; ++k) z[k] = __builtin_elementwise_fma(z[k], f32x2{-C2, -C2}, f32x2{-C1, -C1});
#pragma unroll
        for (int k = 0; k < 4; ++k) z[k] *= v[k];
        GSB();
#pragma unroll
        for (int k = 0; k < 4; ++k) z[k] = f32x2{__builtin_amdgcn_exp2f(z[k][0]), __builtin_amdgcn_exp2f(z[k][1])};
        GSB();
#pragma unroll
        for (int k = 0; k < 4; ++k) z[k] += f32x2{1.0f, 1.0f};
        GSB();
#pragma unroll
        for (int k = 0; k < 4; ++k) z[k] = f32x2{__builtin_amdgcn_rcpf(z[k][0]), __builtin_amdgcn_rcpf(z[k][1])};
        GSB();
#pragma unroll
        for (int k = 0; k < 4; ++k) v[k] *= z[k];
        hv[t] = widen(u32x2{pk2(v[0][0], v[0][1]), pk2(v[1][0], v[1][1])}, u32x2{pk2(v[2][0], v[2][1]), pk2(v[3][0], v[3][1])});
        asm volatile("" : "+v"(hv[t]));
    }
#pragma unroll
    for (int t = 0; t < NF; ++t)
        __builtin_amdgcn_raw_buffer_store_b128(hv[t], wsr, WS_H + (unsigned)((4 * mt + wm) * 112 + 7 * nt + T0) * 1024 + (lane >> 5) * 512 + (lane & 31) * 16 + t * 1024, 0, 16);
}

#define WAIT_BAR(n) asm volatile("s_waitcnt vmcnt(%0)\n\ts_barrier" ::"n"(n) : "memory")
template <int N, class F>
__device__ __forceinline__ void g1_load_x(F issue) {
#pragma unroll 1
    for (int s = 0; s < NSLOT; ++s) issue(s, s * SLOT);
    WAIT_BAR(4 * N);
    WAIT_BAR(3 * N);
#pragma unroll 1
    for (int s = 5; s < 8; ++s) { issue(s, (s - 5) * SLOT); WAIT_BAR(3 * N); }
    WAIT_BAR(2 * N); WAIT_BAR(N); WAIT_BAR(0);
    WAIT_BAR(0); WAIT_BAR(0);
    issue(13, 3 * SLOT); WAIT_BAR(N); WAIT_BAR(N);
    issue(14, 4 * SLOT); WAIT_BAR(2 * N);
    issue(15, 0); WAIT_BAR(2 * N);
    WAIT_BAR(N); WAIT_BAR(0);
}
template <class F>
__device__ __forceinline__ void g1_load_w(F issue) {
#pragma unroll 1
    for (int s = 0; s < NSLOT; ++s) issue(s, s * SLOT);
    WAIT_BAR(48); WAIT_BAR(36);
    int slot = 0;
#pragma unroll 1
    for (int s = 5; s < 14; ++s) { issue(s, slot); slot = (slot == (NSLOT - 1) * SLOT) ? 0 : slot + SLOT; WAIT_BAR(36); }
    WAIT_BAR(28);
    issue(14, 4 * SLOT); WAIT_BAR(28);
    issue(15, 0); WAIT_BAR(28);
    WAIT_BAR(14); WAIT_BAR(0);
}

__device__ __forceinline__ bf16x16 lo8(const f32x16& s) { return __builtin_bit_cast(bf16x16, __builtin_shufflevector(s, s, 0, 1, 2, 3, 4, 5, 6, 7)); }
__device__ __forceinline__ bf16x16 hi8(const f32x16& s) { return __builtin_bit_cast(bf16x16, __builtin_shufflevector(s, s, 8, 9, 10, 11, 12, 13, 14, 15)); }

#define ATT_ASM \
    "v_mov_b32 v161, %6\n" \
    "v_mov_b32 v162, %7\n" \
    "v_mov_b32 v163, %8\n" \
    "v_mov_b32 v160, 0xdddddddd\n" \
    "v_mov_b32 v84, 0\n" \
    "v_mov_b32 v85, 0\n" \
    "v_mov_b32 v86, 0\n" \
    "v_mov_b32 v87, 0\n" \
    "v_mov_b32 v88, 0\n" \
    "v_mov_b32 v89, 0\n" \
    "v_mov_b32 v90, 0\n" \
    "v_mov_b32 v91, 0\n" \
    "v_mov_b32 v120, 0\n" \
    "v_mov_b32 v121, 0\n" \
    "v_mov_b32 v122, 0\n" \
    "v_mov_b32 v123, 0\n" \
    "s_mov_b32 s40, 0x400000\n" \
    "ATT_C0_%=:\n" \
    "ds_read_b32 v59, %9 offset:16\n" \
    "s_waitcnt lgkmcnt(0)\n" \
    "v_readfirstlane_b32 vcc_lo, v59\n" \
    "s_add_i32 s40, s40, -1\n" \
    "s_cmp_eq_u32 s40, 0\n" \
    "s_cbranch_scc1 ATT_C0X_%=\n" \
    "s_cmp_ge_u32 vcc_lo, 4\n" \
    "s_cbranch_scc1 ATT_C0X_%=\n" \
    "s_sleep 2\n" \
    "s_branch ATT_C0_%=\n" \
    "ATT_C0X_%=:\n" \
    "ds_read_b32 v92, %5\n" \
    "ds_read_b128 v[108:111], %4\n" \
    "ds_read_b128 v[112:115], v161\n" \
    "s_waitcnt lgkmcnt(2)\n" \
    "v_cmp_nle_f32 vcc, 0xc2480000, v92\n" \
    "v_mov_b32 v93, v92\n" \
    "v_mov_b32 v94, v92\n" \
    "v_mov_b32 v95, v92\n" \
    "v_mov_b32 v96, v92\n" \
    "v_mov_b32 v97, v92\n" \
    "v_mov_b32 v98, v92\n" \
    "v_mov_b32 v99, v92\n" \
    "v_mov_b32 v100, v92\n" \
    "v_mov_b32 v101, v92\n" \
    "v_mov_b32 v102, v92\n" \
    "v_mov_b32 v103, v92\n" \
    "v_mov_b32 v104, v92\n" \
    "v_mov_b32 v105, v92\n" \
    "v_mov_b32 v106, v92\n" \
    "v_mov_b32 v107, v92\n" \
    "s_cmp_lg_u64 vcc, 0\n" \
    "s_cselect_b32 %3, 1, 0\n" \
    "s_waitcnt lgkmcnt(0)\n" \
    "s_cbranch_scc1 ATT_END_%=\n" \
    "v_mfma_f32_32x32x16_bf16 v[60:75], v[112:115], v[108:111], v[92:107]\n" \
    "ds_read_b128 v[112:115], v161 offset:1024\n" \
    "s_nop 7\n" \
    "s_nop 3\n" \
    "s_setprio 2\n" \
    "s_waitcnt lgkmcnt(1)\n" \
    "v_exp_f32 v60, v60\n" \
    "v_exp_f32 v61, v61\n" \
    "v_smfmac_f32_32x32x32_bf16 %0, v[120:123], v[84:91], v160\n" \
    "ds_read_b64_tr_b16 v[116:117], v162 offset:0\n" \
    "ds_read_b64_tr_b16 v[118:119], v163 offset:0\n" \
    "v_exp_f32 v62, v62\n" \
    "v_exp_f32 v63, v63\n" \
    "v_exp_f32 v64, v64\n" \
    "v_exp_f32 v65, v65\n" \
    "s_waitcnt lgkmcnt(2)\n" \
    "v_mfma_f32_32x32x16_bf16 v[76:91], v[112:115], v[108:111], v[92:107]\n" \
    "ds_read_b64_tr_b16 v[120:121], v162 offset:544\n" \
    "ds_read_b64_tr_b16 v[122:123], v163 offset:544\n" \
    "v_exp_f32 v66, v66\n" \
    "v_exp_f32 v67, v67\n" \
    "v_exp_f32 v68, v68\n" \
    "v_exp_f32 v69, v69\n" \
    "s_waitcnt lgkmcnt(2)\n" \
    "v_smfmac_f32_32x32x32_bf16 %0, v[116:119], v[60:67], v160\n" \
    "ds_read_b128 v[112:115], v161 offset:2048\n" \
    "v_exp_f32 v70, v70\n" \
    "v_exp_f32 v71, v71\n" \
    "v_exp_f32 v72, v72\n" \
    "v_exp_f32 v73, v73\n" \
    "v_exp_f32 v74, v74\n" \
    "v_exp_f32 v75, v75\n" \
    "s_waitcnt lgkmcnt(1)\n" \
    "v_exp_f32 v76, v76\n" \
    "v_exp_f32 v77, v77\n" \
    "v_smfmac_f32_32x32x32_bf16 %0, v[120:123], v[68:75], v160\n" \
    "ds_read_b64_tr_b16 v[116:117], v162 offset:1088\n" \
    "ds_read_b64_tr_b16 v[118:119], v163 offset:1088\n" \
    "v_exp_f32 v78, v78\n" \
    "v_exp_f32 v79, v79\n" \
    "v_exp_f32 v80, v80\n" \
    "v_exp_f32 v81, v81\n" \
    "s_waitcnt lgkmcnt(2)\n" \
    "v_mfma_f32_32x32x16_bf16 v[60:75], v[112:115], v[108:111], v[92:107]\n" \
    "ds_read_b64_tr_b16 v[120:121], v162 offset:1632\n" \
    "ds_read_b64_tr_b16 v[122:123], v163 offset:1632\n" \
    "v_exp_f32 v82, v82\n" \
    "v_exp_f32 v83, v83\n" \
    "v_exp_f32 v84, v84\n" \
    "v_exp_f32 v85, v85\n" \
    "s_waitcnt lgkmcnt(2)\n" \
    "v_smfmac_f32_32x32x32_bf16 %0, v[116:119], v[76:83], v160\n" \
    "ds_read_b128 v[112:115], v161 offset:3072\n" \
    "v_exp_f32 v86, v86\n" \
    "v_exp_f32 v87, v87\n" \
    "v_exp_f32 v88, v88\n" \
    "v_exp_f32 v89, v89\n" \
    "v_exp_f32 v90, v90\n" \
    "v_exp_f32 v91, v91\n" \
    "v_mov_b32 v59, 1\n" \
    "s_waitcnt vmcnt(0)\n" \
    "ds_add_u32 %9, v59 offset:4\n" \
    "s_waitcnt lgkmcnt(0)\n" \
    "s_waitcnt lgkmcnt(1)\n" \
    "v_exp_f32 v60, v60\n" \
    "v_exp_f32 v61, v61\n" \
    "v_smfmac_f32_32x32x32_bf16 %0, v[120:123], v[84:91], v160\n" \
    "ds_read_b64_tr_b16 v[116:117], v162 offset:2176\n" \
    "ds_read_b64_tr_b16 v[118:119], v163 offset:2176\n" \
    "v_exp_f32 v62, v62\n" \
    "v_exp_f32 v63, v63\n" \
    "v_exp_f32 v64, v64\n" \
    "v_exp_f32 v65, v65\n" \
    "s_waitcnt lgkmcnt(2)\n" \
    "v_mfma_f32_32x32x16_bf16 v[76:91], v[112:115], v[108:111], v[92:107]\n" \
    "ds_read_b64_tr_b16 v[120:121], v162 offset:2720\n" \
    "ds_read_b64_tr_b16 v[122:123], v163 offset:2720\n" \
    "v_exp_f32 v66, v66\n" \
    "v_exp_f32 v67, v67\n" \
    "v_exp_f32 v68, v68\n" \
    "v_exp_f32 v69, v69\n" \
    "s_waitcnt lgkmcnt(2)\n" \
    "v_smfmac_f32_32x32x32_bf16 %0, v[116:119], v[60:67], v160\n" \
    "ds_read_b128 v[112:115], v161 offset:4096\n" \
    "v_exp_f32 v70, v70\n" \
    "v_exp_f32 v71, v71\n" \
    "v_exp_f32 v72, v72\n" \
    "v_exp_f32 v73, v73\n" \
    "v_exp_f32 v74, v74\n" \
    "v_exp_f32 v75, v75\n" \
    "s_setprio 1\n" \
    "s_waitcnt lgkmcnt(1)\n" \
    "v_exp_f32 v76, v76\n" \
    "v_exp_f32 v77, v77\n" \
    "v_smfmac_f32_32x32x32_bf16 %0, v[120:123], v[68:75], v160\n" \
    "ds_read_b64_tr_b16 v[116:117], v162 offset:3264\n" \
    "ds_read_b64_tr_b16 v[118:119], v163 offset:3264\n" \
    "v_exp_f32 v78, v78\n" \
    "v_exp_f32 v79, v79\n" \
    "v_exp_f32 v80, v80\n" \
    "v_exp_f32 v81, v81\n" \
    "s_waitcnt lgkmcnt(2)\n" \
    "v_mfma_f32_32x32x16_bf16 v[60:75], v[112:115], v[108:111], v[92:107]\n" \
    "ds_read_b64_tr_b16 v[120:121], v162 offset:3808\n" \
    "ds_read_b64_tr_b16 v[122:123], v163 offset:3808\n" \
    "v_exp_f32 v82, v82\n" \
    "v_exp_f32 v83, v83\n" \
    "v_exp_f32 v84, v84\n" \
    "v_exp_f32 v85, v85\n" \
    "s_waitcnt lgkmcnt(2)\n" \
    "v_smfmac_f32_32x32x32_bf16 %0, v[116:119], v[76:83], v160\n" \
    "ds_read_b128 v[112:115], v161 offset:5120\n" \
    "v_exp_f32 v86, v86\n" \
    "v_exp_f32 v87, v87\n" \
    "v_exp_f32 v88, v88\n" \
    "v_exp_f32 v89, v89\n" \
    "v_exp_f32 v90, v90\n" \
    "v_exp_f32 v91, v91\n" \
    "s_waitcnt lgkmcnt(1)\n" \
    "v_exp_f32 v60, v60\n" \
    "v_exp_f32 v61, v61\n" \
    "v_smfmac_f32_32x32x32_bf16 %0, v[120:123], v[84:91], v160\n" \
    "ds_read_b64_tr_b16 v[116:117], v162 offset:4352\n" \
    "ds_read_b64_tr_b16 v[118:119], v163 offset:4352\n" \
    "v_exp_f32 v62, v62\n" \
    "v_exp_f32 v63, v63\n" \
    "v_exp_f32 v64, v64\n" \
    "v_exp_f32 v65, v65\n" \
    "s_waitcnt lgkmcnt(2)\n" \
    "v_mfma_f32_32x32x16_bf16 v[76:91], v[112:115], v[108:111], v[92:107]\n" \
    "ds_read_b64_tr_b16 v[120:121], v162 offset:4896\n" \
    "ds_read_b64_tr_b16 v[122:123], v163 offset:4896\n" \
    "v_exp_f32 v66, v66\n" \
    "v_exp_f32 v67, v67\n" \
    "v_exp_f32 v68, v68\n" \
    "v_exp_f32 v69, v69\n" \
    "s_waitcnt lgkmcnt(2)\n" \
    "v_smfmac_f32_32x32x32_bf16 %0, v[116:119], v[60:67], v160\n" \
    "ds_read_b128 v[112:115], v161 offset:6144\n" \
    "v_exp_f32 v70, v70\n" \
    "v_exp_f32 v71, v71\n" \
    "v_exp_f32 v72, v72\n" \
    "v_exp_f32 v73, v73\n" \
    "v_exp_f32 v74, v74\n" \
    "v_exp_f32 v75, v75\n" \
    "s_waitcnt lgkmcnt(1)\n" \
    "v_exp_f32 v76, v76\n" \
    "v_exp_f32 v77, v77\n" \
    "v_smfmac_f32_32x32x32_bf16 %0, v[120:123], v[68:75], v160\n" \
    "ds_read_b64_tr_b16 v[116:117], v162 offset:5440\n" \
    "ds_read_b64_tr_b16 v[118:119], v163 offset:5440\n" \
    "v_exp_f32 v78, v78\n" \
    "v_exp_f32 v79, v79\n" \
    "v_exp_f32 v80, v80\n" \
    "v_exp_f32 v81, v81\n" \
    "s_waitcnt lgkmcnt(2)\n" \
    "v_mfma_f32_32x32x16_bf16 v[60:75], v[112:115], v[108:111], v[92:107]\n" \
    "ds_read_b64_tr_b16 v[120:121], v162 offset:5984\n" \
    "ds_read_b64_tr_b16 v[122:123], v163 offset:5984\n" \
    "v_exp_f32 v82, v82\n" \
    "v_exp_f32 v83, v83\n" \
    "v_exp_f32 v84, v84\n" \
    "v_exp_f32 v85, v85\n" \
    "s_waitcnt lgkmcnt(2)\n" \
    "v_smfmac_f32_32x32x32_bf16 %0, v[116:119], v[76:83], v160\n" \
    "ds_read_b128 v[112:115], v161 offset:7168\n" \
    "v_exp_f32 v86, v86\n" \
    "v_exp_f32 v87, v87\n" \
    "v_exp_f32 v88, v88\n" \
    "v_exp_f32 v89, v89\n" \
    "v_exp_f32 v90, v90\n" \
    "v_exp_f32 v91, v91\n" \
    "s_waitcnt lgkmcnt(1)\n" \
    "v_exp_f32 v60, v60\n" \
    "v_exp_f32 v61, v61\n" \
    "v_smfmac_f32_32x32x32_bf16 %0, v[120:123], v[84:91], v160\n" \
    "ds_read_b64_tr_b16 v[116:117], v162 offset:6528\n" \
    "ds_read_b64_tr_b16 v[118:119], v163 offset:6528\n" \
    "v_exp_f32 v62, v62\n" \
    "v_exp_f32 v63, v63\n" \
    "v_exp_f32 v64, v64\n" \
    "v_exp_f32 v65, v65\n" \
    "s_waitcnt lgkmcnt(2)\n" \
    "v_mfma_f32_32x32x16_bf16 v[76:91], v[112:115], v[108:111], v[92:107]\n" \
    "ds_read_b64_tr_b16 v[120:121], v162 offset:7072\n" \
    "ds_read_b64_tr_b16 v[122:123], v163 offset:7072\n" \
    "v_exp_f32 v66, v66\n" \
    "v_exp_f32 v67, v67\n" \
    "v_exp_f32 v68, v68\n" \
    "v_exp_f32 v69, v69\n" \
    "s_waitcnt lgkmcnt(2)\n" \
    "v_smfmac_f32_32x32x32_bf16 %0, v[116:119], v[60:67], v160\n" \
    "ds_read_b128 v[112:115], v161 offset:8192\n" \
    "v_exp_f32 v70, v70\n" \
    "v_exp_f32 v71, v71\n" \
    "v_exp_f32 v72, v72\n" \
    "v_exp_f32 v73, v73\n" \
    "v_exp_f32 v74, v74\n" \
    "v_exp_f32 v75, v75\n" \
    "s_waitcnt lgkmcnt(1)\n" \
    "v_exp_f32 v76, v76\n" \
    "v_exp_f32 v77, v77\n" \
    "v_smfmac_f32_32x32x32_bf16 %0, v[120:123], v[68:75], v160\n" \
    "ds_read_b64_tr_b16 v[116:117], v162 offset:7616\n" \
    "ds_read_b64_tr_b16 v[118:119], v163 offset:7616\n" \
    "v_exp_f32 v78, v78\n" \
    "v_exp_f32 v79, v79\n" \
    "v_exp_f32 v80, v80\n" \
    "v_exp_f32 v81, v81\n" \
    "s_waitcnt lgkmcnt(2)\n" \
    "v_mfma_f32_32x32x16_bf16 v[60:75], v[112:115], v[108:111], v[92:107]\n" \
    "ds_read_b64_tr_b16 v[120:121], v162 offset:8160\n" \
    "ds_read_b64_tr_b16 v[122:123], v163 offset:8160\n" \
    "v_exp_f32 v82, v82\n" \
    "v_exp_f32 v83, v83\n" \
    "v_exp_f32 v84, v84\n" \
    "v_exp_f32 v85, v85\n" \
    "s_waitcnt lgkmcnt(2)\n" \
    "v_smfmac_f32_32x32x32_bf16 %0, v[116:119], v[76:83], v160\n" \
    "ds_read_b128 v[112:115], v161 offset:9216\n" \
    "v_exp_f32 v86, v86\n" \
    "v_exp_f32 v87, v87\n" \
    "v_exp_f32 v88, v88\n" \
    "v_exp_f32 v89, v89\n" \
    "v_exp_f32 v90, v90\n" \
    "v_exp_f32 v91, v91\n" \
    "s_waitcnt lgkmcnt(1)\n" \
    "v_exp_f32 v60, v60\n" \
    "v_exp_f32 v61, v61\n" \
    "v_smfmac_f32_32x32x32_bf16 %0, v[120:123], v[84:91], v160\n" \
    "ds_read_b64_tr_b16 v[116:117], v162 offset:8704\n" \
    "ds_read_b64_tr_b16 v[118:119], v163 offset:8704\n" \
    "v_exp_f32 v62, v62\n" \
    "v_exp_f32 v63, v63\n" \
    "v_exp_f32 v64, v64\n" \
    "v_exp_f32 v65, v65\n" \
    "s_waitcnt lgkmcnt(2)\n" \
    "v_mfma_f32_32x32x16_bf16 v[76:91], v[112:115], v[108:111], v[92:107]\n" \
    "ds_read_b64_tr_b16 v[120:121], v162 offset:9248\n" \
    "ds_read_b64_tr_b16 v[122:123], v163 offset:9248\n" \
    "v_exp_f32 v66, v66\n" \
    "v_exp_f32 v67, v67\n" \
    "v_exp_f32 v68, v68\n" \
    "v_exp_f32 v69, v69\n" \
    "s_waitcnt lgkmcnt(2)\n" \
    "v_smfmac_f32_32x32x32_bf16 %0, v[116:119], v[60:67], v160\n" \
    "ds_read_b128 v[112:115], v161 offset:10240\n" \
    "v_exp_f32 v70, v70\n" \
    "v_exp_f32 v71, v71\n" \
    "v_exp_f32 v72, v72\n" \
    "v_exp_f32 v73, v73\n" \
    "v_exp_f32 v74, v74\n" \
    "v_exp_f32 v75, v75\n" \
    "s_waitcnt lgkmcnt(1)\n" \
    "v_exp_f32 v76, v76\n" \
    "v_exp_f32 v77, v77\n" \
    "v_smfmac_f32_32x32x32_bf16 %0, v[120:123], v[68:75], v160\n" \
    "ds_read_b64_tr_b16 v[116:117], v162 offset:9792\n" \
    "ds_read_b64_tr_b16 v[118:119], v163 offset:9792\n" \
    "v_exp_f32 v78, v78\n" \
    "v_exp_f32 v79, v79\n" \
    "v_exp_f32 v80, v80\n" \
    "v_exp_f32 v81, v81\n" \
    "s_waitcnt lgkmcnt(2)\n" \
    "v_mfma_f32_32x32x16_bf16 v[60:75], v[112:115], v[108:111], v[92:107]\n" \
    "ds_read_b64_tr_b16 v[120:121], v162 offset:10336\n" \
    "ds_read_b64_tr_b16 v[122:123], v163 offset:10336\n" \
    "v_exp_f32 v82, v82\n" \
    "v_exp_f32 v83, v83\n" \
    "v_exp_f32 v84, v84\n" \
    "v_exp_f32 v85, v85\n" \
    "s_waitcnt lgkmcnt(2)\n" \
    "v_smfmac_f32_32x32x32_bf16 %0, v[116:119], v[76:83], v160\n" \
    "ds_read_b128 v[112:115], v161 offset:11264\n" \
    "v_exp_f32 v86, v86\n" \
    "v_exp_f32 v87, v87\n" \
    "v_exp_f32 v88, v88\n" \
    "v_exp_f32 v89, v89\n" \
    "v_exp_f32 v90, v90\n" \
    "v_exp_f32 v91, v91\n" \
    "s_waitcnt lgkmcnt(1)\n" \
    "v_exp_f32 v60, v60\n" \
    "v_exp_f32 v61, v61\n" \
    "v_smfmac_f32_32x32x32_bf16 %0, v[120:123], v[84:91], v160\n" \
    "ds_read_b64_tr_b16 v[116:117], v162 offset:10880\n" \
    "ds_read_b64_tr_b16 v[118:119], v163 offset:10880\n" \
    "v_exp_f32 v62, v62\n" \
    "v_exp_f32 v63, v63\n" \
    "v_exp_f32 v64, v64\n" \
    "v_exp_f32 v65, v65\n" \
    "s_waitcnt lgkmcnt(2)\n" \
    "v_mfma_f32_32x32x16_bf16 v[76:91], v[112:115], v[108:111], v[92:107]\n" \
    "ds_read_b64_tr_b16 v[120:121], v162 offset:11424\n" \
    "ds_read_b64_tr_b16 v[122:123], v163 offset:11424\n" \
    "v_exp_f32 v66, v66\n" \
    "v_exp_f32 v67, v67\n" \
    "v_exp_f32 v68, v68\n" \
    "v_exp_f32 v69, v69\n" \
    "s_waitcnt lgkmcnt(2)\n" \
    "v_smfmac_f32_32x32x32_bf16 %0, v[116:119], v[60:67], v160\n" \
    "ds_read_b128 v[112:115], v161 offset:12288\n" \
    "v_exp_f32 v70, v70\n" \
    "v_exp_f32 v71, v71\n" \
    "v_exp_f32 v72, v72\n" \
    "v_exp_f32 v73, v73\n" \
    "v_exp_f32 v74, v74\n" \
    "v_exp_f32 v75, v75\n" \
    "s_waitcnt lgkmcnt(1)\n" \
    "v_exp_f32 v76, v76\n" \
    "v_exp_f32 v77, v77\n" \
    "v_smfmac_f32_32x32x32_bf16 %0, v[120:123], v[68:75], v160\n" \
    "ds_read_b64_tr_b16 v[116:117], v162 offset:11968\n" \
    "ds_read_b64_tr_b16 v[118:119], v163 offset:11968\n" \
    "v_exp_f32 v78, v78\n" \
    "v_exp_f32 v79, v79\n" \
    "v_exp_f32 v80, v80\n" \
    "v_exp_f32 v81, v81\n" \
    "s_waitcnt lgkmcnt(2)\n" \
    "v_mfma_f32_32x32x16_bf16 v[60:75], v[112:115], v[108:111], v[92:107]\n" \
    "ds_read_b64_tr_b16 v[120:121], v162 offset:12512\n" \
    "ds_read_b64_tr_b16 v[122:123], v163 offset:12512\n" \
    "v_exp_f32 v82, v82\n" \
    "v_exp_f32 v83, v83\n" \
    "v_exp_f32 v84, v84\n" \
    "v_exp_f32 v85, v85\n" \
    "s_waitcnt lgkmcnt(2)\n" \
    "v_smfmac_f32_32x32x32_bf16 %0, v[116:119], v[76:83], v160\n" \
    "ds_read_b128 v[112:115], v161 offset:13312\n" \
    "v_exp_f32 v86, v86\n" \
    "v_exp_f32 v87, v87\n" \
    "v_exp_f32 v88, v88\n" \
    "v_exp_f32 v89, v89\n" \
    "v_exp_f32 v90, v90\n" \
    "v_exp_f32 v91, v91\n" \
    "s_mov_b32 s40, 0x400000\n" \
    "ATT_KV_%=:\n" \
    "ds_read_b32 v59, %9 offset:0\n" \
    "s_waitcnt lgkmcnt(0)\n" \
    "v_readfirstlane_b32 vcc_lo, v59\n" \
    "s_add_i32 s40, s40, -1\n" \
    "s_cmp_eq_u32 s40, 0\n" \
    "s_cbranch_scc1 ATT_KVX_%=\n" \
    "s_cmp_ge_u32 vcc_lo, 4\n" \
    "s_cbranch_scc1 ATT_KVX_%=\n" \
    "s_sleep 2\n" \
    "s_branch ATT_KV_%=\n" \
    "ATT_KVX_%=:\n" \
    "s_waitcnt lgkmcnt(1)\n" \
    "v_exp_f32 v60, v60\n" \
    "v_exp_f32 v61, v61\n" \
    "v_smfmac_f32_32x32x32_bf16 %0, v[120:123], v[84:91], v160\n" \
    "ds_read_b64_tr_b16 v[116:117], v162 offset:13056\n" \
    "ds_read_b64_tr_b16 v[118:119], v163 offset:13056\n" \
    "v_exp_f32 v62, v62\n" \
    "v_exp_f32 v63, v63\n" \
    "v_exp_f32 v64, v64\n" \
    "v_exp_f32 v65, v65\n" \
    "s_waitcnt lgkmcnt(2)\n" \
    "v_mfma_f32_32x32x16_bf16 v[76:91], v[112:115], v[108:111], v[92:107]\n" \
    "ds_read_b64_tr_b16 v[120:121], v162 offset:13600\n" \
    "ds_read_b64_tr_b16 v[122:123], v163 offset:13600\n" \
    "v_exp_f32 v66, v66\n" \
    "v_exp_f32 v67, v67\n" \
    "v_exp_f32 v68, v68\n" \
    "v_exp_f32 v69, v69\n" \
    "s_waitcnt lgkmcnt(2)\n" \
    "v_smfmac_f32_32x32x32_bf16 %0, v[116:119], v[60:67], v160\n" \
    "ds_read_b128 v[112:115], v161 offset:14336\n" \
    "v_exp_f32 v70, v70\n" \
    "v_exp_f32 v71, v71\n" \
    "v_exp_f32 v72, v72\n" \
    "v_exp_f32 v73, v73\n" \
    "v_exp_f32 v74, v74\n" \
    "v_exp_f32 v75, v75\n" \
    "s_waitcnt lgkmcnt(1)\n" \
    "v_exp_f32 v76, v76\n" \
    "v_exp_f32 v77, v77\n" \
    "v_smfmac_f32_32x32x32_bf16 %0, v[120:123], v[68:75], v160\n" \
    "ds_read_b64_tr_b16 v[116:117], v162 offset:14144\n" \
    "ds_read_b64_tr_b16 v[118:119], v163 offset:14144\n" \
    "v_exp_f32 v78, v78\n" \
    "v_exp_f32 v79, v79\n" \
    "v_exp_f32 v80, v80\n" \
    "v_exp_f32 v81, v81\n" \
    "s_waitcnt lgkmcnt(2)\n" \
    "v_mfma_f32_32x32x16_bf16 v[60:75], v[112:115], v[108:111], v[92:107]\n" \
    "ds_read_b64_tr_b16 v[120:121], v162 offset:14688\n" \
    "ds_read_b64_tr_b16 v[122:123], v163 offset:14688\n" \
    "v_exp_f32 v82, v82\n" \
    "v_exp_f32 v83, v83\n" \
    "v_exp_f32 v84, v84\n" \
    "v_exp_f32 v85, v85\n" \
    "s_waitcnt lgkmcnt(2)\n" \
    "v_smfmac_f32_32x32x32_bf16 %0, v[116:119], v[76:83], v160\n" \
    "ds_read_b128 v[112:115], v161 offset:15360\n" \
    "v_exp_f32 v86, v86\n" \
    "v_exp_f32 v87, v87\n" \
    "v_exp_f32 v88, v88\n" \
    "v_exp_f32 v89, v89\n" \
    "v_exp_f32 v90, v90\n" \
    "v_exp_f32 v91, v91\n" \
    "s_waitcnt lgkmcnt(1)\n" \
    "v_exp_f32 v60, v60\n" \
    "v_exp_f32 v61, v61\n" \
    "v_smfmac_f32_32x32x32_bf16 %0, v[120:123], v[84:91], v160\n" \
    "ds_read_b64_tr_b16 v[116:117], v162 offset:15232\n" \
    "ds_read_b64_tr_b16 v[118:119], v163 offset:15232\n" \
    "v_exp_f32 v62, v62\n" \
    "v_exp_f32 v63, v63\n" \
    "v_exp_f32 v64, v64\n" \
    "v_exp_f32 v65, v65\n" \
    "s_waitcnt lgkmcnt(2)\n" \
    "v_mfma_f32_32x32x16_bf16 v[76:91], v[112:115], v[108:111], v[92:107]\n" \
    "ds_read_b64_tr_b16 v[120:121], v162 offset:15776\n" \
    "ds_read_b64_tr_b16 v[122:123], v163 offset:15776\n" \
    "v_exp_f32 v66, v66\n" \
    "v_exp_f32 v67, v67\n" \
    "v_exp_f32 v68, v68\n" \
    "v_exp_f32 v69, v69\n" \
    "s_waitcnt lgkmcnt(2)\n" \
    "v_smfmac_f32_32x32x32_bf16 %0, v[116:119], v[60:67], v160\n" \
    "ds_read_b128 v[112:115], v161 offset:16384\n" \
    "v_exp_f32 v70, v70\n" \
    "v_exp_f32 v71, v71\n" \
    "v_exp_f32 v72, v72\n" \
    "v_exp_f32 v73, v73\n" \
    "v_exp_f32 v74, v74\n" \
    "v_exp_f32 v75, v75\n" \
    "s_waitcnt lgkmcnt(1)\n" \
    "v_exp_f32 v76, v76\n" \
    "v_exp_f32 v77, v77\n" \
    "v_smfmac_f32_32x32x32_bf16 %0, v[120:123], v[68:75], v160\n" \
    "ds_read_b64_tr_b16 v[116:117], v162 offset:16320\n" \
    "ds_read_b64_tr_b16 v[118:119], v163 offset:16320\n" \
    "v_exp_f32 v78, v78\n" \
    "v_exp_f32 v79, v79\n" \
    "v_exp_f32 v80, v80\n" \
    "v_exp_f32 v81, v81\n" \
    "s_waitcnt lgkmcnt(2)\n" \
    "v_mfma_f32_32x32x16_bf16 v[60:75], v[112:115], v[108:111], v[92:107]\n" \
    "ds_read_b64_tr_b16 v[120:121], v162 offset:16864\n" \
    "ds_read_b64_tr_b16 v[122:123], v163 offset:16864\n" \
    "v_exp_f32 v82, v82\n" \
    "v_exp_f32 v83, v83\n" \
    "v_exp_f32 v84, v84\n" \
    "v_exp_f32 v85, v85\n" \
    "s_waitcnt lgkmcnt(2)\n" \
    "v_smfmac_f32_32x32x32_bf16 %0, v[116:119], v[76:83], v160\n" \
    "ds_read_b128 v[112:115], v161 offset:17408\n" \
    "v_exp_f32 v86, v86\n" \
    "v_exp_f32 v87, v87\n" \
    "v_exp_f32 v88, v88\n" \
    "v_exp_f32 v89, v89\n" \
    "v_exp_f32 v90, v90\n" \
    "v_exp_f32 v91, v91\n" \
    "s_setprio 0\n" \
    "s_waitcnt lgkmcnt(1)\n" \
    "v_exp_f32 v60, v60\n" \
    "v_exp_f32 v61, v61\n" \
    "v_smfmac_f32_32x32x32_bf16 %0, v[120:123], v[84:91], v160\n" \
    "ds_read_b64_tr_b16 v[116:117], v162 offset:17408\n" \
    "ds_read_b64_tr_b16 v[118:119], v163 offset:17408\n" \
    "v_exp_f32 v62, v62\n" \
    "v_exp_f32 v63, v63\n" \
    "v_exp_f32 v64, v64\n" \
    "v_exp_f32 v65, v65\n" \
    "s_waitcnt lgkmcnt(2)\n" \
    "v_mfma_f32_32x32x16_bf16 v[76:91], v[112:115], v[108:111], v[92:107]\n" \
    "ds_read_b64_tr_b16 v[120:121], v162 offset:17952\n" \
    "ds_read_b64_tr_b16 v[122:123], v163 offset:17952\n" \
    "v_exp_f32 v66, v66\n" \
    "v_exp_f32 v67, v67\n" \
    "v_exp_f32 v68, v68\n" \
    "v_exp_f32 v69, v69\n" \
    "s_waitcnt lgkmcnt(2)\n" \
    "v_smfmac_f32_32x32x32_bf16 %0, v[116:119], v[60:67], v160\n" \
    "ds_read_b128 v[112:115], v161 offset:18432\n" \
    "v_exp_f32 v70, v70\n" \
    "v_exp_f32 v71, v71\n" \
    "v_exp_f32 v72, v72\n" \
    "v_exp_f32 v73, v73\n" \
    "v_exp_f32 v74, v74\n" \
    "v_exp_f32 v75, v75\n" \
    "s_waitcnt lgkmcnt(1)\n" \
    "v_exp_f32 v76, v76\n" \
    "v_exp_f32 v77, v77\n" \
    "v_smfmac_f32_32x32x32_bf16 %0, v[120:123], v[68:75], v160\n" \
    "ds_read_b64_tr_b16 v[116:117], v162 offset:18496\n" \
    "ds_read_b64_tr_b16 v[118:119], v163 offset:18496\n" \
    "v_exp_f32 v78, v78\n" \
    "v_exp_f32 v79, v79\n" \
    "v_exp_f32 v80, v80\n" \
    "v_exp_f32 v81, v81\n" \
    "s_waitcnt lgkmcnt(2)\n" \
    "v_mfma_f32_32x32x16_bf16 v[60:75], v[112:115], v[108:111], v[92:107]\n" \
    "ds_read_b64_tr_b16 v[120:121], v162 offset:19040\n" \
    "ds_read_b64_tr_b16 v[122:123], v163 offset:19040\n" \
    "v_exp_f32 v82, v82\n" \
    "v_exp_f32 v83, v83\n" \
    "v_exp_f32 v84, v84\n" \
    "v_exp_f32 v85, v85\n" \
    "s_waitcnt lgkmcnt(2)\n" \
    "v_smfmac_f32_32x32x32_bf16 %0, v[116:119], v[76:83], v160\n" \
    "ds_read_b128 v[112:115], v161 offset:19456\n" \
    "v_exp_f32 v86, v86\n" \
    "v_exp_f32 v87, v87\n" \
    "v_exp_f32 v88, v88\n" \
    "v_exp_f32 v89, v89\n" \
    "v_exp_f32 v90, v90\n" \
    "v_exp_f32 v91, v91\n" \
    "s_waitcnt lgkmcnt(1)\n" \
    "v_exp_f32 v60, v60\n" \
    "v_exp_f32 v61, v61\n" \
    "v_smfmac_f32_32x32x32_bf16 %0, v[120:123], v[84:91], v160\n" \
    "ds_read_b64_tr_b16 v[116:117], v162 offset:19584\n" \
    "ds_read_b64_tr_b16 v[118:119], v163 offset:19584\n" \
    "v_exp_f32 v62, v62\n" \
    "v_exp_f32 v63, v63\n" \
    "v_exp_f32 v64, v64\n" \
    "v_exp_f32 v65, v65\n" \
    "s_waitcnt lgkmcnt(2)\n" \
    "v_mfma_f32_32x32x16_bf16 v[76:91], v[112:115], v[108:111], v[92:107]\n" \
    "ds_read_b64_tr_b16 v[120:121], v162 offset:20128\n" \
    "ds_read_b64_tr_b16 v[122:123], v163 offset:20128\n" \
    "v_exp_f32 v66, v66\n" \
    "v_exp_f32 v67, v67\n" \
    "v_exp_f32 v68, v68\n" \
    "v_exp_f32 v69, v69\n" \
    "s_waitcnt lgkmcnt(2)\n" \
    "v_smfmac_f32_32x32x32_bf16 %0, v[116:119], v[60:67], v160\n" \
    "ds_read_b128 v[112:115], v161 offset:20480\n" \
    "v_exp_f32 v70, v70\n" \
    "v_exp_f32 v71, v71\n" \
    "v_exp_f32 v72, v72\n" \
    "v_exp_f32 v73, v73\n" \
    "v_exp_f32 v74, v74\n" \
    "v_exp_f32 v75, v75\n" \
    "s_waitcnt lgkmcnt(1)\n" \
    "v_exp_f32 v76, v76\n" \
    "v_exp_f32 v77, v77\n" \
    "v_smfmac_f32_32x32x32_bf16 %0, v[120:123], v[68:75], v160\n" \
    "ds_read_b64_tr_b16 v[116:117], v162 offset:20672\n" \
    "ds_read_b64_tr_b16 v[118:119], v163 offset:20672\n" \
    "v_exp_f32 v78, v78\n" \
    "v_exp_f32 v79, v79\n" \
    "v_exp_f32 v80, v80\n" \
    "v_exp_f32 v81, v81\n" \
    "s_waitcnt lgkmcnt(2)\n" \
    "v_mfma_f32_32x32x16_bf16 v[60:75], v[112:115], v[108:111], v[92:107]\n" \
    "ds_read_b64_tr_b16 v[120:121], v162 offset:21216\n" \
    "ds_read_b64_tr_b16 v[122:123], v163 offset:21216\n" \
    "v_exp_f32 v82, v82\n" \
    "v_exp_f32 v83, v83\n" \
    "v_exp_f32 v84, v84\n" \
    "v_exp_f32 v85, v85\n" \
    "s_waitcnt lgkmcnt(2)\n" \
    "v_smfmac_f32_32x32x32_bf16 %0, v[116:119], v[76:83], v160\n" \
    "ds_read_b128 v[112:115], v161 offset:21504\n" \
    "v_exp_f32 v86, v86\n" \
    "v_exp_f32 v87, v87\n" \
    "v_exp_f32 v88, v88\n" \
    "v_exp_f32 v89, v89\n" \
    "v_exp_f32 v90, v90\n" \
    "v_exp_f32 v91, v91\n" \
    "s_waitcnt lgkmcnt(1)\n" \
    "v_exp_f32 v60, v60\n" \
    "v_exp_f32 v61, v61\n" \
    "v_smfmac_f32_32x32x32_bf16 %0, v[120:123], v[84:91], v160\n" \
    "ds_read_b64_tr_b16 v[116:117], v162 offset:21760\n" \
    "ds_read_b64_tr_b16 v[118:119], v163 offset:21760\n" \
    "v_exp_f32 v62, v62\n" \
    "v_exp_f32 v63, v63\n" \
    "v_exp_f32 v64, v64\n" \
    "v_exp_f32 v65, v65\n" \
    "s_waitcnt lgkmcnt(2)\n" \
    "v_mfma_f32_32x32x16_bf16 v[76:91], v[112:115], v[108:111], v[92:107]\n" \
    "ds_read_b64_tr_b16 v[120:121], v162 offset:22304\n" \
    "ds_read_b64_tr_b16 v[122:123], v163 offset:22304\n" \
    "v_exp_f32 v66, v66\n" \
    "v_exp_f32 v67, v67\n" \
    "v_exp_f32 v68, v68\n" \
    "v_exp_f32 v69, v69\n" \
    "s_waitcnt lgkmcnt(2)\n" \
    "v_smfmac_f32_32x32x32_bf16 %0, v[116:119], v[60:67], v160\n" \
    "ds_read_b128 v[112:115], v161 offset:22528\n" \
    "v_exp_f32 v70, v70\n" \
    "v_exp_f32 v71, v71\n" \
    "v_exp_f32 v72, v72\n" \
    "v_exp_f32 v73, v73\n" \
    "v_exp_f32 v74, v74\n" \
    "v_exp_f32 v75, v75\n" \
    "s_waitcnt lgkmcnt(1)\n" \
    "v_exp_f32 v76, v76\n" \
    "v_exp_f32 v77, v77\n" \
    "v_smfmac_f32_32x32x32_bf16 %0, v[120:123], v[68:75], v160\n" \
    "ds_read_b64_tr_b16 v[116:117], v162 offset:22848\n" \
    "ds_read_b64_tr_b16 v[118:119], v163 offset:22848\n" \
    "v_exp_f32 v78, v78\n" \
    "v_exp_f32 v79, v79\n" \
    "v_exp_f32 v80, v80\n" \
    "v_exp_f32 v81, v81\n" \
    "s_waitcnt lgkmcnt(2)\n" \
    "v_mfma_f32_32x32x16_bf16 v[60:75], v[112:115], v[108:111], v[92:107]\n" \
    "ds_read_b64_tr_b16 v[120:121], v162 offset:23392\n" \
    "ds_read_b64_tr_b16 v[122:123], v163 offset:23392\n" \
    "v_exp_f32 v82, v82\n" \
    "v_exp_f32 v83, v83\n" \
    "v_exp_f32 v84, v84\n" \
    "v_exp_f32 v85, v85\n" \
    "s_waitcnt lgkmcnt(2)\n" \
    "v_smfmac_f32_32x32x32_bf16 %0, v[116:119], v[76:83], v160\n" \
    "ds_read_b128 v[112:115], v161 offset:23552\n" \
    "v_exp_f32 v86, v86\n" \
    "v_exp_f32 v87, v87\n" \
    "v_exp_f32 v88, v88\n" \
    "v_exp_f32 v89, v89\n" \
    "v_exp_f32 v90, v90\n" \
    "v_exp_f32 v91, v91\n" \
    "s_waitcnt lgkmcnt(1)\n" \
    "v_exp_f32 v60, v60\n" \
    "v_exp_f32 v61, v61\n" \
    "v_smfmac_f32_32x32x32_bf16 %0, v[120:123], v[84:91], v160\n" \
    "ds_read_b64_tr_b16 v[116:117], v162 offset:23936\n" \
    "ds_read_b64_tr_b16 v[118:119], v163 offset:23936\n" \
    "v_exp_f32 v62, v62\n" \
    "v_exp_f32 v63, v63\n" \
    "v_exp_f32 v64, v64\n" \
    "v_exp_f32 v65, v65\n" \
    "s_waitcnt lgkmcnt(2)\n" \
    "v_mfma_f32_32x32x16_bf16 v[76:91], v[112:115], v[108:111], v[92:107]\n" \
    "ds_read_b64_tr_b16 v[120:121], v162 offset:24480\n" \
    "ds_read_b64_tr_b16 v[122:123], v163 offset:24480\n" \
    "v_exp_f32 v66, v66\n" \
    "v_exp_f32 v67, v67\n" \
    "v_exp_f32 v68, v68\n" \
    "v_exp_f32 v69, v69\n" \
    "s_waitcnt lgkmcnt(2)\n" \
    "v_smfmac_f32_32x32x32_bf16 %0, v[116:119], v[60:67], v160\n" \
    "ds_read_b128 v[112:115], v161 offset:24576\n" \
    "v_exp_f32 v70, v70\n" \
    "v_exp_f32 v71, v71\n" \
    "v_exp_f32 v72, v72\n" \
    "v_exp_f32 v73, v73\n" \
    "v_exp_f32 v74, v74\n" \
    "v_exp_f32 v75, v75\n" \
    "s_waitcnt lgkmcnt(1)\n" \
    "v_exp_f32 v76, v76\n" \
    "v_exp_f32 v77, v77\n" \
    "v_smfmac_f32_32x32x32_bf16 %0, v[120:123], v[68:75], v160\n" \
    "ds_read_b64_tr_b16 v[116:117], v162 offset:25024\n" \
    "ds_read_b64_tr_b16 v[118:119], v163 offset:25024\n" \
    "v_exp_f32 v78, v78\n" \
    "v_exp_f32 v79, v79\n" \
    "v_exp_f32 v80, v80\n" \
    "v_exp_f32 v81, v81\n" \
    "s_waitcnt lgkmcnt(2)\n" \
    "v_mfma_f32_32x32x16_bf16 v[60:75], v[112:115], v[108:111], v[92:107]\n" \
    "ds_read_b64_tr_b16 v[120:121], v162 offset:25568\n" \
    "ds_read_b64_tr_b16 v[122:123], v163 offset:25568\n" \
    "v_exp_f32 v82, v82\n" \
    "v_exp_f32 v83, v83\n" \
    "v_exp_f32 v84, v84\n" \
    "v_exp_f32 v85, v85\n" \
    "s_waitcnt lgkmcnt(2)\n" \
    "v_smfmac_f32_32x32x32_bf16 %0, v[116:119], v[76:83], v160\n" \
    "ds_read_b128 v[112:115], v161 offset:25600\n" \
    "v_exp_f32 v86, v86\n" \
    "v_exp_f32 v87, v87\n" \
    "v_exp_f32 v88, v88\n" \
    "v_exp_f32 v89, v89\n" \
    "v_exp_f32 v90, v90\n" \
    "v_exp_f32 v91, v91\n" \
    "s_waitcnt lgkmcnt(1)\n" \
    "v_exp_f32 v60, v60\n" \
    "v_exp_f32 v61, v61\n" \
    "v_smfmac_f32_32x32x32_bf16 %0, v[120:123], v[84:91], v160\n" \
    "ds_read_b64_tr_b16 v[116:117], v162 offset:26112\n" \
    "ds_read_b64_tr_b16 v[118:119], v163 offset:26112\n" \
    "v_exp_f32 v62, v62\n" \
    "v_exp_f32 v63, v63\n" \
    "v_exp_f32 v64, v64\n" \
    "v_exp_f32 v65, v65\n" \
    "s_waitcnt lgkmcnt(2)\n" \
    "v_mfma_f32_32x32x16_bf16 v[76:91], v[112:115], v[108:111], v[92:107]\n" \
    "ds_read_b64_tr_b16 v[120:121], v162 offset:26656\n" \
    "ds_read_b64_tr_b16 v[122:123], v163 offset:26656\n" \
    "v_exp_f32 v66, v66\n" \
    "v_exp_f32 v67, v67\n" \
    "v_exp_f32 v68, v68\n" \
    "v_exp_f32 v69, v69\n" \
    "s_waitcnt lgkmcnt(2)\n" \
    "v_smfmac_f32_32x32x32_bf16 %0, v[116:119], v[60:67], v160\n" \
    "ds_read_b128 v[112:115], v161 offset:26624\n" \
    "v_exp_f32 v70, v70\n" \
    "v_exp_f32 v71, v71\n" \
    "v_exp_f32 v72, v72\n" \
    "v_exp_f32 v73, v73\n" \
    "v_exp_f32 v74, v74\n" \
    "v_exp_f32 v75, v75\n" \
    "s_waitcnt lgkmcnt(1)\n" \
    "v_exp_f32 v76, v76\n" \
    "v_exp_f32 v77, v77\n" \
    "v_smfmac_f32_32x32x32_bf16 %0, v[120:123], v[68:75], v160\n" \
    "ds_read_b64_tr_b16 v[116:117], v162 offset:27200\n" \
    "ds_read_b64_tr_b16 v[118:119], v163 offset:27200\n" \
    "v_exp_f32 v78, v78\n" \
    "v_exp_f32 v79, v79\n" \
    "v_exp_f32 v80, v80\n" \
    "v_exp_f32 v81, v81\n" \
    "s_waitcnt lgkmcnt(2)\n" \
    "v_mfma_f32_32x32x16_bf16 v[60:75], v[112:115], v[108:111], v[92:107]\n" \
    "ds_read_b64_tr_b16 v[120:121], v162 offset:27744\n" \
    "ds_read_b64_tr_b16 v[122:123], v163 offset:27744\n" \
    "v_exp_f32 v82, v82\n" \
    "v_exp_f32 v83, v83\n" \
    "v_exp_f32 v84, v84\n" \
    "v_exp_f32 v85, v85\n" \
    "s_waitcnt lgkmcnt(2)\n" \
    "v_smfmac_f32_32x32x32_bf16 %0, v[116:119], v[76:83], v160\n" \
    "ds_read_b128 v[112:115], v161 offset:27648\n" \
    "v_exp_f32 v86, v86\n" \
    "v_exp_f32 v87, v87\n" \
    "v_exp_f32 v88, v88\n" \
    "v_exp_f32 v89, v89\n" \
    "v_exp_f32 v90, v90\n" \
    "v_exp_f32 v91, v91\n" \
    "s_waitcnt lgkmcnt(1)\n" \
    "v_exp_f32 v60, v60\n" \
    "v_exp_f32 v61, v61\n" \
    "v_smfmac_f32_32x32x32_bf16 %0, v[120:123], v[84:91], v160\n" \
    "ds_read_b64_tr_b16 v[116:117], v162 offset:28288\n" \
    "ds_read_b64_tr_b16 v[118:119], v163 offset:28288\n" \
    "v_exp_f32 v62, v62\n" \
    "v_exp_f32 v63, v63\n" \
    "v_exp_f32 v64, v64\n" \
    "v_exp_f32 v65, v65\n" \
    "s_waitcnt lgkmcnt(2)\n" \
    "v_mfma_f32_32x32x16_bf16 v[76:91], v[112:115], v[108:111], v[92:107]\n" \
    "ds_read_b64_tr_b16 v[120:121], v162 offset:28832\n" \
    "ds_read_b64_tr_b16 v[122:123], v163 offset:28832\n" \
    "v_exp_f32 v66, v66\n" \
    "v_exp_f32 v67, v67\n" \
    "v_exp_f32 v68, v68\n" \
    "v_exp_f32 v69, v69\n" \
    "s_waitcnt lgkmcnt(2)\n" \
    "v_smfmac_f32_32x32x32_bf16 %0, v[116:119], v[60:67], v160\n" \
    "ds_read_b128 v[112:115], v161 offset:28672\n" \
    "v_exp_f32 v70, v70\n" \
    "v_exp_f32 v71, v71\n" \
    "v_exp_f32 v72, v72\n" \
    "v_exp_f32 v73, v73\n" \
    "v_exp_f32 v74, v74\n" \
    "v_exp_f32 v75, v75\n" \
    "s_waitcnt lgkmcnt(1)\n" \
    "v_exp_f32 v76, v76\n" \
    "v_exp_f32 v77, v77\n" \
    "v_smfmac_f32_32x32x32_bf16 %0, v[120:123], v[68:75], v160\n" \
    "ds_read_b64_tr_b16 v[116:117], v162 offset:29376\n" \
    "ds_read_b64_tr_b16 v[118:119], v163 offset:29376\n" \
    "v_exp_f32 v78, v78\n" \
    "v_exp_f32 v79, v79\n" \
    "v_exp_f32 v80, v80\n" \
    "v_exp_f32 v81, v81\n" \
    "s_waitcnt lgkmcnt(2)\n" \
    "v_mfma_f32_32x32x16_bf16 v[60:75], v[112:115], v[108:111], v[92:107]\n" \
    "ds_read_b64_tr_b16 v[120:121], v162 offset:29920\n" \
    "ds_read_b64_tr_b16 v[122:123], v163 offset:29920\n" \
    "v_exp_f32 v82, v82\n" \
    "v_exp_f32 v83, v83\n" \
    "v_exp_f32 v84, v84\n" \
    "v_exp_f32 v85, v85\n" \
    "s_waitcnt lgkmcnt(2)\n" \
    "v_smfmac_f32_32x32x32_bf16 %0, v[116:119], v[76:83], v160\n" \
    "ds_read_b128 v[112:115], v161 offset:29696\n" \
    "v_exp_f32 v86, v86\n" \
    "v_exp_f32 v87, v87\n" \
    "v_exp_f32 v88, v88\n" \
    "v_exp_f32 v89, v89\n" \
    "v_exp_f32 v90, v90\n" \
    "v_exp_f32 v91, v91\n" \
    "s_mov_b32 s40, 0x400000\n" \
    "ATT_KW_%=:\n" \
    "ds_read_b32 v59, %9 offset:20\n" \
    "s_waitcnt lgkmcnt(0)\n" \
    "v_readfirstlane_b32 vcc_lo, v59\n" \
    "s_add_i32 s40, s40, -1\n" \
    "s_cmp_eq_u32 s40, 0\n" \
    "s_cbranch_scc1 ATT_KWX_%=\n" \
    "s_cmp_ge_u32 vcc_lo, 4\n" \
    "s_cbranch_scc1 ATT_KWX_%=\n" \
    "s_sleep 2\n" \
    "s_branch ATT_KW_%=\n" \
    "ATT_KWX_%=:\n" \
    "s_mov_b32 s40, 0x400000\n" \
    "ATT_F_%=:\n" \
    "ds_read_b32 v59, %9 offset:12\n" \
    "s_waitcnt lgkmcnt(0)\n" \
    "v_readfirstlane_b32 vcc_lo, v59\n" \
    "s_add_i32 s40, s40, -1\n" \
    "s_cmp_eq_u32 s40, 0\n" \
    "s_cbranch_scc1 ATT_FX_%=\n" \
    "s_cmp_ge_u32 vcc_lo, 1\n" \
    "s_cbranch_scc1 ATT_FX_%=\n" \
    "s_sleep 2\n" \
    "s_branch ATT_F_%=\n" \
    "ATT_FX_%=:\n" \
    "s_movk_i32 s40, 0\n" \
    "s_mov_b32 s41, 0\n" \
    "buffer_load_dwordx4 v[124:127], %10, %11, s40 offen offset:0 sc1\n" \
    "buffer_load_dwordx4 v[128:131], %10, %12, s41 offen offset:0 sc1\n" \
    "buffer_load_dwordx4 v[132:135], %10, %12, s41 offen offset:1024 sc1\n" \
    "s_movk_i32 s40, 0\n" \
    "s_mov_b32 s41, 0\n" \
    "buffer_load_dwordx4 v[136:139], %10, %11, s40 offen offset:1024 sc1\n" \
    "buffer_load_dwordx4 v[140:143], %10, %12, s41 offen offset:2048 sc1\n" \
    "buffer_load_dwordx4 v[144:147], %10, %12, s41 offen offset:3072 sc1\n" \
    "s_movk_i32 s40, 0\n" \
    "s_mov_b32 s41, 4096\n" \
    "buffer_load_dwordx4 v[148:151], %10, %11, s40 offen offset:2048 sc1\n" \
    "buffer_load_dwordx4 v[152:155], %10, %12, s41 offen offset:0 sc1\n" \
    "buffer_load_dwordx4 v[156:159], %10, %12, s41 offen offset:1024 sc1\n" \
    "s_waitcnt lgkmcnt(1)\n" \
    "v_exp_f32 v60, v60\n" \
    "v_exp_f32 v61, v61\n" \
    "v_smfmac_f32_32x32x32_bf16 %0, v[120:123], v[84:91], v160\n" \
    "ds_read_b64_tr_b16 v[116:117], v162 offset:30464\n" \
    "ds_read_b64_tr_b16 v[118:119], v163 offset:30464\n" \
    "v_exp_f32 v62, v62\n" \
    "v_exp_f32 v63, v63\n" \
    "v_exp_f32 v64, v64\n" \
    "v_exp_f32 v65, v65\n" \
    "s_waitcnt lgkmcnt(2)\n" \
    "v_mfma_f32_32x32x16_bf16 v[76:91], v[112:115], v[108:111], v[92:107]\n" \
    "ds_read_b64_tr_b16 v[120:121], v162 offset:31008\n" \
    "ds_read_b64_tr_b16 v[122:123], v163 offset:31008\n" \
    "v_exp_f32 v66, v66\n" \
    "v_exp_f32 v67, v67\n" \
    "v_exp_f32 v68, v68\n" \
    "v_exp_f32 v69, v69\n" \
    "s_waitcnt lgkmcnt(2)\n" \
    "v_smfmac_f32_32x32x32_bf16 %0, v[116:119], v[60:67], v160\n" \
    "ds_read_b128 v[112:115], v161 offset:30720\n" \
    "v_exp_f32 v70, v70\n" \
    "v_exp_f32 v71, v71\n" \
    "v_exp_f32 v72, v72\n" \
    "v_exp_f32 v73, v73\n" \
    "v_exp_f32 v74, v74\n" \
    "v_exp_f32 v75, v75\n" \
    "s_waitcnt lgkmcnt(1)\n" \
    "v_exp_f32 v76, v76\n" \
    "v_exp_f32 v77, v77\n" \
    "v_smfmac_f32_32x32x32_bf16 %0, v[120:123], v[68:75], v160\n" \
    "ds_read_b64_tr_b16 v[116:117], v162 offset:31552\n" \
    "ds_read_b64_tr_b16 v[118:119], v163 offset:31552\n" \
    "v_exp_f32 v78, v78\n" \
    "v_exp_f32 v79, v79\n" \
    "v_exp_f32 v80, v80\n" \
    "v_exp_f32 v81, v81\n" \
    "s_waitcnt lgkmcnt(2)\n" \
    "v_mfma_f32_32x32x16_bf16 v[60:75], v[112:115], v[108:111], v[92:107]\n" \
    "ds_read_b64_tr_b16 v[120:121], v162 offset:32096\n" \
    "ds_read_b64_tr_b16 v[122:123], v163 offset:32096\n" \
    "v_exp_f32 v82, v82\n" \
    "v_exp_f32 v83, v83\n" \
    "v_exp_f32 v84, v84\n" \
    "v_exp_f32 v85, v85\n" \
    "s_waitcnt lgkmcnt(2)\n" \
    "v_smfmac_f32_32x32x32_bf16 %0, v[116:119], v[76:83], v160\n" \
    "ds_read_b128 v[112:115], v161 offset:31744\n" \
    "v_exp_f32 v86, v86\n" \
    "v_exp_f32 v87, v87\n" \
    "v_exp_f32 v88, v88\n" \
    "v_exp_f32 v89, v89\n" \
    "v_exp_f32 v90, v90\n" \
    "v_exp_f32 v91, v91\n" \
    "s_setprio 3\n" \
    "s_waitcnt lgkmcnt(1)\n" \
    "v_exp_f32 v60, v60\n" \
    "v_exp_f32 v61, v61\n" \
    "v_smfmac_f32_32x32x32_bf16 %0, v[120:123], v[84:91], v160\n" \
    "ds_read_b64_tr_b16 v[116:117], v162 offset:32640\n" \
    "ds_read_b64_tr_b16 v[118:119], v163 offset:32640\n" \
    "v_exp_f32 v62, v62\n" \
    "v_exp_f32 v63, v63\n" \
    "v_exp_f32 v64, v64\n" \
    "v_exp_f32 v65, v65\n" \
    "s_waitcnt lgkmcnt(2)\n" \
    "v_mfma_f32_32x32x16_bf16 v[76:91], v[112:115], v[108:111], v[92:107]\n" \
    "ds_read_b64_tr_b16 v[120:121], v162 offset:33184\n" \
    "ds_read_b64_tr_b16 v[122:123], v163 offset:33184\n" \
    "v_exp_f32 v66, v66\n" \
    "v_exp_f32 v67, v67\n" \
    "v_exp_f32 v68, v68\n" \
    "v_exp_f32 v69, v69\n" \
    "s_waitcnt lgkmcnt(2)\n" \
    "v_smfmac_f32_32x32x32_bf16 %0, v[116:119], v[60:67], v160\n" \
    "ds_read_b128 v[112:115], v161 offset:32768\n" \
    "v_exp_f32 v70, v70\n" \
    "v_exp_f32 v71, v71\n" \
    "s_waitcnt vmcnt(7)\n" \
    "v_mfma_f32_32x32x16_bf16 %1, v[128:131], v[124:127], %1\n" \
    "v_exp_f32 v72, v72\n" \
    "v_exp_f32 v73, v73\n" \
    "v_exp_f32 v74, v74\n" \
    "s_waitcnt vmcnt(6)\n" \
    "v_mfma_f32_32x32x16_bf16 %2, v[132:135], v[124:127], %2\n" \
    "v_exp_f32 v75, v75\n" \
    "s_movk_i32 s40, 0\n" \
    "s_mov_b32 s41, 4096\n" \
    "buffer_load_dwordx4 v[124:127], %10, %11, s40 offen offset:3072 sc1\n" \
    "buffer_load_dwordx4 v[128:131], %10, %12, s41 offen offset:2048 sc1\n" \
    "buffer_load_dwordx4 v[132:135], %10, %12, s41 offen offset:3072 sc1\n" \
    "s_waitcnt lgkmcnt(1)\n" \
    "v_exp_f32 v76, v76\n" \
    "v_exp_f32 v77, v77\n" \
    "v_smfmac_f32_32x32x32_bf16 %0, v[120:123], v[68:75], v160\n" \
    "ds_read_b64_tr_b16 v[116:117], v162 offset:33728\n" \
    "ds_read_b64_tr_b16 v[118:119], v163 offset:33728\n" \
    "v_exp_f32 v78, v78\n" \
    "v_exp_f32 v79, v79\n" \
    "v_exp_f32 v80, v80\n" \
    "v_exp_f32 v81, v81\n" \
    "s_waitcnt lgkmcnt(2)\n" \
    "v_mfma_f32_32x32x16_bf16 v[60:75], v[112:115], v[108:111], v[92:107]\n" \
    "ds_read_b64_tr_b16 v[120:121], v162 offset:34272\n" \
    "ds_read_b64_tr_b16 v[122:123], v163 offset:34272\n" \
    "v_exp_f32 v82, v82\n" \
    "v_exp_f32 v83, v83\n" \
    "v_exp_f32 v84, v84\n" \
    "v_exp_f32 v85, v85\n" \
    "s_waitcnt lgkmcnt(2)\n" \
    "v_smfmac_f32_32x32x32_bf16 %0, v[116:119], v[76:83], v160\n" \
    "ds_read_b128 v[112:115], v161 offset:33792\n" \
    "v_exp_f32 v86, v86\n" \
    "v_exp_f32 v87, v87\n" \
    "s_waitcnt vmcnt(7)\n" \
    "v_mfma_f32_32x32x16_bf16 %1, v[140:143], v[136:139], %1\n" \
    "v_exp_f32 v88, v88\n" \
    "v_exp_f32 v89, v89\n" \
    "v_exp_f32 v90, v90\n" \
    "s_waitcnt vmcnt(6)\n" \
    "v_mfma_f32_32x32x16_bf16 %2, v[144:147], v[136:139], %2\n" \
    "v_exp_f32 v91, v91\n" \
    "s_movk_i32 s40, 4096\n" \
    "s_mov_b32 s41, 8192\n" \
    "buffer_load_dwordx4 v[136:139], %10, %11, s40 offen offset:0 sc1\n" \
    "buffer_load_dwordx4 v[140:143], %10, %12, s41 offen offset:0 sc1\n" \
    "buffer_load_dwordx4 v[144:147], %10, %12, s41 offen offset:1024 sc1\n" \
    "v_add_u32 v161, 32768, v161\n" \
    "v_add_u32 v162, 34816, v162\n" \
    "v_add_u32 v163, 34816, v163\n" \
    "v_mov_b32 v59, 1\n" \
    "ds_add_u32 %9, v59 offset:8\n" \
    "s_waitcnt lgkmcnt(0)\n" \
    "s_waitcnt lgkmcnt(1)\n" \
    "v_exp_f32 v60, v60\n" \
    "v_exp_f32 v61, v61\n" \
    "v_smfmac_f32_32x32x32_bf16 %0, v[120:123], v[84:91], v160\n" \
    "ds_read_b64_tr_b16 v[116:117], v162 offset:0\n" \
    "ds_read_b64_tr_b16 v[118:119], v163 offset:0\n" \
    "v_exp_f32 v62, v62\n" \
    "v_exp_f32 v63, v63\n" \
    "v_exp_f32 v64, v64\n" \
    "v_exp_f32 v65, v65\n" \
    "s_waitcnt lgkmcnt(2)\n" \
    "v_mfma_f32_32x32x16_bf16 v[76:91], v[112:115], v[108:111], v[92:107]\n" \
    "ds_read_b64_tr_b16 v[120:121], v162 offset:544\n" \
    "ds_read_b64_tr_b16 v[122:123], v163 offset:544\n" \
    "v_exp_f32 v66, v66\n" \
    "v_exp_f32 v67, v67\n" \
    "v_exp_f32 v68, v68\n" \
    "v_exp_f32 v69, v69\n" \
    "s_waitcnt lgkmcnt(2)\n" \
    "v_smfmac_f32_32x32x32_bf16 %0, v[116:119], v[60:67], v160\n" \
    "ds_read_b128 v[112:115], v161 offset:2048\n" \
    "v_exp_f32 v70, v70\n" \
    "v_exp_f32 v71, v71\n" \
    "v_exp_f32 v72, v72\n" \
    "s_waitcnt vmcnt(7)\n" \
    "v_mfma_f32_32x32x16_bf16 %1, v[152:155], v[148:151], %1\n" \
    "v_exp_f32 v73, v73\n" \
    "v_exp_f32 v74, v74\n" \
    "v_exp_f32 v75, v75\n" \
    "s_waitcnt lgkmcnt(1)\n" \
    "v_exp_f32 v76, v76\n" \
    "v_exp_f32 v77, v77\n" \
    "v_smfmac_f32_32x32x32_bf16 %0, v[120:123], v[68:75], v160\n" \
    "ds_read_b64_tr_b16 v[116:117], v162 offset:1088\n" \
    "ds_read_b64_tr_b16 v[118:119], v163 offset:1088\n" \
    "v_exp_f32 v78, v78\n" \
    "v_exp_f32 v79, v79\n" \
    "v_exp_f32 v80, v80\n" \
    "v_exp_f32 v81, v81\n" \
    "s_waitcnt lgkmcnt(2)\n" \
    "v_mfma_f32_32x32x16_bf16 v[60:75], v[112:115], v[108:111], v[92:107]\n" \
    "ds_read_b64_tr_b16 v[120:121], v162 offset:1632\n" \
    "ds_read_b64_tr_b16 v[122:123], v163 offset:1632\n" \
    "v_exp_f32 v82, v82\n" \
    "v_exp_f32 v83, v83\n" \
    "v_exp_f32 v84, v84\n" \
    "v_exp_f32 v85, v85\n" \
    "s_waitcnt lgkmcnt(2)\n" \
    "v_smfmac_f32_32x32x32_bf16 %0, v[116:119], v[76:83], v160\n" \
    "ds_read_b128 v[112:115], v161 offset:3072\n" \
    "v_exp_f32 v86, v86\n" \
    "v_exp_f32 v87, v87\n" \
    "s_waitcnt vmcnt(6)\n" \
    "v_mfma_f32_32x32x16_bf16 %2, v[156:159], v[148:151], %2\n" \
    "v_exp_f32 v88, v88\n" \
    "v_exp_f32 v89, v89\n" \
    "v_exp_f32 v90, v90\n" \
    "s_movk_i32 s40, 4096\n" \
    "s_mov_b32 s41, 8192\n" \
    "buffer_load_dwordx4 v[148:151], %10, %11, s40 offen offset:1024 sc1\n" \
    "buffer_load_dwordx4 v[152:155], %10, %12, s41 offen offset:2048 sc1\n" \
    "buffer_load_dwordx4 v[156:159], %10, %12, s41 offen offset:3072 sc1\n" \
    "s_waitcnt vmcnt(7)\n" \
    "v_mfma_f32_32x32x16_bf16 %1, v[128:131], v[124:127], %1\n" \
    "v_exp_f32 v91, v91\n" \
    "s_waitcnt lgkmcnt(1)\n" \
    "v_exp_f32 v60, v60\n" \
    "v_exp_f32 v61, v61\n" \
    "v_smfmac_f32_32x32x32_bf16 %0, v[120:123], v[84:91], v160\n" \
    "ds_read_b64_tr_b16 v[116:117], v162 offset:2176\n" \
    "ds_read_b64_tr_b16 v[118:119], v163 offset:2176\n" \
    "v_exp_f32 v62, v62\n" \
    "v_exp_f32 v63, v63\n" \
    "v_exp_f32 v64, v64\n" \
    "v_exp_f32 v65, v65\n" \
    "s_waitcnt lgkmcnt(2)\n" \
    "v_mfma_f32_32x32x16_bf16 v[76:91], v[112:115], v[108:111], v[92:107]\n" \
    "ds_read_b64_tr_b16 v[120:121], v162 offset:2720\n" \
    "ds_read_b64_tr_b16 v[122:123], v163 offset:2720\n" \
    "v_exp_f32 v66, v66\n" \
    "v_exp_f32 v67, v67\n" \
    "v_exp_f32 v68, v68\n" \
    "v_exp_f32 v69, v69\n" \
    "s_waitcnt lgkmcnt(2)\n" \
    "v_smfmac_f32_32x32x32_bf16 %0, v[116:119], v[60:67], v160\n" \
    "ds_read_b128 v[112:115], v161 offset:4096\n" \
    "v_exp_f32 v70, v70\n" \
    "v_exp_f32 v71, v71\n" \
    "s_waitcnt vmcnt(6)\n" \
    "v_mfma_f32_32x32x16_bf16 %2, v[132:135], v[124:127], %2\n" \
    "v_exp_f32 v72, v72\n" \
    "v_exp_f32 v73, v73\n" \
    "v_exp_f32 v74, v74\n" \
    "s_movk_i32 s40, 4096\n" \
    "s_mov_b32 s41, 12288\n" \
    "buffer_load_dwordx4 v[124:127], %10, %11, s40 offen offset:2048 sc1\n" \
    "buffer_load_dwordx4 v[128:131], %10, %12, s41 offen offset:0 sc1\n" \
    "buffer_load_dwordx4 v[132:135], %10, %12, s41 offen offset:1024 sc1\n" \
    "s_waitcnt vmcnt(7)\n" \
    "v_mfma_f32_32x32x16_bf16 %1, v[140:143], v[136:139], %1\n" \
    "v_exp_f32 v75, v75\n" \
    "s_waitcnt lgkmcnt(1)\n" \
    "v_exp_f32 v76, v76\n" \
    "v_exp_f32 v77, v77\n" \
    "v_smfmac_f32_32x32x32_bf16 %0, v[120:123], v[68:75], v160\n" \
    "ds_read_b64_tr_b16 v[116:117], v162 offset:3264\n" \
    "ds_read_b64_tr_b16 v[118:119], v163 offset:3264\n" \
    "v_exp_f32 v78, v78\n" \
    "v_exp_f32 v79, v79\n" \
    "v_exp_f32 v80, v80\n" \
    "v_exp_f32 v81, v81\n" \
    "s_waitcnt lgkmcnt(2)\n" \
    "v_mfma_f32_32x32x16_bf16 v[60:75], v[112:115], v[108:111], v[92:107]\n" \
    "ds_read_b64_tr_b16 v[120:121], v162 offset:3808\n" \
    "ds_read_b64_tr_b16 v[122:123], v163 offset:3808\n" \
    "v_exp_f32 v82, v82\n" \
    "v_exp_f32 v83, v83\n" \
    "v_exp_f32 v84, v84\n" \
    "v_exp_f32 v85, v85\n" \
    "s_waitcnt lgkmcnt(2)\n" \
    "v_smfmac_f32_32x32x32_bf16 %0, v[116:119], v[76:83], v160\n" \
    "ds_read_b128 v[112:115], v161 offset:5120\n" \
    "v_exp_f32 v86, v86\n" \
    "v_exp_f32 v87, v87\n" \
    "v_exp_f32 v88, v88\n" \
    "s_waitcnt vmcnt(6)\n" \
    "v_mfma_f32_32x32x16_bf16 %2, v[144:147], v[136:139], %2\n" \
    "v_exp_f32 v89, v89\n" \
    "v_exp_f32 v90, v90\n" \
    "v_exp_f32 v91, v91\n" \
    "s_movk_i32 s40, 4096\n" \
    "s_mov_b32 s41, 12288\n" \
    "buffer_load_dwordx4 v[136:139], %10, %11, s40 offen offset:3072 sc1\n" \
    "buffer_load_dwordx4 v[140:143], %10, %12, s41 offen offset:2048 sc1\n" \
    "buffer_load_dwordx4 v[144:147], %10, %12, s41 offen offset:3072 sc1\n" \
    "s_waitcnt lgkmcnt(1)\n" \
    "v_exp_f32 v60, v60\n" \
    "v_exp_f32 v61, v61\n" \
    "v_smfmac_f32_32x32x32_bf16 %0, v[120:123], v[84:91], v160\n" \
    "ds_read_b64_tr_b16 v[116:117], v162 offset:4352\n" \
    "ds_read_b64_tr_b16 v[118:119], v163 offset:4352\n" \
    "v_exp_f32 v62, v62\n" \
    "v_exp_f32 v63, v63\n" \
    "v_exp_f32 v64, v64\n" \
    "v_exp_f32 v65, v65\n" \
    "s_waitcnt lgkmcnt(2)\n" \
    "v_mfma_f32_32x32x16_bf16 v[76:91], v[112:115], v[108:111], v[92:107]\n" \
    "ds_read_b64_tr_b16 v[120:121], v162 offset:4896\n" \
    "ds_read_b64_tr_b16 v[122:123], v163 offset:4896\n" \
    "v_exp_f32 v66, v66\n" \
    "v_exp_f32 v67, v67\n" \
    "v_exp_f32 v68, v68\n" \
    "v_exp_f32 v69, v69\n" \
    "s_waitcnt lgkmcnt(2)\n" \
    "v_smfmac_f32_32x32x32_bf16 %0, v[116:119], v[60:67], v160\n" \
    "ds_read_b128 v[112:115], v161 offset:6144\n" \
    "v_exp_f32 v70, v70\n" \
    "v_exp_f32 v71, v71\n" \
    "s_waitcnt vmcnt(7)\n" \
    "v_mfma_f32_32x32x16_bf16 %1, v[152:155], v[148:151], %1\n" \
    "v_exp_f32 v72, v72\n" \
    "v_exp_f32 v73, v73\n" \
    "v_exp_f32 v74, v74\n" \
    "s_waitcnt vmcnt(6)\n" \
    "v_mfma_f32_32x32x16_bf16 %2, v[156:159], v[148:151], %2\n" \
    "v_exp_f32 v75, v75\n" \
    "s_movk_i32 s40, 8192\n" \
    "s_mov_b32 s41, 16384\n" \
    "buffer_load_dwordx4 v[148:151], %10, %11, s40 offen offset:0 sc1\n" \
    "buffer_load_dwordx4 v[152:155], %10, %12, s41 offen offset:0 sc1\n" \
    "buffer_load_dwordx4 v[156:159], %10, %12, s41 offen offset:1024 sc1\n" \
    "s_waitcnt lgkmcnt(1)\n" \
    "v_exp_f32 v76, v76\n" \
    "v_exp_f32 v77, v77\n" \
    "v_smfmac_f32_32x32x32_bf16 %0, v[120:123], v[68:75], v160\n" \
    "ds_read_b64_tr_b16 v[116:117], v162 offset:5440\n" \
    "ds_read_b64_tr_b16 v[118:119], v163 offset:5440\n" \
    "v_exp_f32 v78, v78\n" \
    "v_exp_f32 v79, v79\n" \
    "v_exp_f32 v80, v80\n" \
    "v_exp_f32 v81, v81\n" \
    "s_waitcnt lgkmcnt(2)\n" \
    "v_mfma_f32_32x32x16_bf16 v[60:75], v[112:115], v[108:111], v[92:107]\n" \
    "ds_read_b64_tr_b16 v[120:121], v162 offset:5984\n" \
    "ds_read_b64_tr_b16 v[122:123], v163 offset:5984\n" \
    "v_exp_f32 v82, v82\n" \
    "v_exp_f32 v83, v83\n" \
    "v_exp_f32 v84, v84\n" \
    "v_exp_f32 v85, v85\n" \
    "s_waitcnt lgkmcnt(2)\n" \
    "v_smfmac_f32_32x32x32_bf16 %0, v[116:119], v[76:83], v160\n" \
    "ds_read_b128 v[112:115], v161 offset:7168\n" \
    "v_exp_f32 v86, v86\n" \
    "v_exp_f32 v87, v87\n" \
    "s_waitcnt vmcnt(7)\n" \
    "v_mfma_f32_32x32x16_bf16 %1, v[128:131], v[124:127], %1\n" \
    "v_exp_f32 v88, v88\n" \
    "v_exp_f32 v89, v89\n" \
    "v_exp_f32 v90, v90\n" \
    "s_waitcnt vmcnt(6)\n" \
    "v_mfma_f32_32x32x16_bf16 %2, v[132:135], v[124:127], %2\n" \
    "v_exp_f32 v91, v91\n" \
    "s_movk_i32 s40, 8192\n" \
    "s_mov_b32 s41, 16384\n" \
    "buffer_load_dwordx4 v[124:127], %10, %11, s40 offen offset:1024 sc1\n" \
    "buffer_load_dwordx4 v[128:131], %10, %12, s41 offen offset:2048 sc1\n" \
    "buffer_load_dwordx4 v[132:135], %10, %12, s41 offen offset:3072 sc1\n" \
    "s_waitcnt lgkmcnt(1)\n" \
    "v_exp_f32 v60, v60\n" \
    "v_exp_f32 v61, v61\n" \
    "v_smfmac_f32_32x32x32_bf16 %0, v[120:123], v[84:91], v160\n" \
    "ds_read_b64_tr_b16 v[116:117], v162 offset:6528\n" \
    "ds_read_b64_tr_b16 v[118:119], v163 offset:6528\n" \
    "v_exp_f32 v62, v62\n" \
    "v_exp_f32 v63, v63\n" \
    "v_exp_f32 v64, v64\n" \
    "v_exp_f32 v65, v65\n" \
    "s_waitcnt lgkmcnt(2)\n" \
    "v_mfma_f32_32x32x16_bf16 v[76:91], v[112:115], v[108:111], v[92:107]\n" \
    "ds_read_b64_tr_b16 v[120:121], v162 offset:7072\n" \
    "ds_read_b64_tr_b16 v[122:123], v163 offset:7072\n" \
    "v_exp_f32 v66, v66\n" \
    "v_exp_f32 v67, v67\n" \
    "v_exp_f32 v68, v68\n" \
    "v_exp_f32 v69, v69\n" \
    "s_waitcnt lgkmcnt(2)\n" \
    "v_smfmac_f32_32x32x32_bf16 %0, v[116:119], v[60:67], v160\n" \
    "ds_read_b128 v[112:115], v161 offset:8192\n" \
    "v_exp_f32 v70, v70\n" \
    "v_exp_f32 v71, v71\n" \
    "v_exp_f32 v72, v72\n" \
    "s_waitcnt vmcnt(7)\n" \
    "v_mfma_f32_32x32x16_bf16 %1, v[140:143], v[136:139], %1\n" \
    "v_exp_f32 v73, v73\n" \
    "v_exp_f32 v74, v74\n" \
    "v_exp_f32 v75, v75\n" \
    "s_waitcnt lgkmcnt(1)\n" \
    "v_exp_f32 v76, v76\n" \
    "v_exp_f32 v77, v77\n" \
    "v_smfmac_f32_32x32x32_bf16 %0, v[120:123], v[68:75], v160\n" \
    "ds_read_b64_tr_b16 v[116:117], v162 offset:7616\n" \
    "ds_read_b64_tr_b16 v[118:119], v163 offset:7616\n" \
    "v_exp_f32 v78, v78\n" \
    "v_exp_f32 v79, v79\n" \
    "v_exp_f32 v80, v80\n" \
    "v_exp_f32 v81, v81\n" \
    "s_waitcnt lgkmcnt(2)\n" \
    "v_mfma_f32_32x32x16_bf16 v[60:75], v[112:115], v[108:111], v[92:107]\n" \
    "ds_read_b64_tr_b16 v[120:121], v162 offset:8160\n" \
    "ds_read_b64_tr_b16 v[122:123], v163 offset:8160\n" \
    "v_exp_f32 v82, v82\n" \
    "v_exp_f32 v83, v83\n" \
    "v_exp_f32 v84, v84\n" \
    "v_exp_f32 v85, v85\n" \
    "s_waitcnt lgkmcnt(2)\n" \
    "v_smfmac_f32_32x32x32_bf16 %0, v[116:119], v[76:83], v160\n" \
    "ds_read_b128 v[112:115], v161 offset:9216\n" \
    "v_exp_f32 v86, v86\n" \
    "v_exp_f32 v87, v87\n" \
    "s_waitcnt vmcnt(6)\n" \
    "v_mfma_f32_32x32x16_bf16 %2, v[144:147], v[136:139], %2\n" \
    "v_exp_f32 v88, v88\n" \
    "v_exp_f32 v89, v89\n" \
    "v_exp_f32 v90, v90\n" \
    "s_movk_i32 s40, 8192\n" \
    "s_mov_b32 s41, 20480\n" \
    "buffer_load_dwordx4 v[136:139], %10, %11, s40 offen offset:2048 sc1\n" \
    "buffer_load_dwordx4 v[140:143], %10, %12, s41 offen offset:0 sc1\n" \
    "buffer_load_dwordx4 v[144:147], %10, %12, s41 offen offset:1024 sc1\n" \
    "s_waitcnt vmcnt(7)\n" \
    "v_mfma_f32_32x32x16_bf16 %1, v[152:155], v[148:151], %1\n" \
    "v_exp_f32 v91, v91\n" \
    "s_waitcnt lgkmcnt(1)\n" \
    "v_exp_f32 v60, v60\n" \
    "v_exp_f32 v61, v61\n" \
    "v_smfmac_f32_32x32x32_bf16 %0, v[120:123], v[84:91], v160\n" \
    "ds_read_b64_tr_b16 v[116:117], v162 offset:8704\n" \
    "ds_read_b64_tr_b16 v[118:119], v163 offset:8704\n" \
    "v_exp_f32 v62, v62\n" \
    "v_exp_f32 v63, v63\n" \
    "v_exp_f32 v64, v64\n" \
    "v_exp_f32 v65, v65\n" \
    "s_waitcnt lgkmcnt(2)\n" \
    "v_mfma_f32_32x32x16_bf16 v[76:91], v[112:115], v[108:111], v[92:107]\n" \
    "ds_read_b64_tr_b16 v[120:121], v162 offset:9248\n" \
    "ds_read_b64_tr_b16 v[122:123], v163 offset:9248\n" \
    "v_exp_f32 v66, v66\n" \
    "v_exp_f32 v67, v67\n" \
    "v_exp_f32 v68, v68\n" \
    "v_exp_f32 v69, v69\n" \
    "s_waitcnt lgkmcnt(2)\n" \
    "v_smfmac_f32_32x32x32_bf16 %0, v[116:119], v[60:67], v160\n" \
    "ds_read_b128 v[112:115], v161 offset:10240\n" \
    "v_exp_f32 v70, v70\n" \
    "v_exp_f32 v71, v71\n" \
    "s_waitcnt vmcnt(6)\n" \
    "v_mfma_f32_32x32x16_bf16 %2, v[156:159], v[148:151], %2\n" \
    "v_exp_f32 v72, v72\n" \
    "v_exp_f32 v73, v73\n" \
    "v_exp_f32 v74, v74\n" \
    "s_movk_i32 s40, 8192\n" \
    "s_mov_b32 s41, 20480\n" \
    "buffer_load_dwordx4 v[148:151], %10, %11, s40 offen offset:3072 sc1\n" \
    "buffer_load_dwordx4 v[152:155], %10, %12, s41 offen offset:2048 sc1\n" \
    "buffer_load_dwordx4 v[156:159], %10, %12, s41 offen offset:3072 sc1\n" \
    "s_waitcnt vmcnt(7)\n" \
    "v_mfma_f32_32x32x16_bf16 %1, v[128:131], v[124:127], %1\n" \
    "v_exp_f32 v75, v75\n" \
    "s_waitcnt lgkmcnt(1)\n" \
    "v_exp_f32 v76, v76\n" \
    "v_exp_f32 v77, v77\n" \
    "v_smfmac_f32_32x32x32_bf16 %0, v[120:123], v[68:75], v160\n" \
    "ds_read_b64_tr_b16 v[116:117], v162 offset:9792\n" \
    "ds_read_b64_tr_b16 v[118:119], v163 offset:9792\n" \
    "v_exp_f32 v78, v78\n" \
    "v_exp_f32 v79, v79\n" \
    "v_exp_f32 v80, v80\n" \
    "v_exp_f32 v81, v81\n" \
    "s_waitcnt lgkmcnt(2)\n" \
    "v_mfma_f32_32x32x16_bf16 v[60:75], v[112:115], v[108:111], v[92:107]\n" \
    "ds_read_b64_tr_b16 v[120:121], v162 offset:10336\n" \
    "ds_read_b64_tr_b16 v[122:123], v163 offset:10336\n" \
    "v_exp_f32 v82, v82\n" \
    "v_exp_f32 v83, v83\n" \
    "v_exp_f32 v84, v84\n" \
    "v_exp_f32 v85, v85\n" \
    "s_waitcnt lgkmcnt(2)\n" \
    "v_smfmac_f32_32x32x32_bf16 %0, v[116:119], v[76:83], v160\n" \
    "ds_read_b128 v[112:115], v161 offset:11264\n" \
    "v_exp_f32 v86, v86\n" \
    "v_exp_f32 v87, v87\n" \
    "v_exp_f32 v88, v88\n" \
    "s_waitcnt vmcnt(6)\n" \
    "v_mfma_f32_32x32x16_bf16 %2, v[132:135], v[124:127], %2\n" \
    "v_exp_f32 v89, v89\n" \
    "v_exp_f32 v90, v90\n" \
    "v_exp_f32 v91, v91\n" \
    "s_movk_i32 s40, 12288\n" \
    "s_mov_b32 s41, 24576\n" \
    "buffer_load_dwordx4 v[124:127], %10, %11, s40 offen offset:0 sc1\n" \
    "buffer_load_dwordx4 v[128:131], %10, %12, s41 offen offset:0 sc1\n" \
    "buffer_load_dwordx4 v[132:135], %10, %12, s41 offen offset:1024 sc1\n" \
    "s_waitcnt lgkmcnt(1)\n" \
    "v_exp_f32 v60, v60\n" \
    "v_exp_f32 v61, v61\n" \
    "v_smfmac_f32_32x32x32_bf16 %0, v[120:123], v[84:91], v160\n" \
    "ds_read_b64_tr_b16 v[116:117], v162 offset:10880\n" \
    "ds_read_b64_tr_b16 v[118:119], v163 offset:10880\n" \
    "v_exp_f32 v62, v62\n" \
    "v_exp_f32 v63, v63\n" \
    "v_exp_f32 v64, v64\n" \
    "v_exp_f32 v65, v65\n" \
    "s_waitcnt lgkmcnt(2)\n" \
    "v_mfma_f32_32x32x16_bf16 v[76:91], v[112:115], v[108:111], v[92:107]\n" \
    "ds_read_b64_tr_b16 v[120:121], v162 offset:11424\n" \
    "ds_read_b64_tr_b16 v[122:123], v163 offset:11424\n" \
    "v_exp_f32 v66, v66\n" \
    "v_exp_f32 v67, v67\n" \
    "v_exp_f32 v68, v68\n" \
    "v_exp_f32 v69, v69\n" \
    "s_waitcnt lgkmcnt(2)\n" \
    "v_smfmac_f32_32x32x32_bf16 %0, v[116:119], v[60:67], v160\n" \
    "ds_read_b128 v[112:115], v161 offset:12288\n" \
    "v_exp_f32 v70, v70\n" \
    "v_exp_f32 v71, v71\n" \
    "s_waitcnt vmcnt(7)\n" \
    "v_mfma_f32_32x32x16_bf16 %1, v[140:143], v[136:139], %1\n" \
    "v_exp_f32 v72, v72\n" \
    "v_exp_f32 v73, v73\n" \
    "v_exp_f32 v74, v74\n" \
    "s_waitcnt vmcnt(6)\n" \
    "v_mfma_f32_32x32x16_bf16 %2, v[144:147], v[136:139], %2\n" \
    "v_exp_f32 v75, v75\n" \
    "s_movk_i32 s40, 12288\n" \
    "s_mov_b32 s41, 24576\n" \
    "buffer_load_dwordx4 v[136:139], %10, %11, s40 offen offset:1024 sc1\n" \
    "buffer_load_dwordx4 v[140:143], %10, %12, s41 offen offset:2048 sc1\n" \
    "buffer_load_dwordx4 v[144:147], %10, %12, s41 offen offset:3072 sc1\n" \
    "s_waitcnt lgkmcnt(1)\n" \
    "v_exp_f32 v76, v76\n" \
    "v_exp_f32 v77, v77\n" \
    "v_smfmac_f32_32x32x32_bf16 %0, v[120:123], v[68:75], v160\n" \
    "ds_read_b64_tr_b16 v[116:117], v162 offset:11968\n" \
    "ds_read_b64_tr_b16 v[118:119], v163 offset:11968\n" \
    "v_exp_f32 v78, v78\n" \
    "v_exp_f32 v79, v79\n" \
    "v_exp_f32 v80, v80\n" \
    "v_exp_f32 v81, v81\n" \
    "s_waitcnt lgkmcnt(2)\n" \
    "v_mfma_f32_32x32x16_bf16 v[60:75], v[112:115], v[108:111], v[92:107]\n" \
    "ds_read_b64_tr_b16 v[120:121], v162 offset:12512\n" \
    "ds_read_b64_tr_b16 v[122:123], v163 offset:12512\n" \
    "v_exp_f32 v82, v82\n" \
    "v_exp_f32 v83, v83\n" \
    "v_exp_f32 v84, v84\n" \
    "v_exp_f32 v85, v85\n" \
    "s_waitcnt lgkmcnt(2)\n" \
    "v_smfmac_f32_32x32x32_bf16 %0, v[116:119], v[76:83], v160\n" \
    "ds_read_b128 v[112:115], v161 offset:13312\n" \
    "v_exp_f32 v86, v86\n" \
    "v_exp_f32 v87, v87\n" \
    "s_waitcnt vmcnt(7)\n" \
    "v_mfma_f32_32x32x16_bf16 %1, v[152:155], v[148:151], %1\n" \
    "v_exp_f32 v88, v88\n" \
    "v_exp_f32 v89, v89\n" \
    "v_exp_f32 v90, v90\n" \
    "s_waitcnt vmcnt(6)\n" \
    "v_mfma_f32_32x32x16_bf16 %2, v[156:159], v[148:151], %2\n" \
    "v_exp_f32 v91, v91\n" \
    "s_movk_i32 s40, 12288\n" \
    "s_mov_b32 s41, 28672\n" \
    "buffer_load_dwordx4 v[148:151], %10, %11, s40 offen offset:2048 sc1\n" \
    "buffer_load_dwordx4 v[152:155], %10, %12, s41 offen offset:0 sc1\n" \
    "buffer_load_dwordx4 v[156:159], %10, %12, s41 offen offset:1024 sc1\n" \
    "s_setprio 2\n" \
    "s_waitcnt lgkmcnt(1)\n" \
    "v_exp_f32 v60, v60\n" \
    "v_exp_f32 v61, v61\n" \
    "v_smfmac_f32_32x32x32_bf16 %0, v[120:123], v[84:91], v160\n" \
    "ds_read_b64_tr_b16 v[116:117], v162 offset:13056\n" \
    "ds_read_b64_tr_b16 v[118:119], v163 offset:13056\n" \
    "v_exp_f32 v62, v62\n" \
    "v_exp_f32 v63, v63\n" \
    "v_exp_f32 v64, v64\n" \
    "v_exp_f32 v65, v65\n" \
    "s_waitcnt lgkmcnt(2)\n" \
    "v_mfma_f32_32x32x16_bf16 v[76:91], v[112:115], v[108:111], v[92:107]\n" \
    "ds_read_b64_tr_b16 v[120:121], v162 offset:13600\n" \
    "ds_read_b64_tr_b16 v[122:123], v163 offset:13600\n" \
    "v_exp_f32 v66, v66\n" \
    "v_exp_f32 v67, v67\n" \
    "v_exp_f32 v68, v68\n" \
    "v_exp_f32 v69, v69\n" \
    "s_waitcnt lgkmcnt(2)\n" \
    "v_smfmac_f32_32x32x32_bf16 %0, v[116:119], v[60:67], v160\n" \
    "ds_read_b128 v[112:115], v161 offset:14336\n" \
    "v_exp_f32 v70, v70\n" \
    "v_exp_f32 v71, v71\n" \
    "v_exp_f32 v72, v72\n" \
    "s_waitcnt vmcnt(7)\n" \
    "v_mfma_f32_32x32x16_bf16 %1, v[128:131], v[124:127], %1\n" \
    "v_exp_f32 v73, v73\n" \
    "v_exp_f32 v74, v74\n" \
    "v_exp_f32 v75, v75\n" \
    "s_waitcnt lgkmcnt(1)\n" \
    "v_exp_f32 v76, v76\n" \
    "v_exp_f32 v77, v77\n" \
    "v_smfmac_f32_32x32x32_bf16 %0, v[120:123], v[68:75], v160\n" \
    "ds_read_b64_tr_b16 v[116:117], v162 offset:14144\n" \
    "ds_read_b64_tr_b16 v[118:119], v163 offset:14144\n" \
    "v_exp_f32 v78, v78\n" \
    "v_exp_f32 v79, v79\n" \
    "v_exp_f32 v80, v80\n" \
    "v_exp_f32 v81, v81\n" \
    "s_waitcnt lgkmcnt(2)\n" \
    "v_mfma_f32_32x32x16_bf16 v[60:75], v[112:115], v[108:111], v[92:107]\n" \
    "ds_read_b64_tr_b16 v[120:121], v162 offset:14688\n" \
    "ds_read_b64_tr_b16 v[122:123], v163 offset:14688\n" \
    "v_exp_f32 v82, v82\n" \
    "v_exp_f32 v83, v83\n" \
    "v_exp_f32 v84, v84\n" \
    "v_exp_f32 v85, v85\n" \
    "s_waitcnt lgkmcnt(2)\n" \
    "v_smfmac_f32_32x32x32_bf16 %0, v[116:119], v[76:83], v160\n" \
    "ds_read_b128 v[112:115], v161 offset:15360\n" \
    "v_exp_f32 v86, v86\n" \
    "v_exp_f32 v87, v87\n" \
    "s_waitcnt vmcnt(6)\n" \
    "v_mfma_f32_32x32x16_bf16 %2, v[132:135], v[124:127], %2\n" \
    "v_exp_f32 v88, v88\n" \
    "v_exp_f32 v89, v89\n" \
    "v_exp_f32 v90, v90\n" \
    "s_movk_i32 s40, 12288\n" \
    "s_mov_b32 s41, 28672\n" \
    "buffer_load_dwordx4 v[124:127], %10, %11, s40 offen offset:3072 sc1\n" \
    "buffer_load_dwordx4 v[128:131], %10, %12, s41 offen offset:2048 sc1\n" \
    "buffer_load_dwordx4 v[132:135], %10, %12, s41 offen offset:3072 sc1\n" \
    "s_waitcnt vmcnt(7)\n" \
    "v_mfma_f32_32x32x16_bf16 %1, v[140:143], v[136:139], %1\n" \
    "v_exp_f32 v91, v91\n" \
    "s_waitcnt lgkmcnt(1)\n" \
    "v_exp_f32 v60, v60\n" \
    "v_exp_f32 v61, v61\n" \
    "v_smfmac_f32_32x32x32_bf16 %0, v[120:123], v[84:91], v160\n" \
    "ds_read_b64_tr_b16 v[116:117], v162 offset:15232\n" \
    "ds_read_b64_tr_b16 v[118:119], v163 offset:15232\n" \
    "v_exp_f32 v62, v62\n" \
    "v_exp_f32 v63, v63\n" \
    "v_exp_f32 v64, v64\n" \
    "v_exp_f32 v65, v65\n" \
    "s_waitcnt lgkmcnt(2)\n" \
    "v_mfma_f32_32x32x16_bf16 v[76:91], v[112:115], v[108:111], v[92:107]\n" \
    "ds_read_b64_tr_b16 v[120:121], v162 offset:15776\n" \
    "ds_read_b64_tr_b16 v[122:123], v163 offset:15776\n" \
    "v_exp_f32 v66, v66\n" \
    "v_exp_f32 v67, v67\n" \
    "v_exp_f32 v68, v68\n" \
    "v_exp_f32 v69, v69\n" \
    "s_waitcnt lgkmcnt(2)\n" \
    "v_smfmac_f32_32x32x32_bf16 %0, v[116:119], v[60:67], v160\n" \
    "ds_read_b128 v[112:115], v161 offset:16384\n" \
    "v_exp_f32 v70, v70\n" \
    "v_exp_f32 v71, v71\n" \
    "v_exp_f32 v72, v72\n" \
    "s_waitcnt vmcnt(6)\n" \
    "v_mfma_f32_32x32x16_bf16 %2, v[144:147], v[136:139], %2\n" \
    "v_exp_f32 v73, v73\n" \
    "v_exp_f32 v74, v74\n" \
    "v_exp_f32 v75, v75\n" \
    "s_movk_i32 s40, 16384\n" \
    "s_mov_b32 s41, 32768\n" \
    "buffer_load_dwordx4 v[136:139], %10, %11, s40 offen offset:0 sc1\n" \
    "buffer_load_dwordx4 v[140:143], %10, %12, s41 offen offset:0 sc1\n" \
    "buffer_load_dwordx4 v[144:147], %10, %12, s41 offen offset:1024 sc1\n" \
    "s_waitcnt lgkmcnt(1)\n" \
    "v_exp_f32 v76, v76\n" \
    "v_exp_f32 v77, v77\n" \
    "v_smfmac_f32_32x32x32_bf16 %0, v[120:123], v[68:75], v160\n" \
    "ds_read_b64_tr_b16 v[116:117], v162 offset:16320\n" \
    "ds_read_b64_tr_b16 v[118:119], v163 offset:16320\n" \
    "v_exp_f32 v78, v78\n" \
    "v_exp_f32 v79, v79\n" \
    "v_exp_f32 v80, v80\n" \
    "v_exp_f32 v81, v81\n" \
    "s_waitcnt lgkmcnt(2)\n" \
    "v_mfma_f32_32x32x16_bf16 v[60:75], v[112:115], v[108:111], v[92:107]\n" \
    "ds_read_b64_tr_b16 v[120:121], v162 offset:16864\n" \
    "ds_read_b64_tr_b16 v[122:123], v163 offset:16864\n" \
    "v_exp_f32 v82, v82\n" \
    "v_exp_f32 v83, v83\n" \
    "v_exp_f32 v84, v84\n" \
    "v_exp_f32 v85, v85\n" \
    "s_waitcnt lgkmcnt(2)\n" \
    "v_smfmac_f32_32x32x32_bf16 %0, v[116:119], v[76:83], v160\n" \
    "ds_read_b128 v[112:115], v161 offset:17408\n" \
    "v_exp_f32 v86, v86\n" \
    "v_exp_f32 v87, v87\n" \
    "s_waitcnt vmcnt(7)\n" \
    "v_mfma_f32_32x32x16_bf16 %1, v[152:155], v[148:151], %1\n" \
    "v_exp_f32 v88, v88\n" \
    "v_exp_f32 v89, v89\n" \
    "v_exp_f32 v90, v90\n" \
    "s_waitcnt vmcnt(6)\n" \
    "v_mfma_f32_32x32x16_bf16 %2, v[156:159], v[148:151], %2\n" \
    "v_exp_f32 v91, v91\n" \
    "s_movk_i32 s40, 16384\n" \
    "s_mov_b32 s41, 32768\n" \
    "buffer_load_dwordx4 v[148:151], %10, %11, s40 offen offset:1024 sc1\n" \
    "buffer_load_dwordx4 v[152:155], %10, %12, s41 offen offset:2048 sc1\n" \
    "buffer_load_dwordx4 v[156:159], %10, %12, s41 offen offset:3072 sc1\n" \
    "s_waitcnt lgkmcnt(1)\n" \
    "v_exp_f32 v60, v60\n" \
    "v_exp_f32 v61, v61\n" \
    "v_smfmac_f32_32x32x32_bf16 %0, v[120:123], v[84:91], v160\n" \
    "ds_read_b64_tr_b16 v[116:117], v162 offset:17408\n" \
    "ds_read_b64_tr_b16 v[118:119], v163 offset:17408\n" \
    "v_exp_f32 v62, v62\n" \
    "v_exp_f32 v63, v63\n" \
    "v_exp_f32 v64, v64\n" \
    "v_exp_f32 v65, v65\n" \
    "s_waitcnt lgkmcnt(2)\n" \
    "v_mfma_f32_32x32x16_bf16 v[76:91], v[112:115], v[108:111], v[92:107]\n" \
    "ds_read_b64_tr_b16 v[120:121], v162 offset:17952\n" \
    "ds_read_b64_tr_b16 v[122:123], v163 offset:17952\n" \
    "v_exp_f32 v66, v66\n" \
    "v_exp_f32 v67, v67\n" \
    "v_exp_f32 v68, v68\n" \
    "v_exp_f32 v69, v69\n" \
    "s_waitcnt lgkmcnt(2)\n" \
    "v_smfmac_f32_32x32x32_bf16 %0, v[116:119], v[60:67], v160\n" \
    "ds_read_b128 v[112:115], v161 offset:18432\n" \
    "v_exp_f32 v70, v70\n" \
    "v_exp_f32 v71, v71\n" \
    "s_waitcnt vmcnt(7)\n" \
    "v_mfma_f32_32x32x16_bf16 %1, v[128:131], v[124:127], %1\n" \
    "v_exp_f32 v72, v72\n" \
    "v_exp_f32 v73, v73\n" \
    "v_exp_f32 v74, v74\n" \
    "s_waitcnt vmcnt(6)\n" \
    "v_mfma_f32_32x32x16_bf16 %2, v[132:135], v[124:127], %2\n" \
    "v_exp_f32 v75, v75\n" \
    "s_movk_i32 s40, 16384\n" \
    "s_mov_b32 s41, 36864\n" \
    "buffer_load_dwordx4 v[124:127], %10, %11, s40 offen offset:2048 sc1\n" \
    "buffer_load_dwordx4 v[128:131], %10, %12, s41 offen offset:0 sc1\n" \
    "buffer_load_dwordx4 v[132:135], %10, %12, s41 offen offset:1024 sc1\n" \
    "s_waitcnt lgkmcnt(1)\n" \
    "v_exp_f32 v76, v76\n" \
    "v_exp_f32 v77, v77\n" \
    "v_smfmac_f32_32x32x32_bf16 %0, v[120:123], v[68:75], v160\n" \
    "ds_read_b64_tr_b16 v[116:117], v162 offset:18496\n" \
    "ds_read_b64_tr_b16 v[118:119], v163 offset:18496\n" \
    "v_exp_f32 v78, v78\n" \
    "v_exp_f32 v79, v79\n" \
    "v_exp_f32 v80, v80\n" \
    "v_exp_f32 v81, v81\n" \
    "s_waitcnt lgkmcnt(2)\n" \
    "v_mfma_f32_32x32x16_bf16 v[60:75], v[112:115], v[108:111], v[92:107]\n" \
    "ds_read_b64_tr_b16 v[120:121], v162 offset:19040\n" \
    "ds_read_b64_tr_b16 v[122:123], v163 offset:19040\n" \
    "v_exp_f32 v82, v82\n" \
    "v_exp_f32 v83, v83\n" \
    "v_exp_f32 v84, v84\n" \
    "v_exp_f32 v85, v85\n" \
    "s_waitcnt lgkmcnt(2)\n" \
    "v_smfmac_f32_32x32x32_bf16 %0, v[116:119], v[76:83], v160\n" \
    "ds_read_b128 v[112:115], v161 offset:19456\n" \
    "v_exp_f32 v86, v86\n" \
    "v_exp_f32 v87, v87\n" \
    "v_exp_f32 v88, v88\n" \
    "s_waitcnt vmcnt(7)\n" \
    "v_mfma_f32_32x32x16_bf16 %1, v[140:143], v[136:139], %1\n" \
    "v_exp_f32 v89, v89\n" \
    "v_exp_f32 v90, v90\n" \
    "v_exp_f32 v91, v91\n" \
    "s_waitcnt lgkmcnt(1)\n" \
    "v_exp_f32 v60, v60\n" \
    "v_exp_f32 v61, v61\n" \
    "v_smfmac_f32_32x32x32_bf16 %0, v[120:123], v[84:91], v160\n" \
    "ds_read_b64_tr_b16 v[116:117], v162 offset:19584\n" \
    "ds_read_b64_tr_b16 v[118:119], v163 offset:19584\n" \
    "v_exp_f32 v62, v62\n" \
    "v_exp_f32 v63, v63\n" \
    "v_exp_f32 v64, v64\n" \
    "v_exp_f32 v65, v65\n" \
    "s_waitcnt lgkmcnt(2)\n" \
    "v_mfma_f32_32x32x16_bf16 v[76:91], v[112:115], v[108:111], v[92:107]\n" \
    "ds_read_b64_tr_b16 v[120:121], v162 offset:20128\n" \
    "ds_read_b64_tr_b16 v[122:123], v163 offset:20128\n" \
    "v_exp_f32 v66, v66\n" \
    "v_exp_f32 v67, v67\n" \
    "v_exp_f32 v68, v68\n" \
    "v_exp_f32 v69, v69\n" \
    "s_waitcnt lgkmcnt(2)\n" \
    "v_smfmac_f32_32x32x32_bf16 %0, v[116:119], v[60:67], v160\n" \
    "ds_read_b128 v[112:115], v161 offset:20480\n" \
    "v_exp_f32 v70, v70\n" \
    "v_exp_f32 v71, v71\n" \
    "s_waitcnt vmcnt(6)\n" \
    "v_mfma_f32_32x32x16_bf16 %2, v[144:147], v[136:139], %2\n" \
    "v_exp_f32 v72, v72\n" \
    "v_exp_f32 v73, v73\n" \
    "v_exp_f32 v74, v74\n" \
    "s_movk_i32 s40, 16384\n" \
    "s_mov_b32 s41, 36864\n" \
    "buffer_load_dwordx4 v[136:139], %10, %11, s40 offen offset:3072 sc1\n" \
    "buffer_load_dwordx4 v[140:143], %10, %12, s41 offen offset:2048 sc1\n" \
    "buffer_load_dwordx4 v[144:147], %10, %12, s41 offen offset:3072 sc1\n" \
    "s_waitcnt vmcnt(7)\n" \
    "v_mfma_f32_32x32x16_bf16 %1, v[152:155], v[148:151], %1\n" \
    "v_exp_f32 v75, v75\n" \
    "s_waitcnt lgkmcnt(1)\n" \
    "v_exp_f32 v76, v76\n" \
    "v_exp_f32 v77, v77\n" \
    "v_smfmac_f32_32x32x32_bf16 %0, v[120:123], v[68:75], v160\n" \
    "ds_read_b64_tr_b16 v[116:117], v162 offset:20672\n" \
    "ds_read_b64_tr_b16 v[118:119], v163 offset:20672\n" \
    "v_exp_f32 v78, v78\n" \
    "v_exp_f32 v79, v79\n" \
    "v_exp_f32 v80, v80\n" \
    "v_exp_f32 v81, v81\n" \
    "s_waitcnt lgkmcnt(2)\n" \
    "v_mfma_f32_32x32x16_bf16 v[60:75], v[112:115], v[108:111], v[92:107]\n" \
    "ds_read_b64_tr_b16 v[120:121], v162 offset:21216\n" \
    "ds_read_b64_tr_b16 v[122:123], v163 offset:21216\n" \
    "v_exp_f32 v82, v82\n" \
    "v_exp_f32 v83, v83\n" \
    "v_exp_f32 v84, v84\n" \
    "v_exp_f32 v85, v85\n" \
    "s_waitcnt lgkmcnt(2)\n" \
    "v_smfmac_f32_32x32x32_bf16 %0, v[116:119], v[76:83], v160\n" \
    "ds_read_b128 v[112:115], v161 offset:21504\n" \
    "v_exp_f32 v86, v86\n" \
    "v_exp_f32 v87, v87\n" \
    "s_waitcnt vmcnt(6)\n" \
    "v_mfma_f32_32x32x16_bf16 %2, v[156:159], v[148:151], %2\n" \
    "v_exp_f32 v88, v88\n" \
    "v_exp_f32 v89, v89\n" \
    "v_exp_f32 v90, v90\n" \
    "s_movk_i32 s40, 20480\n" \
    "s_mov_b32 s41, 40960\n" \
    "buffer_load_dwordx4 v[148:151], %10, %11, s40 offen offset:0 sc1\n" \
    "buffer_load_dwordx4 v[152:155], %10, %12, s41 offen offset:0 sc1\n" \
    "buffer_load_dwordx4 v[156:159], %10, %12, s41 offen offset:1024 sc1\n" \
    "s_waitcnt vmcnt(7)\n" \
    "v_mfma_f32_32x32x16_bf16 %1, v[128:131], v[124:127], %1\n" \
    "v_exp_f32 v91, v91\n" \
    "s_waitcnt lgkmcnt(1)\n" \
    "v_exp_f32 v60, v60\n" \
    "v_exp_f32 v61, v61\n" \
    "v_smfmac_f32_32x32x32_bf16 %0, v[120:123], v[84:91], v160\n" \
    "ds_read_b64_tr_b16 v[116:117], v162 offset:21760\n" \
    "ds_read_b64_tr_b16 v[118:119], v163 offset:21760\n" \
    "v_exp_f32 v62, v62\n" \
    "v_exp_f32 v63, v63\n" \
    "v_exp_f32 v64, v64\n" \
    "v_exp_f32 v65, v65\n" \
    "s_waitcnt lgkmcnt(2)\n" \
    "v_mfma_f32_32x32x16_bf16 v[76:91], v[112:115], v[108:111], v[92:107]\n" \
    "ds_read_b64_tr_b16 v[120:121], v162 offset:22304\n" \
    "ds_read_b64_tr_b16 v[122:123], v163 offset:22304\n" \
    "v_exp_f32 v66, v66\n" \
    "v_exp_f32 v67, v67\n" \
    "v_exp_f32 v68, v68\n" \
    "v_exp_f32 v69, v69\n" \
    "s_waitcnt lgkmcnt(2)\n" \
    "v_smfmac_f32_32x32x32_bf16 %0, v[116:119], v[60:67], v160\n" \
    "ds_read_b128 v[112:115], v161 offset:22528\n" \
    "v_exp_f32 v70, v70\n" \
    "v_exp_f32 v71, v71\n" \
    "v_exp_f32 v72, v72\n" \
    "s_waitcnt vmcnt(6)\n" \
    "v_mfma_f32_32x32x16_bf16 %2, v[132:135], v[124:127], %2\n" \
    "v_exp_f32 v73, v73\n" \
    "v_exp_f32 v74, v74\n" \
    "v_exp_f32 v75, v75\n" \
    "s_movk_i32 s40, 20480\n" \
    "s_mov_b32 s41, 40960\n" \
    "buffer_load_dwordx4 v[124:127], %10, %11, s40 offen offset:1024 sc1\n" \
    "buffer_load_dwordx4 v[128:131], %10, %12, s41 offen offset:2048 sc1\n" \
    "buffer_load_dwordx4 v[132:135], %10, %12, s41 offen offset:3072 sc1\n" \
    "s_waitcnt lgkmcnt(1)\n" \
    "v_exp_f32 v76, v76\n" \
    "v_exp_f32 v77, v77\n" \
    "v_smfmac_f32_32x32x32_bf16 %0, v[120:123], v[68:75], v160\n" \
    "ds_read_b64_tr_b16 v[116:117], v162 offset:22848\n" \
    "ds_read_b64_tr_b16 v[118:119], v163 offset:22848\n" \
    "v_exp_f32 v78, v78\n" \
    "v_exp_f32 v79, v79\n" \
    "v_exp_f32 v80, v80\n" \
    "v_exp_f32 v81, v81\n" \
    "s_waitcnt lgkmcnt(2)\n" \
    "v_mfma_f32_32x32x16_bf16 v[60:75], v[112:115], v[108:111], v[92:107]\n" \
    "ds_read_b64_tr_b16 v[120:121], v162 offset:23392\n" \
    "ds_read_b64_tr_b16 v[122:123], v163 offset:23392\n" \
    "v_exp_f32 v82, v82\n" \
    "v_exp_f32 v83, v83\n" \
    "v_exp_f32 v84, v84\n" \
    "v_exp_f32 v85, v85\n" \
    "s_waitcnt lgkmcnt(2)\n" \
    "v_smfmac_f32_32x32x32_bf16 %0, v[116:119], v[76:83], v160\n" \
    "ds_read_b128 v[112:115], v161 offset:23552\n" \
    "v_exp_f32 v86, v86\n" \
    "v_exp_f32 v87, v87\n" \
    "s_waitcnt vmcnt(7)\n" \
    "v_mfma_f32_32x32x16_bf16 %1, v[140:143], v[136:139], %1\n" \
    "v_exp_f32 v88, v88\n" \
    "v_exp_f32 v89, v89\n" \
    "v_exp_f32 v90, v90\n" \
    "s_waitcnt vmcnt(6)\n" \
    "v_mfma_f32_32x32x16_bf16 %2, v[144:147], v[136:139], %2\n" \
    "v_exp_f32 v91, v91\n" \
    "s_movk_i32 s40, 20480\n" \
    "s_mov_b32 s41, 45056\n" \
    "buffer_load_dwordx4 v[136:139], %10, %11, s40 offen offset:2048 sc1\n" \
    "buffer_load_dwordx4 v[140:143], %10, %12, s41 offen offset:0 sc1\n" \
    "buffer_load_dwordx4 v[144:147], %10, %12, s41 offen offset:1024 sc1\n" \
    "s_setprio 1\n" \
    "s_waitcnt lgkmcnt(1)\n" \
    "v_exp_f32 v60, v60\n" \
    "v_exp_f32 v61, v61\n" \
    "v_smfmac_f32_32x32x32_bf16 %0, v[120:123], v[84:91], v160\n" \
    "ds_read_b64_tr_b16 v[116:117], v162 offset:23936\n" \
    "ds_read_b64_tr_b16 v[118:119], v163 offset:23936\n" \
    "v_exp_f32 v62, v62\n" \
    "v_exp_f32 v63, v63\n" \
    "v_exp_f32 v64, v64\n" \
    "v_exp_f32 v65, v65\n" \
    "s_waitcnt lgkmcnt(2)\n" \
    "v_mfma_f32_32x32x16_bf16 v[76:91], v[112:115], v[108:111], v[92:107]\n" \
    "ds_read_b64_tr_b16 v[120:121], v162 offset:24480\n" \
    "ds_read_b64_tr_b16 v[122:123], v163 offset:24480\n" \
    "v_exp_f32 v66, v66\n" \
    "v_exp_f32 v67, v67\n" \
    "v_exp_f32 v68, v68\n" \
    "v_exp_f32 v69, v69\n" \
    "s_waitcnt lgkmcnt(2)\n" \
    "v_smfmac_f32_32x32x32_bf16 %0, v[116:119], v[60:67], v160\n" \
    "ds_read_b128 v[112:115], v161 offset:24576\n" \
    "v_exp_f32 v70, v70\n" \
    "v_exp_f32 v71, v71\n" \
    "s_waitcnt vmcnt(7)\n" \
    "v_mfma_f32_32x32x16_bf16 %1, v[152:155], v[148:151], %1\n" \
    "v_exp_f32 v72, v72\n" \
    "v_exp_f32 v73, v73\n" \
    "v_exp_f32 v74, v74\n" \
    "s_waitcnt vmcnt(6)\n" \
    "v_mfma_f32_32x32x16_bf16 %2, v[156:159], v[148:151], %2\n" \
    "v_exp_f32 v75, v75\n" \
    "s_movk_i32 s40, 20480\n" \
    "s_mov_b32 s41, 45056\n" \
    "buffer_load_dwordx4 v[148:151], %10, %11, s40 offen offset:3072 sc1\n" \
    "buffer_load_dwordx4 v[152:155], %10, %12, s41 offen offset:2048 sc1\n" \
    "buffer_load_dwordx4 v[156:159], %10, %12, s41 offen offset:3072 sc1\n" \
    "s_waitcnt lgkmcnt(1)\n" \
    "v_exp_f32 v76, v76\n" \
    "v_exp_f32 v77, v77\n" \
    "v_smfmac_f32_32x32x32_bf16 %0, v[120:123], v[68:75], v160\n" \
    "ds_read_b64_tr_b16 v[116:117], v162 offset:25024\n" \
    "ds_read_b64_tr_b16 v[118:119], v163 offset:25024\n" \
    "v_exp_f32 v78, v78\n" \
    "v_exp_f32 v79, v79\n" \
    "v_exp_f32 v80, v80\n" \
    "v_exp_f32 v81, v81\n" \
    "s_waitcnt lgkmcnt(2)\n" \
    "v_mfma_f32_32x32x16_bf16 v[60:75], v[112:115], v[108:111], v[92:107]\n" \
    "ds_read_b64_tr_b16 v[120:121], v162 offset:25568\n" \
    "ds_read_b64_tr_b16 v[122:123], v163 offset:25568\n" \
    "v_exp_f32 v82, v82\n" \
    "v_exp_f32 v83, v83\n" \
    "v_exp_f32 v84, v84\n" \
    "v_exp_f32 v85, v85\n" \
    "s_waitcnt lgkmcnt(2)\n" \
    "v_smfmac_f32_32x32x32_bf16 %0, v[116:119], v[76:83], v160\n" \
    "ds_read_b128 v[112:115], v161 offset:25600\n" \
    "v_exp_f32 v86, v86\n" \
    "v_exp_f32 v87, v87\n" \
    "v_exp_f32 v88, v88\n" \
    "s_waitcnt vmcnt(7)\n" \
    "v_mfma_f32_32x32x16_bf16 %1, v[128:131], v[124:127], %1\n" \
    "v_exp_f32 v89, v89\n" \
    "v_exp_f32 v90, v90\n" \
    "v_exp_f32 v91, v91\n" \
    "s_waitcnt lgkmcnt(1)\n" \
    "v_exp_f32 v60, v60\n" \
    "v_exp_f32 v61, v61\n" \
    "v_smfmac_f32_32x32x32_bf16 %0, v[120:123], v[84:91], v160\n" \
    "ds_read_b64_tr_b16 v[116:117], v162 offset:26112\n" \
    "ds_read_b64_tr_b16 v[118:119], v163 offset:26112\n" \
    "v_exp_f32 v62, v62\n" \
    "v_exp_f32 v63, v63\n" \
    "v_exp_f32 v64, v64\n" \
    "v_exp_f32 v65, v65\n" \
    "s_waitcnt lgkmcnt(2)\n" \
    "v_mfma_f32_32x32x16_bf16 v[76:91], v[112:115], v[108:111], v[92:107]\n" \
    "ds_read_b64_tr_b16 v[120:121], v162 offset:26656\n" \
    "ds_read_b64_tr_b16 v[122:123], v163 offset:26656\n" \
    "v_exp_f32 v66, v66\n" \
    "v_exp_f32 v67, v67\n" \
    "v_exp_f32 v68, v68\n" \
    "v_exp_f32 v69, v69\n" \
    "s_waitcnt lgkmcnt(2)\n" \
    "v_smfmac_f32_32x32x32_bf16 %0, v[116:119], v[60:67], v160\n" \
    "ds_read_b128 v[112:115], v161 offset:26624\n" \
    "v_exp_f32 v70, v70\n" \
    "v_exp_f32 v71, v71\n" \
    "s_waitcnt vmcnt(6)\n" \
    "v_mfma_f32_32x32x16_bf16 %2, v[132:135], v[124:127], %2\n" \
    "v_exp_f32 v72, v72\n" \
    "v_exp_f32 v73, v73\n" \
    "v_exp_f32 v74, v74\n" \
    "s_movk_i32 s40, 24576\n" \
    "s_mov_b32 s41, 49152\n" \
    "buffer_load_dwordx4 v[124:127], %10, %11, s40 offen offset:0 sc1\n" \
    "buffer_load_dwordx4 v[128:131], %10, %12, s41 offen offset:0 sc1\n" \
    "buffer_load_dwordx4 v[132:135], %10, %12, s41 offen offset:1024 sc1\n" \
    "s_waitcnt vmcnt(7)\n" \
    "v_mfma_f32_32x32x16_bf16 %1, v[140:143], v[136:139], %1\n" \
    "v_exp_f32 v75, v75\n" \
    "s_waitcnt lgkmcnt(1)\n" \
    "v_exp_f32 v76, v76\n" \
    "v_exp_f32 v77, v77\n" \
    "v_smfmac_f32_32x32x32_bf16 %0, v[120:123], v[68:75], v160\n" \
    "ds_read_b64_tr_b16 v[116:117], v162 offset:27200\n" \
    "ds_read_b64_tr_b16 v[118:119], v163 offset:27200\n" \
    "v_exp_f32 v78, v78\n" \
    "v_exp_f32 v79, v79\n" \
    "v_exp_f32 v80, v80\n" \
    "v_exp_f32 v81, v81\n" \
    "s_waitcnt lgkmcnt(2)\n" \
    "v_mfma_f32_32x32x16_bf16 v[60:75], v[112:115], v[108:111], v[92:107]\n" \
    "ds_read_b64_tr_b16 v[120:121], v162 offset:27744\n" \
    "ds_read_b64_tr_b16 v[122:123], v163 offset:27744\n" \
    "v_exp_f32 v82, v82\n" \
    "v_exp_f32 v83, v83\n" \
    "v_exp_f32 v84, v84\n" \
    "v_exp_f32 v85, v85\n" \
    "s_waitcnt lgkmcnt(2)\n" \
    "v_smfmac_f32_32x32x32_bf16 %0, v[116:119], v[76:83], v160\n" \
    "ds_read_b128 v[112:115], v161 offset:27648\n" \
    "v_exp_f32 v86, v86\n" \
    "v_exp_f32 v87, v87\n" \
    "s_waitcnt vmcnt(6)\n" \
    "v_mfma_f32_32x32x16_bf16 %2, v[144:147], v[136:139], %2\n" \
    "v_exp_f32 v88, v88\n" \
    "v_exp_f32 v89, v89\n" \
    "v_exp_f32 v90, v90\n" \
    "s_movk_i32 s40, 24576\n" \
    "s_mov_b32 s41, 49152\n" \
    "buffer_load_dwordx4 v[136:139], %10, %11, s40 offen offset:1024 sc1\n" \
    "buffer_load_dwordx4 v[140:143], %10, %12, s41 offen offset:2048 sc1\n" \
    "buffer_load_dwordx4 v[144:147], %10, %12, s41 offen offset:3072 sc1\n" \
    "s_waitcnt vmcnt(7)\n" \
    "v_mfma_f32_32x32x16_bf16 %1, v[152:155], v[148:151], %1\n" \
    "v_exp_f32 v91, v91\n" \
    "s_waitcnt lgkmcnt(1)\n" \
    "v_exp_f32 v60, v60\n" \
    "v_exp_f32 v61, v61\n" \
    "v_smfmac_f32_32x32x32_bf16 %0, v[120:123], v[84:91], v160\n" \
    "ds_read_b64_tr_b16 v[116:117], v162 offset:28288\n" \
    "ds_read_b64_tr_b16 v[118:119], v163 offset:28288\n" \
    "v_exp_f32 v62, v62\n" \
    "v_exp_f32 v63, v63\n" \
    "v_exp_f32 v64, v64\n" \
    "v_exp_f32 v65, v65\n" \
    "s_waitcnt lgkmcnt(2)\n" \
    "v_mfma_f32_32x32x16_bf16 v[76:91], v[112:115], v[108:111], v[92:107]\n" \
    "ds_read_b64_tr_b16 v[120:121], v162 offset:28832\n" \
    "ds_read_b64_tr_b16 v[122:123], v163 offset:28832\n" \
    "v_exp_f32 v66, v66\n" \
    "v_exp_f32 v67, v67\n" \
    "v_exp_f32 v68, v68\n" \
    "v_exp_f32 v69, v69\n" \
    "s_waitcnt lgkmcnt(2)\n" \
    "v_smfmac_f32_32x32x32_bf16 %0, v[116:119], v[60:67], v160\n" \
    "ds_read_b128 v[112:115], v161 offset:28672\n" \
    "v_exp_f32 v70, v70\n" \
    "v_exp_f32 v71, v71\n" \
    "v_exp_f32 v72, v72\n" \
    "s_waitcnt vmcnt(6)\n" \
    "v_mfma_f32_32x32x16_bf16 %2, v[156:159], v[148:151], %2\n" \
    "v_exp_f32 v73, v73\n" \
    "v_exp_f32 v74, v74\n" \
    "v_exp_f32 v75, v75\n" \
    "s_movk_i32 s40, 24576\n" \
    "s_mov_b32 s41, 53248\n" \
    "buffer_load_dwordx4 v[148:151], %10, %11, s40 offen offset:2048 sc1\n" \
    "buffer_load_dwordx4 v[152:155], %10, %12, s41 offen offset:0 sc1\n" \
    "buffer_load_dwordx4 v[156:159], %10, %12, s41 offen offset:1024 sc1\n" \
    "s_waitcnt lgkmcnt(1)\n" \
    "v_exp_f32 v76, v76\n" \
    "v_exp_f32 v77, v77\n" \
    "v_smfmac_f32_32x32x32_bf16 %0, v[120:123], v[68:75], v160\n" \
    "ds_read_b64_tr_b16 v[116:117], v162 offset:29376\n" \
    "ds_read_b64_tr_b16 v[118:119], v163 offset:29376\n" \
    "v_exp_f32 v78, v78\n" \
    "v_exp_f32 v79, v79\n" \
    "v_exp_f32 v80, v80\n" \
    "v_exp_f32 v81, v81\n" \
    "s_waitcnt lgkmcnt(2)\n" \
    "v_mfma_f32_32x32x16_bf16 v[60:75], v[112:115], v[108:111], v[92:107]\n" \
    "ds_read_b64_tr_b16 v[120:121], v162 offset:29920\n" \
    "ds_read_b64_tr_b16 v[122:123], v163 offset:29920\n" \
    "v_exp_f32 v82, v82\n" \
    "v_exp_f32 v83, v83\n" \
    "v_exp_f32 v84, v84\n" \
    "v_exp_f32 v85, v85\n" \
    "s_waitcnt lgkmcnt(2)\n" \
    "v_smfmac_f32_32x32x32_bf16 %0, v[116:119], v[76:83], v160\n" \
    "ds_read_b128 v[112:115], v161 offset:29696\n" \
    "v_exp_f32 v86, v86\n" \
    "v_exp_f32 v87, v87\n" \
    "s_waitcnt vmcnt(7)\n" \
    "v_mfma_f32_32x32x16_bf16 %1, v[128:131], v[124:127], %1\n" \
    "v_exp_f32 v88, v88\n" \
    "v_exp_f32 v89, v89\n" \
    "v_exp_f32 v90, v90\n" \
    "s_waitcnt vmcnt(6)\n" \
    "v_mfma_f32_32x32x16_bf16 %2, v[132:135], v[124:127], %2\n" \
    "v_exp_f32 v91, v91\n" \
    "s_movk_i32 s40, 24576\n" \
    "s_mov_b32 s41, 53248\n" \
    "buffer_load_dwordx4 v[124:127], %10, %11, s40 offen offset:3072 sc1\n" \
    "buffer_load_dwordx4 v[128:131], %10, %12, s41 offen offset:2048 sc1\n" \
    "buffer_load_dwordx4 v[132:135], %10, %12, s41 offen offset:3072 sc1\n" \
    "s_waitcnt lgkmcnt(1)\n" \
    "v_exp_f32 v60, v60\n" \
    "v_exp_f32 v61, v61\n" \
    "v_smfmac_f32_32x32x32_bf16 %0, v[120:123], v[84:91], v160\n" \
    "ds_read_b64_tr_b16 v[116:117], v162 offset:30464\n" \
    "ds_read_b64_tr_b16 v[118:119], v163 offset:30464\n" \
    "v_exp_f32 v62, v62\n" \
    "v_exp_f32 v63, v63\n" \
    "v_exp_f32 v64, v64\n" \
    "v_exp_f32 v65, v65\n" \
    "s_waitcnt lgkmcnt(2)\n" \
    "v_mfma_f32_32x32x16_bf16 v[76:91], v[112:115], v[108:111], v[92:107]\n" \
    "ds_read_b64_tr_b16 v[120:121], v162 offset:31008\n" \
    "ds_read_b64_tr_b16 v[122:123], v163 offset:31008\n" \
    "v_exp_f32 v66, v66\n" \
    "v_exp_f32 v67, v67\n" \
    "v_exp_f32 v68, v68\n" \
    "v_exp_f32 v69, v69\n" \
    "s_waitcnt lgkmcnt(2)\n" \
    "v_smfmac_f32_32x32x32_bf16 %0, v[116:119], v[60:67], v160\n" \
    "ds_read_b128 v[112:115], v161 offset:30720\n" \
    "v_exp_f32 v70, v70\n" \
    "v_exp_f32 v71, v71\n" \
    "s_waitcnt vmcnt(7)\n" \
    "v_mfma_f32_32x32x16_bf16 %1, v[140:143], v[136:139], %1\n" \
    "v_exp_f32 v72, v72\n" \
    "v_exp_f32 v73, v73\n" \
    "v_exp_f32 v74, v74\n" \
    "s_waitcnt vmcnt(6)\n" \
    "v_mfma_f32_32x32x16_bf16 %2, v[144:147], v[136:139], %2\n" \
    "v_exp_f32 v75, v75\n" \
    "s_setprio 0\n" \
    "s_waitcnt lgkmcnt(1)\n" \
    "v_exp_f32 v76, v76\n" \
    "v_exp_f32 v77, v77\n" \
    "v_smfmac_f32_32x32x32_bf16 %0, v[120:123], v[68:75], v160\n" \
    "ds_read_b64_tr_b16 v[116:117], v162 offset:31552\n" \
    "ds_read_b64_tr_b16 v[118:119], v163 offset:31552\n" \
    "v_exp_f32 v78, v78\n" \
    "v_exp_f32 v79, v79\n" \
    "v_exp_f32 v80, v80\n" \
    "v_exp_f32 v81, v81\n" \
    "s_waitcnt lgkmcnt(2)\n" \
    "v_mfma_f32_32x32x16_bf16 v[60:75], v[112:115], v[108:111], v[92:107]\n" \
    "ds_read_b64_tr_b16 v[120:121], v162 offset:32096\n" \
    "ds_read_b64_tr_b16 v[122:123], v163 offset:32096\n" \
    "v_exp_f32 v82, v82\n" \
    "v_exp_f32 v83, v83\n" \
    "v_exp_f32 v84, v84\n" \
    "v_exp_f32 v85, v85\n" \
    "s_waitcnt lgkmcnt(2)\n" \
    "v_smfmac_f32_32x32x32_bf16 %0, v[116:119], v[76:83], v160\n" \
    "ds_read_b128 v[112:115], v161 offset:31744\n" \
    "v_exp_f32 v86, v86\n" \
    "v_exp_f32 v87, v87\n" \
    "v_exp_f32 v88, v88\n" \
    "s_waitcnt vmcnt(4)\n" \
    "v_mfma_f32_32x32x16_bf16 %1, v[152:155], v[148:151], %1\n" \
    "v_exp_f32 v89, v89\n" \
    "v_exp_f32 v90, v90\n" \
    "v_exp_f32 v91, v91\n" \
    "s_waitcnt lgkmcnt(1)\n" \
    "v_exp_f32 v60, v60\n" \
    "v_exp_f32 v61, v61\n" \
    "v_smfmac_f32_32x32x32_bf16 %0, v[120:123], v[84:91], v160\n" \
    "ds_read_b64_tr_b16 v[116:117], v162 offset:32640\n" \
    "ds_read_b64_tr_b16 v[118:119], v163 offset:32640\n" \
    "v_exp_f32 v62, v62\n" \
    "v_exp_f32 v63, v63\n" \
    "v_exp_f32 v64, v64\n" \
    "v_exp_f32 v65, v65\n" \
    "s_waitcnt lgkmcnt(2)\n" \
    "v_mfma_f32_32x32x16_bf16 v[76:91], v[112:115], v[108:111], v[92:107]\n" \
    "ds_read_b64_tr_b16 v[120:121], v162 offset:33184\n" \
    "ds_read_b64_tr_b16 v[122:123], v163 offset:33184\n" \
    "v_exp_f32 v66, v66\n" \
    "v_exp_f32 v67, v67\n" \
    "v_exp_f32 v68, v68\n" \
    "v_exp_f32 v69, v69\n" \
    "s_waitcnt lgkmcnt(2)\n" \
    "v_smfmac_f32_32x32x32_bf16 %0, v[116:119], v[60:67], v160\n" \
    "ds_read_b128 v[112:115], v161 offset:32768\n" \
    "v_exp_f32 v70, v70\n" \
    "v_exp_f32 v71, v71\n" \
    "s_waitcnt vmcnt(3)\n" \
    "v_mfma_f32_32x32x16_bf16 %2, v[156:159], v[148:151], %2\n" \
    "v_exp_f32 v72, v72\n" \
    "v_exp_f32 v73, v73\n" \
    "v_exp_f32 v74, v74\n" \
    "s_waitcnt vmcnt(1)\n" \
    "v_mfma_f32_32x32x16_bf16 %1, v[128:131], v[124:127], %1\n" \
    "v_exp_f32 v75, v75\n" \
    "s_waitcnt lgkmcnt(1)\n" \
    "v_exp_f32 v76, v76\n" \
    "v_exp_f32 v77, v77\n" \
    "v_smfmac_f32_32x32x32_bf16 %0, v[120:123], v[68:75], v160\n" \
    "ds_read_b64_tr_b16 v[116:117], v162 offset:33728\n" \
    "ds_read_b64_tr_b16 v[118:119], v163 offset:33728\n" \
    "v_exp_f32 v78, v78\n" \
    "v_exp_f32 v79, v79\n" \
    "v_exp_f32 v80, v80\n" \
    "v_exp_f32 v81, v81\n" \
    "s_waitcnt lgkmcnt(2)\n" \
    "v_mfma_f32_32x32x16_bf16 v[60:75], v[112:115], v[108:111], v[92:107]\n" \
    "ds_read_b64_tr_b16 v[120:121], v162 offset:34272\n" \
    "ds_read_b64_tr_b16 v[122:123], v163 offset:34272\n" \
    "v_exp_f32 v82, v82\n" \
    "v_exp_f32 v83, v83\n" \
    "v_exp_f32 v84, v84\n" \
    "v_exp_f32 v85, v85\n" \
    "s_waitcnt lgkmcnt(2)\n" \
    "v_smfmac_f32_32x32x32_bf16 %0, v[116:119], v[76:83], v160\n" \
    "ds_read_b128 v[112:115], v161 offset:33792\n" \
    "v_exp_f32 v86, v86\n" \
    "v_exp_f32 v87, v87\n" \
    "v_exp_f32 v88, v88\n" \
    "s_waitcnt vmcnt(0)\n" \
    "v_mfma_f32_32x32x16_bf16 %2, v[132:135], v[124:127], %2\n" \
    "v_exp_f32 v89, v89\n" \
    "v_exp_f32 v90, v90\n" \
    "v_exp_f32 v91, v91\n" \
    "s_waitcnt lgkmcnt(0)\n" \
    "s_setprio 0\n" \
    "v_smfmac_f32_32x32x32_bf16 %0, v[120:123], v[84:91], v160\n" \
    "s_nop 7\n" \
    "s_nop 3\n" \
    "ATT_END_%=:\n" \
    ""
#define ATT_CLOBBER "v59", "v60", "v61", "v62", "v63", "v64", "v65", "v66", "v67", "v68", "v69", "v70", "v71", "v72", "v73", "v74", "v75", "v76", "v77", "v78", "v79", "v80", "v81", "v82", "v83", "v84", "v85", "v86", "v87", "v88", "v89", "v90", "v91", "v92", "v93", "v94", "v95", "v96", "v97", "v98", "v99", "v100", "v101", "v102", "v103", "v104", "v105", "v106", "v107", "v108", "v109", "v110", "v111", "v112", "v113", "v114", "v115", "v116", "v117", "v118", "v119", "v120", "v121", "v122", "v123", "v124", "v125", "v126", "v127", "v128", "v129", "v130", "v131", "v132", "v133", "v134", "v135", "v136", "v137", "v138", "v139", "v140", "v141", "v142", "v143", "v144", "v145", "v146", "v147", "v148", "v149", "v150", "v151", "v152", "v153", "v154", "v155", "v156", "v157", "v158", "v159", "v160", "v161", "v162", "v163"
__device__ __forceinline__ void attention(ldsp lds, char* ws, const __amdgpu_buffer_rsrc_t wsr, int w, int lane, int head, int qt, int rt, int ct, f32x16 (&oa)[2]) {
    const int G = lane >> 4, q = (lane & 15) >> 2, p = lane & 3;
    ldsp kp = lds + L_K + lane * 16;
    ldsp v0 = lds + L_V + ((G & 1) ? 512 + 8 * p : (8 * (G >> 1) + q) * 32 + 8 * p);
    ldsp v1 = lds + L_V + ((G & 1) ? 512 + 8 * p : (8 * (G >> 1) + 4 + q) * 32 + 8 * p);
    f32x16 o = {};
    const __amdgpu_buffer_rsrc_t hr = __builtin_amdgcn_make_buffer_rsrc(ws + WS_H + (unsigned)((2 * rt + (w & 1)) * 112 + 28 * (w >> 1)) * 1024, 0, 28 * 1024, 0x00020000);
    const __amdgpu_buffer_rsrc_t wr = __builtin_amdgcn_make_buffer_rsrc(ws + WS_WT + (unsigned)(ct * 144 + 28 * (w >> 1)) * 2048, 0, 28 * 2048, 0x00020000);
    int unsafe;
    asm volatile(ATT_ASM
                 : "+v"(o), "+v"(oa[0]), "+v"(oa[1]), "=&s"(unsafe)
                 : "v"((unsigned)(uintptr_t)lds + L_Q + w * 1024 + lane * 16), "v"((unsigned)(uintptr_t)lds + L_NB + w * 256 + lane * 4), "v"((unsigned)(uintptr_t)kp),
                   "v"((unsigned)(uintptr_t)v0), "v"((unsigned)(uintptr_t)v1), "v"((unsigned)(uintptr_t)lds + L_MISC + 28), "v"(lane * 16), "s"(hr), "s"(wr)
                 : "memory", "scc", "vcc", "s40", "s41", ATT_CLOBBER);
    if (unsafe) {
        const bf16x8 qf = ld_b128(lds + L_Q + w * 1024 + lane * 16);
        __builtin_amdgcn_s_setprio(0);
        WAITV(0);
        __hip_atomic_fetch_add((LAS unsigned*)(lds + L_MISC + 32), 1u, __ATOMIC_RELAXED, __HIP_MEMORY_SCOPE_WORKGROUP);
        lds_wait(lds, 8, 4);
        const int idx = (int)0xDDDDDDDDu;
        float mx = -INFINITY;
#pragma unroll 1
        for (int it = 0; it < 64; ++it) {
            f32x16 s = MFMA32(ld_b128(kp), qf, f32x16{});
            const bf16x8 va = ld_tr2(v0, v1), vb = ld_tr2(v0 + VT, v1 + VT);
            float bm = s[0];
#pragma unroll
            for (int r = 1; r < 16; ++r) bm = fmaxf(bm, s[r]);
            const float mn = fmaxf(mx, max_x32(bm));
            const float al = __builtin_amdgcn_exp2f(mx - mn);
            mx = mn;
#pragma unroll
            for (int r = 0; r < 16; ++r) { o[r] *= al; s[r] = __builtin_amdgcn_exp2f(s[r] - mn); }
            o = SMF(va, lo8(s), o, idx); o = SMF(vb, hi8(s), o, idx);
            kp += 1024; v0 += 2 * VT; v1 += 2 * VT;
            if (it == 31) __hip_atomic_fetch_add((LAS unsigned*)(lds + L_MISC + 36), 1u, __ATOMIC_RELAXED, __HIP_MEMORY_SCOPE_WORKGROUP);
        }
        lds_wait(lds, 6, 1);
#pragma unroll 1
        for (int ks = 0; ks < 28; ++ks) {
            const bf16x8 hf = __builtin_bit_cast(bf16x8, __builtin_amdgcn_raw_buffer_load_b128(hr, lane * 16, ks * 1024, 16));
            oa[0] = MFMA32(__builtin_bit_cast(bf16x8, __builtin_amdgcn_raw_buffer_load_b128(wr, lane * 16, ks * 2048, 16)), hf, oa[0]);
            oa[1] = MFMA32(__builtin_bit_cast(bf16x8, __builtin_amdgcn_raw_buffer_load_b128(wr, lane * 16 + 1024, ks * 2048, 16)), hf, oa[1]);
        }
    }
    const float inv = __builtin_amdgcn_rcpf(sum_x32(o[8]));
    u32x4 d = {pk2(o[0] * inv, o[1] * inv), pk2(o[2] * inv, o[3] * inv), pk2(o[4] * inv, o[5] * inv), pk2(o[6] * inv, o[7] * inv)};
#pragma unroll
    for (int i = 0; i < 4; ++i) d[i] = (d[i] == POISON) ? 0x7FC07FC0u : d[i];
    __builtin_amdgcn_raw_buffer_store_b128(d, wsr, WS_O + (unsigned)((8 * qt + w) * 32 + head) * 1024 + lane * 16, 0, 16);
    lds_count(lds, 2, lane);
}

struct G2Ctx { ldsp wr, rd0, rd1; };
__device__ __forceinline__ void g2_wfrag(const G2Ctx& c, int half, const u32x4& r0, const u32x4& r1, bf16x8& w0, bf16x8& w1) {
    *(LAS u32x4*)(c.wr + half * 2048) = r0; *(LAS u32x4*)(c.wr + half * 2048 + 1024) = r1;
    w0 = ld_tr2(c.rd0 + half * 2048, c.rd0 + half * 2048 + 512); w1 = ld_tr2(c.rd1 + half * 2048, c.rd1 + half * 2048 + 512);
}
__global__ __launch_bounds__(768, 3) void fused_block_kernel(const __hip_bfloat16* __restrict__ x, const __hip_bfloat16* __restrict__ wqkv, const __hip_bfloat16* __restrict__ wo,
                                                             const __hip_bfloat16* __restrict__ w1, const __hip_bfloat16* __restrict__ b1, const __hip_bfloat16* __restrict__ w2,
                                                             const __hip_bfloat16* __restrict__ b2, __hip_bfloat16* __restrict__ out, char* __restrict__ ws) {
    extern __shared__ __attribute__((aligned(16))) char smem[];
    ldsp lds = (ldsp)smem;
    int lane = threadIdx.x & 63;
    const int w = __builtin_amdgcn_readfirstlane(threadIdx.x >> 6);
    const int bg = blockIdx.x & 7, br = blockIdx.x >> 3;
    if (w == 9) {
        u64 pc0; asm volatile("s_getpc_b64 %0" : "=s"(pc0));
        const unsigned co = (br % 6u) * 8192u + lane * 128u; unsigned cw;
        asm volatile("global_load_dword %0, %1, %2\n\ts_waitcnt vmcnt(0)" : "=&v"(cw) : "v"(co < CODE_BYTES ? co : 0u), "s"(pc0 & ~(u64)0xFF) : "memory");
    }
    const int mt = (bg & 1) * 8 + (br & 7), nt = (bg >> 1) * 4 + (br >> 3);
    const int head = bg * 4 + (br >> 3), qt = br & 7;
    const int rt = bg * 4 + (br >> 3), ct = br & 7;
    const __amdgpu_buffer_rsrc_t wsr = __builtin_amdgcn_make_buffer_rsrc(ws, 0, WS_BYTES, 0x00020000);
    if (w < 8) {
        if (w < 4) g1_compute<0>(lds, ws, (const char*)x, w, mt, nt); else g1_compute<1>(lds, ws, (const char*)x, w - 4, mt, nt);

        f32x16 oa[2] = {};
        attention(lds, ws, wsr, w, lane_id(), head, qt, rt, ct, oa);
        lane = lane_id();
        const int kq = w >> 1, tb = w & 1;
        const int lrow = 8 * w + (lane >> 3), c8 = lane & 7, hb = w >> 2;
        lds_wait<8>(lds, 1, 4);
        bf16x8 wq[8][2];
#pragma unroll
        for (int d = 0; d < 8; ++d) { wq[d][0] = ld_b128(lds + wo_stage(kq) + d * 2048 + lane * 16); wq[d][1] = ld_b128(lds + wo_stage(kq) + d * 2048 + 1024 + lane * 16);
            asm volatile("" : "+v"(wq[d][0]), "+v"(wq[d][1])); }
        lds_wait<2>(lds, 2, 8);
        u32x4 of[8];
#pragma unroll
        for (int d = 0; d < 8; ++d) of[d] = __builtin_amdgcn_raw_buffer_load_b128(wsr, WS_O + (unsigned)((2 * rt + tb) * 32 + 8 * kq) * 1024 + lane * 16, d * 1024, 16);
        __builtin_amdgcn_sched_barrier(0);
        const u32x4 xres = *(const GAS u32x4*)((const char*)x + ((64 * rt + lrow) * DM + 64 * ct + c8 * 8) * 2), bres = *(const GAS u32x4*)((const char*)b2 + (64 * ct + c8 * 8) * 2);
        __builtin_amdgcn_sched_barrier(0);
#pragma unroll 1
        for (unsigned spins = 0; spins < SPIN_CAP; ++spins) {
            bool ok = true;
#pragma unroll
            for (int d = 0; d < 8; ++d) ok &= umax4(of[d]) != POISON;
            if (__all(ok)) break;
            __builtin_amdgcn_s_sleep(1);
#pragma unroll
            for (int d = 0; d < 8; ++d) of[d] = __builtin_amdgcn_raw_buffer_load_b128(wsr, WS_O + (unsigned)((2 * rt + tb) * 32 + 8 * kq) * 1024 + lane * 16, d * 1024, 16);
        }
#pragma unroll
        for (int d = 0; d < 8; ++d) { oa[0] = MFMA32(wq[d][0], __builtin_bit_cast(bf16x8, of[d]), oa[0]); oa[1] = MFMA32(wq[d][1], __builtin_bit_cast(bf16x8, of[d]), oa[1]); }
#pragma unroll
        for (int a = 0; a < 2; ++a)
#pragma unroll
            for (int r4 = 0; r4 < 4; ++r4) {
                *(LAS f32x4*)(lds + red_slice(w) + (lane & 31) * RED_PITCH + (32 * a + 8 * r4 + 4 * (lane >> 5)) * 4) =
                    f32x4{oa[a][4 * r4], oa[a][4 * r4 + 1], oa[a][4 * r4 + 2], oa[a][4 * r4 + 3]};
            }
        BAR();
        f32x4 s0 = {bf_lo(xres[0]) + bf_lo(bres[0]), bf_hi(xres[0]) + bf_hi(bres[0]), bf_lo(xres[1]) + bf_lo(bres[1]), bf_hi(xres[1]) + bf_hi(bres[1])};
        f32x4 s1 = {bf_lo(xres[2]) + bf_lo(bres[2]), bf_hi(xres[2]) + bf_hi(bres[2]), bf_lo(xres[3]) + bf_lo(bres[3]), bf_hi(xres[3]) + bf_hi(bres[3])};
#pragma unroll
        for (int k = 0; k < 4; ++k) { ldsp rp = lds + (hb ? red_slice(2 * k + 1) : red_slice(2 * k)) + (lrow & 31) * RED_PITCH + c8 * 32; s0 += *(const LAS f32x4*)rp; s1 += *(const LAS f32x4*)(rp + 16); }
        *(GAS u32x4*)((char*)out + ((64 * rt + lrow) * DM + 64 * ct + c8 * 8) * 2) = u32x4{pk2(s0[0], s0[1]), pk2(s0[2], s0[3]), pk2(s1[0], s1[1]), pk2(s1[2], s1[3])};
    } else {
        const int j = w - 8;
        const int rot = (mt + nt) & 7;
        const int ntk = (512 + 16 * head) / 96, ntv = (1024 + 16 * head) / 96, ntq = (16 * head) / 96;
        if (j == 0 || j == 3) {
            const int r0 = 64 * (j & 1) + (lane >> 3);
            const unsigned o0 = r0 * 1024 + (((lane & 7) ^ ((r0 >> 1) & 7)) << 4), o1 = (r0 + 8) * 1024 + (((lane & 7) ^ (((r0 + 8) >> 1) & 7)) << 4);
            const char* src = (const char*)x + (size_t)mt * 128 * 1024; ldsp dst = lds + (j & 1) * 8192;
            g1_load_x<8>([&](int s, int slot) { const char* sp = src + (((s < 8 ? s : s - 13) + rot) & 7) * 128;
#pragma unroll
                for (int i = 0; i < 4; ++i) { glds16(sp + i * 16384 + o0, dst + slot + i * 2048); glds16(sp + i * 16384 + o1, dst + slot + i * 2048 + 1024); } });
        } else if (j == 2) {
            unsigned oq[3], of[7];
#pragma unroll
            for (int i = 0; i < 3; ++i) { const int e = 64 * i + lane, k = e / 12, pos = e - 12 * k; oq[i] = k * 3072 + ((pos ^ (((k >> 3) & 1) << 1)) << 4); }
#pragma unroll
            for (int i = 0; i < 7; ++i) { const int e = 64 * i + lane, rho = e / 14, pos = e - 14 * rho; of[i] = ((rho & ~12) | ((rho & 4) << 1) | ((rho & 8) >> 1)) * 3584 + (pos << 4); }
            const char* sq = (const char*)wqkv + nt * 192; const char* sf = (const char*)w1 + nt * 224; ldsp dst = lds + OFF_B;
            g1_load_w([&](int s, int slot) {
                if (s < 8) { const char* sp = sq + ((s + rot) & 7) * (64 * 3072);
#pragma unroll
                    for (int i = 0; i < 12; ++i) glds16(sp + (i / 3) * (16 * 3072) + oq[i % 3], dst + slot + i * 1024);
                } else { const char* sp = sf + ((s - 5 + rot) & 7) * (64 * 3584);
#pragma unroll
                    for (int i = 0; i < 14; ++i) glds16(sp + (i / 7) * (32 * 3584) + of[i % 7], dst + slot + i * 1024);
                } });
        } else {
            const u64 tg = call_tag(ws);
            const unsigned bw = *(const GAS unsigned*)((const char*)b1 + 224 * nt + (lane < 56 ? lane : 0) * 4);
            BAR(); BAR();
            BAR();
            const GAS u64* pr = (const GAS u64*)(ws + WS_RS) + 128 * mt + lane;
            u64 g0 = __hip_atomic_load(pr, RLX_AGENT), g1 = __hip_atomic_load(pr + 64, RLX_AGENT);
            BAR(); BAR(); BAR(); BAR(); BAR();
#pragma unroll 1
            for (unsigned spins = 0; spins < SPIN_CAP; ++spins) {
                if (__all((unsigned)(g0 >> 32) == (unsigned)tg && (unsigned)(g1 >> 32) == (unsigned)tg)) break;
                __builtin_amdgcn_s_sleep(1);
                g0 = __hip_atomic_load(pr, RLX_AGENT); g1 = __hip_atomic_load(pr + 64, RLX_AGENT);
            }
            if (lane < 56) *(LAS unsigned*)(lds + L_BIAS + lane * 4) = bw;
            if (lane == 0) { *(LAS u64*)(lds + L_MISC) = tg; *(LAS u32x4*)(lds + L_MISC + 16) = u32x4{0u, 0u, 0u, 0u}; *(LAS u32x4*)(lds + L_MISC + 32) = u32x4{0u, 0u, 0u, 0u}; *(LAS u32x4*)(lds + L_MISC + 48) = u32x4{0u, 0u, 0u, 0u}; }
            *(LAS unsigned*)(lds + L_ROWST + lane * 4) = (unsigned)g0; *(LAS unsigned*)(lds + L_ROWST + 256 + lane * 4) = (unsigned)g1;
            BAR();
            BAR();
            BAR(); BAR(); BAR();
            BAR();
            if (lane == 0) __hip_atomic_store((GAS u64*)(ws + WS_F1 + (mt * 16 + nt) * 128), tg, RLX_AGENT);
            BAR();
            BAR();
        }
        const u64 tag = *(const volatile LAS u64*)(lds + L_MISC);
        {
            __builtin_amdgcn_s_setprio(3);
            lane = lane_id();
            const int l16 = lane & 15;
            wait_flags(ws, WS_F1 + ((lane < 32) ? l16 * 16 + ((lane & 16) ? ntv : ntk) : (2 * qt + (lane & 1)) * 16 + ntq) * 128, tag);
            const u32x4 tq0 = __builtin_amdgcn_raw_buffer_load_b128(wsr, WS_Q + head * 65536 + qt * 8192 + j * 2048 + lane * 16, 0, 16);
            const u32x4 tq1 = __builtin_amdgcn_raw_buffer_load_b128(wsr, WS_Q + head * 65536 + qt * 8192 + j * 2048 + lane * 16, 1024, 16);
            unsigned kb = __builtin_amdgcn_raw_buffer_load_b32(wsr, WS_KMAX + (head * 64 + lane) * 4, 0, 16);
            u32x4 tk4[16], tv4[16];
            auto kvload = [&](int i) {
                tk4[i] = __builtin_amdgcn_raw_buffer_load_b128(wsr, WS_K + head * 65536 + j * 4096 + lane * 16, (i >> 2) * 16384 + (i & 3) * 1024, 16);
                tv4[i] = __builtin_amdgcn_raw_buffer_load_b128(wsr, WS_V + head * 65536 + j * 4096 + lane * 16, (i >> 2) * 16384 + (i & 3) * 1024, 16); };
#pragma unroll
            for (int i = 0; i < 8; ++i) kvload(i);
            __builtin_amdgcn_sched_barrier(0);
            lds_wait(lds, 0, 512);
            *(LAS u32x4*)(lds + L_V + (32 * j + (lane >> 1)) * VT + 512 + (lane & 1) * 16) = u32x4{(lane & 1) ? 0u : 0x3f80u, 0u, 0u, 0u};
            *(LAS u32x4*)(lds + L_Q + j * 2048 + lane * 16) = tq0; *(LAS u32x4*)(lds + L_Q + j * 2048 + 1024 + lane * 16) = tq1;
            {
                kb = umax_32lanes(kb);
                const auto r = __builtin_amdgcn_permlane32_swap(kb, kb, false, false);
                const float km = __uint_as_float(r[0] > r[1] ? r[0] : r[1]) * 1.004f;
                const float n0 = sum_x32(dot2(tq0[3], tq0[3], dot2(tq0[2], tq0[2], dot2(tq0[1], tq0[1], dot2(tq0[0], tq0[0], 0.f)))));
                const float n1 = sum_x32(dot2(tq1[3], tq1[3], dot2(tq1[2], tq1[2], dot2(tq1[1], tq1[1], dot2(tq1[0], tq1[0], 0.f)))));
                *(LAS float*)(lds + L_NB + j * 512 + lane * 4) = -__builtin_amdgcn_sqrtf(n0 * km); *(LAS float*)(lds + L_NB + j * 512 + 256 + lane * 4) = -__builtin_amdgcn_sqrtf(n1 * km);
            }
            ldsp kd = lds + L_K + j * 4096 + lane * 16, vd = lds + L_V + (8 * j + ((lane >> 4) & 1)) * VT + l16 * 32 + (lane >> 5) * 16;
#pragma unroll
            for (int i = 0; i < 16; ++i) {
                *(LAS u32x4*)(kd + (i >> 2) * 16384 + (i & 3) * 1024) = tk4[i]; *(LAS u32x4*)(vd + ((i >> 2) * 32 + (i & 3) * 2) * VT) = tv4[i];
                if (i == 3) { asm volatile("s_waitcnt lgkmcnt(0)" ::: "memory"); lds_count(lds, 7, lane);
#pragma unroll
                    for (int k = 8; k < 16; ++k) kvload(k);
                    __builtin_amdgcn_sched_barrier(0);
                }
                if (i == 7) { asm volatile("s_waitcnt lgkmcnt(0)" ::: "memory"); lds_count(lds, 3, lane); }
            }
            asm volatile("s_waitcnt lgkmcnt(0)" ::: "memory");
            lds_count(lds, 8, lane);
        }
        __builtin_amdgcn_s_setprio(3);
        lane = lane_id();
        if (j == 1) {
            u32x4 wt[5][2];
            const int wrow = lane >> 3, wch = (lane & 7) ^ (((wrow >> 1) & 1) << 2);
#pragma unroll
            for (int i = 0; i < 5; ++i) {
                const int ks = (i < 4 || br < 16) ? br + 32 * i : br;
                const char* src = (ks < 112) ? (const char*)w2 + (size_t)(16 * ks + wrow) * 1024 : (const char*)wo + (size_t)(16 * (ks - 112) + ((wrow & 3) | ((wrow & 4) << 1))) * 1024;
                wt[i][0] = *(const GAS u32x4*)(src + bg * 128 + wch * 16); wt[i][1] = *(const GAS u32x4*)(src + bg * 128 + wch * 16 + ((ks < 112) ? 8192 : 4096));
            }
            G2Ctx gc;
            { const int G = lane >> 4, tq = (lane & 15) >> 2, sw = ((tq >> 1) & 1) * 64; ldsp rd = lds + L_WT + (8 * (G >> 1) + tq) * 128 + 32 * (G & 1) + 8 * (lane & 3);
              gc.wr = lds + L_WT + lane * 16; gc.rd0 = rd + sw; gc.rd1 = rd + (sw ^ 64); }
            auto retile = [&](int i) { bf16x8 f0, f1; g2_wfrag(gc, 0, wt[i][0], wt[i][1], f0, f1);
                const unsigned to = WS_WT + (unsigned)(bg * 144 + ((i < 4 || br < 16) ? br + 32 * i : br)) * 2048 + lane * 16;
                __builtin_amdgcn_raw_buffer_store_b128(__builtin_bit_cast(u32x4, f0), wsr, to, 0, 16); __builtin_amdgcn_raw_buffer_store_b128(__builtin_bit_cast(u32x4, f1), wsr, to + 1024, 0, 16); };
            auto poison = [&](int i) { __builtin_amdgcn_raw_buffer_store_b128(u32x4{POISON, POISON, POISON, POISON}, wsr, WS_O + (unsigned)((8 * qt + i) * 32 + head) * 1024 + lane * 16, 0, 16); };
#pragma unroll
            for (int k = 0; k < 8; ++k) poison(k);
#pragma unroll
            for (int k = 0; k < 5; ++k) retile(k);
            lds_wait<2>(lds, 4, 512); WAITV(0);
            if (lane == 0) __hip_atomic_store((GAS u64*)(ws + WS_F2 + (mt * 16 + nt) * 128), tag, RLX_AGENT); }
        if (j == 0) {
            const int i = lane & 31;
            wait_flags(ws, WS_F2 + ((lane < 32) ? ((ct & 1) * 8 + (i & 7)) * 16 + (ct >> 1) * 4 + (i >> 3) : (lane < 48) ? (rt >> 1) * 16 + (lane & 15) : mt * 16 + nt) * 128, tag);
            lds_count(lds, 6, lane);
        }
        {
            const int hp = 8 * j + (lane & 7), pg = hp >> 2, pr = (hp & 3) * 8 + (rt >> 2);
            wait_flags(ws, WS_F2 + (((pg & 1) * 8 + (pr & 7)) * 16 + (pg >> 1) * 4 + (pr >> 3)) * 128, tag);
        }
        if (blockIdx.x == 0 && j == 3) {
#pragma unroll 1
            for (int i = 0; i < 4; ++i) wait_flags(ws, WS_F1 + (i * 64 + lane) * 128, tag);
            if (lane == 0) __hip_atomic_store((GAS u64*)(ws + WS_EPOCH), tag, RLX_AGENT);
        }
        lds_wait<16>(lds, 6, 1);
        {
            u32x4 t[16];
#pragma unroll
            for (int i = 0; i < 16; ++i) t[i] = __builtin_amdgcn_raw_buffer_load_b128(wsr, WS_WT + (unsigned)(ct * 144 + 112 + 8 * j) * 2048 + lane * 16, i * 1024, 16);
            lds_wait<16>(lds, 5, 512);
#pragma unroll
            for (int i = 0; i < 16; ++i) *(LAS u32x4*)(lds + wo_stage(j) + i * 1024 + lane * 16) = t[i];
            asm volatile("s_waitcnt lgkmcnt(0)" ::: "memory");
            lds_count(lds, 1, lane);
        }
        BAR();
    }
}

extern "C" void kernel_launch(void* const* d_in, const int* in_sizes, int n_in, void* d_out, int out_size, void* d_ws, size_t ws_size, hipStream_t stream) {
    (void)in_sizes; (void)n_in; (void)out_size; (void)ws_size;
    (void)hipFuncSetAttribute((const void*)fused_block_kernel, hipFuncAttributeMaxDynamicSharedMemorySize, LDS_BYTES);
    fused_block_kernel<<<256, 768, LDS_BYTES, stream>>>((const __hip_bfloat16*)d_in[0], (const __hip_bfloat16*)d_in[1], (const __hip_bfloat16*)d_in[2],
                                                       (const __hip_bfloat16*)d_in[4], (const __hip_bfloat16*)d_in[5], (const __hip_bfloat16*)d_in[6],
                                                       (const __hip_bfloat16*)d_in[7], (__hip_bfloat16*)d_out, (char*)d_ws);
}
```
